```python
import torch

def _softmax(x, axis=-1):
    return torch.softmax(x, dim=axis)
def _gelu(x):
    return torch.nn.functional.gelu(x, approximate="tanh")
def _silu(x):
    return torch.nn.functional.silu(x)
def _sigmoid(x):
    return torch.sigmoid(x)
def _layernorm(x, axis=-1, eps=1e-5):
    m = x.mean(dim=axis, keepdim=True)
    v = x.var(dim=axis, keepdim=True, unbiased=False)
    return (x - m) / torch.sqrt(v + eps)
def _scatter_add(tgt, idx, src, axis=0):
    return tgt.clone().index_add_(axis, idx, src)
def _segment_sum(src, seg, n):
    out = torch.zeros((n,) + src.shape[1:], dtype=src.dtype)
    return out.index_add_(0, seg, src)
def _pad(x, pw):
    flat = tuple(v for pair in reversed(pw) for v in pair)
    return torch.nn.functional.pad(x, flat)

kernel_name = 'standalone__gqa_softmax__prompt'

def setup_inputs():

    _w = (lambda a: (lambda t: t.to(torch.bfloat16) if t.is_floating_point() else t)(torch.as_tensor(a)))
    B, Hq, Hkv, Sq, Sk, D = 1, 32, 32, 2048, 2048, 128
    return {
        "q": _w((lambda *s: torch.randn(*s, dtype=torch.bfloat16) * 0.8)(B, Hq, Sq, D)),
        "k": _w((lambda *s: torch.randn(*s, dtype=torch.bfloat16) * 0.8)(B, Hkv, Sk, D)),
        "v": _w((lambda *s: torch.randn(*s, dtype=torch.bfloat16) * 0.05)(B, Hkv, Sk, D)),
    }

def reference(q, k, v):
    B, Hq, Sq, D = q.shape
    Hkv = k.shape[1]
    rep = Hq // Hkv
    k = (lambda x, n, axis: torch.repeat_interleave(x, n, dim=axis))(k, rep, axis=1)
    v = (lambda x, n, axis: torch.repeat_interleave(x, n, dim=axis))(v, rep, axis=1)
    scale = D ** -0.5
    scores = torch.einsum("bhqd,bhkd->bhqk", q, k) * scale
    p = _softmax(scores, axis=-1)
    return torch.einsum("bhqk,bhkd->bhqd", p, v)
```

```cpp
#include <hip/hip_runtime.h>
#include <hip/hip_bf16.h>
#include <cstdio>

using bf16 = __hip_bfloat16;
constexpr int   D = 128, NW = 8, QBLK = 32, KVBLK = 64;
constexpr float SCALE = 0.088388347648318440f;
#define ATTN_THR 8.f
constexpr float THR = ATTN_THR;
#define ATTN_LAYOUT 0
constexpr int LAYOUT = ATTN_LAYOUT;
#define ATTN_SDEPTH 2
constexpr int SDEPTH = ATTN_SDEPTH;
static_assert(SDEPTH == 1 || SDEPTH == 2, "SDEPTH is 1 or 2");
#define ATTN_B 1
#define ATTN_HQ 32
#define ATTN_HKV 32
#define ATTN_S 2048
#define ATTN_SKV ATTN_S
constexpr int B = ATTN_B, HQ = ATTN_HQ, HKV = ATTN_HKV, S = ATTN_S, SKV = ATTN_SKV;
constexpr int LDQ = LAYOUT ? HQ * D : D, LDK = LAYOUT ? HKV * D : D, LDO = LDQ;
static_assert(S % (QBLK * NW) == 0 && SKV % (2 * KVBLK) == 0 && HQ % HKV == 0, "S % 256 == 0, SKV % 128 == 0 (even tile count), HQ % HKV == 0");
constexpr size_t SHM_V = KVBLK * D * 2, SHM_K = KVBLK * D * 2, SHM_ATTN = 2 * SHM_V + 2 * SHM_K + NW * 64 * 4;

using bf16x8 = __attribute__((ext_vector_type(8))) short;
using s16x4  = __attribute__((ext_vector_type(4))) short;
using f32x16 = __attribute__((ext_vector_type(16))) float;
using f32x8  = __attribute__((ext_vector_type(8))) float;
using u32x4  = __attribute__((ext_vector_type(4))) unsigned;
#define KSWZ(row, colB) ((row) * 256 + ((colB) ^ (((row) & 7) << 4)))
#define SBAR() __builtin_amdgcn_sched_barrier(0)
__device__ __forceinline__ int crow(int r, int hi) { return (r & 3) + 8 * (r >> 2) + 4 * hi; }
__device__ __forceinline__ unsigned cvtpk(float lo, float hi) {
  unsigned r; asm volatile("v_cvt_pk_bf16_f32 %0, %1, %2" : "=v"(r) : "v"(lo), "v"(hi)); return r;
}
template <typename TIn> struct Stage;
template <> struct Stage<bf16>  { using T = bf16x8;
  __device__ static __forceinline__ T ld8(const bf16* p) { return *reinterpret_cast<const bf16x8*>(p); }
  __device__ static __forceinline__ bf16x8 tobf(T x) { return x; } };
template <> struct Stage<float> { using T = f32x8;
  __device__ static __forceinline__ T ld8(const float* p) { return *reinterpret_cast<const f32x8*>(p); }
  __device__ static __forceinline__ bf16x8 tobf(T x) {
    u32x4 w = {cvtpk(x[0], x[1]), cvtpk(x[2], x[3]), cvtpk(x[4], x[5]), cvtpk(x[6], x[7])}; return *reinterpret_cast<bf16x8*>(&w); } };

__device__ __forceinline__ void partialSM(f32x16& p0, f32x16& p1, float& m_reg, float& mn, float& alpha) {
  constexpr float C = SCALE * 1.4426950408889634f;
  float pmax = p0[0]; for (int r = 1; r < 16; ++r) pmax = fmaxf(pmax, p0[r]); for (int r = 0; r < 16; ++r) pmax = fmaxf(pmax, p1[r]);
  { auto rr = __builtin_amdgcn_permlane32_swap(__float_as_uint(pmax), __float_as_uint(pmax), false, false);
    pmax = fmaxf(__uint_as_float(rr[0]), __uint_as_float(rr[1])); }
  if (__builtin_expect(__all(pmax - m_reg <= THR / SCALE), 1)) { mn = m_reg; alpha = 1.f; }
  else { mn = fmaxf(m_reg, pmax); alpha = __builtin_amdgcn_exp2f((m_reg - mn) * C); m_reg = mn; }
  float mnC = -mn * C;
  for (int r = 0; r < 16; ++r) p0[r] = fmaf(p0[r], C, mnC); for (int r = 0; r < 16; ++r) p1[r] = fmaf(p1[r], C, mnC);
  for (int r = 0; r < 16; ++r) p0[r] = __builtin_amdgcn_exp2f(p0[r]);
}
__device__ __forceinline__ void finishSM(f32x16& p0, f32x16& p1, float alpha, float& l_reg, bf16x8& pa0, bf16x8& pa1, bf16x8& pa2, bf16x8& pa3) {
  for (int r = 0; r < 16; ++r) p1[r] = __builtin_amdgcn_exp2f(p1[r]);
  float ps = 0; for (int r = 0; r < 16; ++r) ps += p0[r]; for (int r = 0; r < 16; ++r) ps += p1[r];
  { auto rr = __builtin_amdgcn_permlane32_swap(__float_as_uint(ps), __float_as_uint(ps), false, false);
    ps = __uint_as_float(rr[0]) + __uint_as_float(rr[1]); }
  l_reg = l_reg * alpha + ps;
#define PK4(P, BASE, OUT) do { unsigned a0 = cvtpk(P[BASE + 0], P[BASE + 1]), a1 = cvtpk(P[BASE + 2], P[BASE + 3]);   \
    unsigned b0 = cvtpk(P[BASE + 4], P[BASE + 5]), b1 = cvtpk(P[BASE + 6], P[BASE + 7]);                              \
    auto r0 = __builtin_amdgcn_permlane32_swap(a0, b0, false, false); auto r1 = __builtin_amdgcn_permlane32_swap(a1, b1, false, false); \
    u32x4 w = {r0[0], r1[0], r0[1], r1[1]}; OUT = *reinterpret_cast<bf16x8*>(&w); } while (0)
  PK4(p0, 0, pa0); PK4(p0, 8, pa1); PK4(p1, 0, pa2); PK4(p1, 8, pa3);
#undef PK4
}
__device__ __forceinline__ void qkt(f32x16& p0, f32x16& p1, const bf16* Ks, const bf16x8* qr, int r32, int hi) {
  p0 = f32x16{}; p1 = f32x16{};
  for (int d0 = 0; d0 < 8; ++d0) { int cb = (d0 * 16 + hi * 8) * 2;
    bf16x8 b0 = *reinterpret_cast<const bf16x8*>((const char*)Ks + KSWZ(r32, cb));
    bf16x8 b1 = *reinterpret_cast<const bf16x8*>((const char*)Ks + KSWZ(32 + r32, cb));
    p0 = __builtin_amdgcn_mfma_f32_32x32x16_bf16(b0, qr[d0], p0, 0, 0, 0);
    p1 = __builtin_amdgcn_mfma_f32_32x32x16_bf16(b1, qr[d0], p1, 0, 0, 0); }
}
__device__ __forceinline__ int v_st(int k, int c) { const int kk = (k & ~0xC) | ((k & 4) << 1) | ((k & 8) >> 1); return ((kk >> 3) * 4 + (c >> 5)) * 512 + ((kk & 7) * 32 + (c & 31)) * 2; }
__device__ __forceinline__ int v_rd_base(int lane) { return ((lane & 3) << 3) | (((lane >> 2) & 3) << 6) | (((lane >> 4) & 1) << 5) | (((lane >> 5) & 1) << 8); }
constexpr int v_rd_off(int d0, int ks, int half) { return d0 * 512 + ks * 4096 + half * 2048; }
template <int OFF> __device__ __forceinline__ s16x4 tr_read(int vb) {
  s16x4 r; asm volatile("ds_read_b64_tr_b16 %0, %1 offset:%2" : "=&v"(r) : "v"(vb), "i"(OFF) : "memory"); return r;
}
template <int D0> __device__ __forceinline__ void pv_one(f32x16& od, int vb, bf16x8 pa0, bf16x8 pa1, bf16x8 pa2, bf16x8 pa3) {
  const s16x4 l0 = tr_read<v_rd_off(D0, 0, 0)>(vb), h0 = tr_read<v_rd_off(D0, 0, 1)>(vb), l1 = tr_read<v_rd_off(D0, 1, 0)>(vb), h1 = tr_read<v_rd_off(D0, 1, 1)>(vb);
  const s16x4 l2 = tr_read<v_rd_off(D0, 2, 0)>(vb), h2 = tr_read<v_rd_off(D0, 2, 1)>(vb), l3 = tr_read<v_rd_off(D0, 3, 0)>(vb), h3 = tr_read<v_rd_off(D0, 3, 1)>(vb);
  asm volatile("s_waitcnt lgkmcnt(0)" ::: "memory"); SBAR();
#define PK(L, H) (bf16x8){L[0], L[1], L[2], L[3], H[0], H[1], H[2], H[3]}
  od = __builtin_amdgcn_mfma_f32_32x32x16_bf16(pa0, PK(l0, h0), od, 0, 0, 0);
  od = __builtin_amdgcn_mfma_f32_32x32x16_bf16(pa1, PK(l1, h1), od, 0, 0, 0);
  od = __builtin_amdgcn_mfma_f32_32x32x16_bf16(pa2, PK(l2, h2), od, 0, 0, 0);
  od = __builtin_amdgcn_mfma_f32_32x32x16_bf16(pa3, PK(l3, h3), od, 0, 0, 0);
#undef PK
}
__device__ __forceinline__ void pv_d0(f32x16* o, int vb, bf16x8 pa0, bf16x8 pa1, bf16x8 pa2, bf16x8 pa3) {
  pv_one<0>(o[0], vb, pa0, pa1, pa2, pa3); pv_one<1>(o[1], vb, pa0, pa1, pa2, pa3); pv_one<2>(o[2], vb, pa0, pa1, pa2, pa3); pv_one<3>(o[3], vb, pa0, pa1, pa2, pa3);
}

template <typename TQ>
__device__ __forceinline__ void attn_dense_body(const TQ* __restrict__ Qb, const bf16* __restrict__ Kh, const bf16* __restrict__ Vh,
                                                bf16* __restrict__ Ob, int seq, char* lds) {
  using St = Stage<bf16>; using SQ = Stage<TQ>;
  const int tid = threadIdx.x, wid = tid >> 6, lane = tid & 63, r32 = lane & 31, hi = lane >> 5;
  bf16* V_lds = (bf16*)lds; bf16* K_lds = (bf16*)(lds + 2 * SHM_V);
  float* ws = (float*)(lds + 2 * SHM_V + 2 * SHM_K) + wid * 64; float* li_l = ws; float* al_l = ws + 32;
  float m_reg = -1e30f, l_reg = 0; f32x16 o[4] = {}; bf16x8 qr[8];
  const TQ* Qw = Qb + (long)(wid * QBLK + r32) * LDQ + hi * 8;
#pragma unroll
  for (int d0 = 0; d0 < 8; ++d0) qr[d0] = SQ::tobf(SQ::ld8(Qw + d0 * 16));
  const int sr = tid >> 4, sc = (tid & 15) * 8, vst0 = v_st(sr, sc), vst1 = v_st(32 + sr, sc);
  const int vb0 = (int)(uintptr_t)V_lds + v_rd_base(lane);
  struct { typename St::T vs0, vs1, ks0, ks1; } sr_[SDEPTH];
#define SLOAD(i, k0) do { sr_[i].vs0 = St::ld8(&Vh[(long)((k0) + sr) * LDK + sc]); sr_[i].vs1 = St::ld8(&Vh[(long)((k0) + 32 + sr) * LDK + sc]); \
    sr_[i].ks0 = St::ld8(&Kh[(long)((k0) + sr) * LDK + sc]); sr_[i].ks1 = St::ld8(&Kh[(long)((k0) + 32 + sr) * LDK + sc]); } while (0)
#define SWRITE(b, i) do { *(bf16x8*)((char*)V_lds + (b) * SHM_V + vst0) = St::tobf(sr_[i].vs0);          \
    *(bf16x8*)((char*)V_lds + (b) * SHM_V + vst1) = St::tobf(sr_[i].vs1); int kc = sc * 2;               \
    *(bf16x8*)((char*)K_lds + (b) * SHM_K + KSWZ(sr, kc)) = St::tobf(sr_[i].ks0);                       \
    *(bf16x8*)((char*)K_lds + (b) * SHM_K + KSWZ(32 + sr, kc)) = St::tobf(sr_[i].ks1); } while (0)
#define SWAIT() do { if constexpr (SDEPTH == 2) asm volatile("s_waitcnt vmcnt(4)" ::: "memory"); else asm volatile("s_waitcnt vmcnt(0)" ::: "memory"); } while (0)
#define RESC(a) do { if (__any((a) < 1.f)) { if (hi == 0) al_l[r32] = (a); asm volatile("s_waitcnt lgkmcnt(0)" ::: "memory"); \
    for (int d = 0; d < 4; ++d) for (int r = 0; r < 16; ++r) o[d][r] *= al_l[crow(r, hi)]; } } while (0)
  f32x16 pA0, pA1, pB0, pB1; float mnA, mnB, alA, alB; bf16x8 pa0, pa1, pa2, pa3; const int NT = seq / KVBLK;
  constexpr int SE = 0, SO = SDEPTH - 1;
  SLOAD(SE, 0); asm volatile("s_waitcnt vmcnt(0)" ::: "memory"); SWRITE(0, SE); __syncthreads();
  qkt(pA0, pA1, K_lds, qr, r32, hi); partialSM(pA0, pA1, m_reg, mnA, alA);
  SLOAD(SO, KVBLK); if constexpr (SDEPTH == 2) { if (2 < NT) SLOAD(SE, 2 * KVBLK); }
  SWAIT(); SWRITE(1, SO); __syncthreads();
  for (int j = 1; j + 1 < NT; j += 2) {
    SBAR(); qkt(pB0, pB1, (bf16*)((char*)K_lds + SHM_K), qr, r32, hi);
    finishSM(pA0, pA1, alA, l_reg, pa0, pa1, pa2, pa3); SBAR();
    SLOAD(SO, (j + SDEPTH) * KVBLK); SBAR();
    pv_d0(o, vb0, pa0, pa1, pa2, pa3); partialSM(pB0, pB1, m_reg, mnB, alB);
    __syncthreads(); SWAIT(); SWRITE(0, SE);
    RESC(alB); __syncthreads();
    SBAR(); qkt(pA0, pA1, K_lds, qr, r32, hi);
    finishSM(pB0, pB1, alB, l_reg, pa0, pa1, pa2, pa3); SBAR();
    if (SDEPTH == 1 || j + 3 < NT) SLOAD(SE, (j + 1 + SDEPTH) * KVBLK); SBAR();
    pv_d0(o, vb0 + (int)SHM_V, pa0, pa1, pa2, pa3); partialSM(pA0, pA1, m_reg, mnA, alA);
    __syncthreads(); SWAIT(); SWRITE(1, SO);
    RESC(alA); __syncthreads();
  }
  SBAR(); qkt(pB0, pB1, (bf16*)((char*)K_lds + SHM_K), qr, r32, hi);
  finishSM(pA0, pA1, alA, l_reg, pa0, pa1, pa2, pa3); SBAR();
  pv_d0(o, vb0, pa0, pa1, pa2, pa3); partialSM(pB0, pB1, m_reg, mnB, alB);
  __syncthreads(); RESC(alB);
  finishSM(pB0, pB1, alB, l_reg, pa0, pa1, pa2, pa3); SBAR();
  pv_d0(o, vb0 + (int)SHM_V, pa0, pa1, pa2, pa3);
  if (hi == 0) li_l[r32] = l_reg; asm volatile("s_waitcnt lgkmcnt(0)" ::: "memory");
  float rli[16];
#pragma unroll
  for (int r = 0; r < 16; ++r) rli[r] = __builtin_amdgcn_rcpf(li_l[crow(r, hi)]);
  bf16* Ow = Ob + (long)(wid * QBLK) * LDO;
#pragma unroll
  for (int r = 0; r < 16; ++r) { int orow = crow(r, hi);
    for (int d0 = 0; d0 < 4; ++d0) Ow[(long)orow * LDO + d0 * 32 + r32] = __float2bfloat16(o[d0][r] * rli[r]); }
#undef SLOAD
#undef SWRITE
#undef SWAIT
#undef RESC
}

template <typename TQ>
__global__ __launch_bounds__(64 * NW, 1)
void attn_fwd_dense(const TQ* __restrict__ Q, const bf16* __restrict__ K, const bf16* __restrict__ V, bf16* __restrict__ O, int sq, int skv, int GQA) {
  extern __shared__ __attribute__((aligned(16))) char shm[];
  const long b = blockIdx.z, h = blockIdx.y, Hq = gridDim.y, Hkv = Hq / GQA, kvh = h / GQA, i0 = (long)blockIdx.x * (QBLK * NW);
  if constexpr (LAYOUT == 0) {
    const long q0 = ((b * Hq + h) * sq + i0) * D, k0 = (b * Hkv + kvh) * skv * (long)D;
    attn_dense_body<TQ>(Q + q0, K + k0, V + k0, O + q0, skv, shm);
  } else {
    const long q0 = ((b * sq + i0) * HQ + h) * (long)D, k0 = (b * skv * HKV + kvh) * (long)D;
    attn_dense_body<TQ>(Q + q0, K + k0, V + k0, O + q0, skv, shm);
  }
}
__global__ __launch_bounds__(256) void cvt_f32_bf16(const float* __restrict__ a, bf16* __restrict__ ao, long na8,
                                                   const float* __restrict__ b, bf16* __restrict__ bo, long nb8) {
  for (long i = blockIdx.x * 256L + threadIdx.x; i < na8 + nb8; i += gridDim.x * 256L) {
    const bool inA = i < na8; const long j = inA ? i : i - na8;
    const f32x8 x = *reinterpret_cast<const f32x8*>((inA ? a : b) + j * 8);
    *reinterpret_cast<bf16x8*>((inA ? ao : bo) + j * 8) = Stage<float>::tobf(x);
  }
}

extern "C" void kernel_launch(void* const* d_in, const int* in_sizes, int n_in, void* d_out, int out_size, void* d_ws, size_t ws_size, hipStream_t stream) {
  hipFuncSetAttribute((const void*)attn_fwd_dense<bf16>, hipFuncAttributeMaxDynamicSharedMemorySize, (int)SHM_ATTN);
  hipLaunchKernelGGL(attn_fwd_dense<bf16>, dim3(S / (QBLK * NW), HQ, B), dim3(64 * NW), SHM_ATTN, stream,
                     (const bf16*)d_in[0], (const bf16*)d_in[1], (const bf16*)d_in[2], (bf16*)d_out, S, SKV, HQ / HKV);
}
```

```cpp
#include <hip/hip_runtime.h>
#include <hip/hip_bf16.h>

extern "C" __global__ __launch_bounds__(256, 1)
void attn_fwd_k(const unsigned short* __restrict__ Q, const unsigned short* __restrict__ K, const unsigned short* __restrict__ V, unsigned short* __restrict__ O) {
  __shared__ __attribute__((aligned(16))) char lds[163840];
  const unsigned blk = blockIdx.x;
  const unsigned h = (blk & 7u) * 4u + ((blk >> 6) & 3u), y = (blk >> 3) & 7u;
  const char* qb = (const char*)Q + ((size_t)h * 2048 + (size_t)y * 256) * 256;
  const char* kb = (const char*)K + (size_t)h * 2048 * 256;
  const char* vb = (const char*)V + (size_t)h * 2048 * 256;
  char* ob = (char*)O + ((size_t)h * 2048 + (size_t)y * 256) * 256;
  asm volatile(
    "v_and_b32_e32 v1, 63, %0\n\t"
    "v_lshrrev_b32_e32 v2, 6, %0\n\t"
    "s_nop 0\n\t"
    "v_readfirstlane_b32 s44, v2\n\t"
    "v_and_b32_e32 v3, 31, v1\n\t"
    "v_lshrrev_b32_e32 v4, 5, v1\n\t"
    "s_mov_b64 s[56:57], %1\n\t"
    "s_and_b32 s57, s57, 0xffff\n\t"
    "s_mov_b32 s58, 65536\n\t"
    "s_mov_b32 s59, 0x20000\n\t"
    "s_mov_b64 s[24:25], %2\n\t"
    "s_and_b32 s25, s25, 0xffff\n\t"
    "s_mov_b32 s26, 524288\n\t"
    "s_mov_b32 s27, 0x20000\n\t"
    "s_mov_b64 s[28:29], %3\n\t"
    "s_and_b32 s29, s29, 0xffff\n\t"
    "s_mov_b32 s30, 524288\n\t"
    "s_mov_b32 s31, 0x20000\n\t"
    "s_mov_b64 s[36:37], %4\n\t"
    "s_and_b32 s37, s37, 0xffff\n\t"
    "s_mov_b32 s38, 65536\n\t"
    "s_mov_b32 s39, 0x20000\n\t"
    "v_and_b32_e32 v5, 7, v3\n\t"
    "v_add_u32_e32 v6, 0, v4\n\t"
    "v_xor_b32_e32 v6, v6, v5\n\t"
    "v_lshlrev_b32_e32 v6, 4, v6\n\t"
    "v_lshl_add_u32 v8, v3, 8, v6\n\t"
    "v_add_u32_e32 v8, %5, v8\n\t"
    "v_add_u32_e32 v6, 2, v4\n\t"
    "v_xor_b32_e32 v6, v6, v5\n\t"
    "v_lshlrev_b32_e32 v6, 4, v6\n\t"
    "v_lshl_add_u32 v9, v3, 8, v6\n\t"
    "v_add_u32_e32 v9, %5, v9\n\t"
    "v_add_u32_e32 v6, 4, v4\n\t"
    "v_xor_b32_e32 v6, v6, v5\n\t"
    "v_lshlrev_b32_e32 v6, 4, v6\n\t"
    "v_lshl_add_u32 v10, v3, 8, v6\n\t"
    "v_add_u32_e32 v10, %5, v10\n\t"
    "v_add_u32_e32 v6, 6, v4\n\t"
    "v_xor_b32_e32 v6, v6, v5\n\t"
    "v_lshlrev_b32_e32 v6, 4, v6\n\t"
    "v_lshl_add_u32 v11, v3, 8, v6\n\t"
    "v_add_u32_e32 v11, %5, v11\n\t"
    "v_and_b32_e32 v5, 3, v1\n\t"
    "v_lshlrev_b32_e32 v5, 3, v5\n\t"
    "v_bfe_u32 v6, v1, 2, 2\n\t"
    "v_lshl_add_u32 v5, v6, 6, v5\n\t"
    "v_bfe_u32 v6, v1, 4, 1\n\t"
    "v_lshl_add_u32 v5, v6, 5, v5\n\t"
    "v_lshl_add_u32 v5, v4, 8, v5\n\t"
    "v_add_u32_e32 v12, 65536, v5\n\t"
    "v_add_u32_e32 v12, %5, v12\n\t"
    "s_lshl_b32 s52, s44, 12\n\t"
    "v_lshrrev_b32_e32 v5, 4, v1\n\t"
    "v_and_b32_e32 v6, 15, v1\n\t"
    "v_add_u32_e32 v7, 0, v5\n\t"
    "v_xor_b32_e32 v7, v7, v6\n\t"
    "v_lshlrev_b32_e32 v7, 4, v7\n\t"
    "v_lshl_add_u32 v7, v5, 8, v7\n\t"
    "v_add_u32_e32 v13, s52, v7\n\t"
    "v_add_u32_e32 v7, 4, v5\n\t"
    "v_xor_b32_e32 v7, v7, v6\n\t"
    "v_lshlrev_b32_e32 v7, 4, v7\n\t"
    "v_lshl_add_u32 v7, v5, 8, v7\n\t"
    "v_add_u32_e32 v14, s52, v7\n\t"
    "v_bfe_u32 v5, v1, 2, 3\n\t"
    "v_lshlrev_b32_e32 v5, 8, v5\n\t"
    "v_lshl_add_u32 v5, v4, 6, v5\n\t"
    "v_and_b32_e32 v6, 3, v1\n\t"
    "v_lshl_add_u32 v5, v6, 4, v5\n\t"
    "v_add_u32_e32 v15, s52, v5\n\t"
    "s_add_u32 s42, s52, 0\n\t"
    "s_add_u32 s43, s52, 65536\n\t"
    "s_add_u32 s42, s42, %5\n\t"
    "s_add_u32 s43, s43, %5\n\t"
    "v_lshlrev_b32_e32 v5, 4, v4\n\t"
    "v_lshl_add_u32 v16, v3, 8, v5\n\t"
    "s_lshl_b32 s50, s44, 14\n\t"
    "s_add_u32 s51, s50, 8192\n\t"
    "s_mov_b32 s40, 0\n\t"
    "s_mov_b32 s41, 0\n\t"
    "s_mov_b32 s45, 0x41000000\n\t"
    "s_mov_b32 s47, 0\n\t"
    "s_nop 4\n\t"
    "s_add_u32 m0, s42, 0\n\t"
    "s_nop 0\n\t"
    "buffer_load_dwordx4 v13, s[24:27], s40 offen offset:0 lds\n\t"
    "buffer_load_dwordx4 v14, s[24:27], s40 offen offset:1024 lds\n\t"
    "buffer_load_dwordx4 v13, s[24:27], s40 offen offset:2048 lds\n\t"
    "buffer_load_dwordx4 v14, s[24:27], s40 offen offset:3072 lds\n\t"
    "s_add_u32 s40, s40, 16384\n\t"
    "buffer_load_dwordx4 v[96:99], v16, s[56:59], s50 offen offset:0\n\t"
    "buffer_load_dwordx4 v[100:103], v16, s[56:59], s50 offen offset:32\n\t"
    "buffer_load_dwordx4 v[104:107], v16, s[56:59], s50 offen offset:64\n\t"
    "buffer_load_dwordx4 v[108:111], v16, s[56:59], s50 offen offset:96\n\t"
    "buffer_load_dwordx4 v[112:115], v16, s[56:59], s50 offen offset:128\n\t"
    "buffer_load_dwordx4 v[116:119], v16, s[56:59], s50 offen offset:160\n\t"
    "buffer_load_dwordx4 v[120:123], v16, s[56:59], s50 offen offset:192\n\t"
    "buffer_load_dwordx4 v[124:127], v16, s[56:59], s50 offen offset:224\n\t"
    "buffer_load_dwordx4 v[128:131], v16, s[56:59], s51 offen offset:0\n\t"
    "buffer_load_dwordx4 v[132:135], v16, s[56:59], s51 offen offset:32\n\t"
    "buffer_load_dwordx4 v[136:139], v16, s[56:59], s51 offen offset:64\n\t"
    "buffer_load_dwordx4 v[140:143], v16, s[56:59], s51 offen offset:96\n\t"
    "buffer_load_dwordx4 v[144:147], v16, s[56:59], s51 offen offset:128\n\t"
    "buffer_load_dwordx4 v[148:151], v16, s[56:59], s51 offen offset:160\n\t"
    "buffer_load_dwordx4 v[152:155], v16, s[56:59], s51 offen offset:192\n\t"
    "buffer_load_dwordx4 v[156:159], v16, s[56:59], s51 offen offset:224\n\t"
    "v_accvgpr_write_b32 a0, 0\n\t"
    "v_accvgpr_write_b32 a1, 0\n\t"
    "v_accvgpr_write_b32 a2, 0\n\t"
    "v_accvgpr_write_b32 a3, 0\n\t"
    "v_accvgpr_write_b32 a4, 0\n\t"
    "v_accvgpr_write_b32 a5, 0\n\t"
    "v_accvgpr_write_b32 a6, 0\n\t"
    "v_accvgpr_write_b32 a7, 0\n\t"
    "v_accvgpr_write_b32 a8, 0\n\t"
    "v_accvgpr_write_b32 a9, 0\n\t"
    "v_accvgpr_write_b32 a10, 0\n\t"
    "v_accvgpr_write_b32 a11, 0\n\t"
    "v_accvgpr_write_b32 a12, 0\n\t"
    "v_accvgpr_write_b32 a13, 0\n\t"
    "v_accvgpr_write_b32 a14, 0\n\t"
    "v_accvgpr_write_b32 a15, 0\n\t"
    "v_accvgpr_write_b32 a16, 0\n\t"
    "v_accvgpr_write_b32 a17, 0\n\t"
    "v_accvgpr_write_b32 a18, 0\n\t"
    "v_accvgpr_write_b32 a19, 0\n\t"
    "v_accvgpr_write_b32 a20, 0\n\t"
    "v_accvgpr_write_b32 a21, 0\n\t"
    "v_accvgpr_write_b32 a22, 0\n\t"
    "v_accvgpr_write_b32 a23, 0\n\t"
    "v_accvgpr_write_b32 a24, 0\n\t"
    "v_accvgpr_write_b32 a25, 0\n\t"
    "v_accvgpr_write_b32 a26, 0\n\t"
    "v_accvgpr_write_b32 a27, 0\n\t"
    "v_accvgpr_write_b32 a28, 0\n\t"
    "v_accvgpr_write_b32 a29, 0\n\t"
    "v_accvgpr_write_b32 a30, 0\n\t"
    "v_accvgpr_write_b32 a31, 0\n\t"
    "v_accvgpr_write_b32 a32, 0\n\t"
    "v_accvgpr_write_b32 a33, 0\n\t"
    "v_accvgpr_write_b32 a34, 0\n\t"
    "v_accvgpr_write_b32 a35, 0\n\t"
    "v_accvgpr_write_b32 a36, 0\n\t"
    "v_accvgpr_write_b32 a37, 0\n\t"
    "v_accvgpr_write_b32 a38, 0\n\t"
    "v_accvgpr_write_b32 a39, 0\n\t"
    "v_accvgpr_write_b32 a40, 0\n\t"
    "v_accvgpr_write_b32 a41, 0\n\t"
    "v_accvgpr_write_b32 a42, 0\n\t"
    "v_accvgpr_write_b32 a43, 0\n\t"
    "v_accvgpr_write_b32 a44, 0\n\t"
    "v_accvgpr_write_b32 a45, 0\n\t"
    "v_accvgpr_write_b32 a46, 0\n\t"
    "v_accvgpr_write_b32 a47, 0\n\t"
    "v_accvgpr_write_b32 a48, 0\n\t"
    "v_accvgpr_write_b32 a49, 0\n\t"
    "v_accvgpr_write_b32 a50, 0\n\t"
    "v_accvgpr_write_b32 a51, 0\n\t"
    "v_accvgpr_write_b32 a52, 0\n\t"
    "v_accvgpr_write_b32 a53, 0\n\t"
    "v_accvgpr_write_b32 a54, 0\n\t"
    "v_accvgpr_write_b32 a55, 0\n\t"
    "v_accvgpr_write_b32 a56, 0\n\t"
    "v_accvgpr_write_b32 a57, 0\n\t"
    "v_accvgpr_write_b32 a58, 0\n\t"
    "v_accvgpr_write_b32 a59, 0\n\t"
    "v_accvgpr_write_b32 a60, 0\n\t"
    "v_accvgpr_write_b32 a61, 0\n\t"
    "v_accvgpr_write_b32 a62, 0\n\t"
    "v_accvgpr_write_b32 a63, 0\n\t"
    "v_accvgpr_write_b32 a64, 0\n\t"
    "v_accvgpr_write_b32 a65, 0\n\t"
    "v_accvgpr_write_b32 a66, 0\n\t"
    "v_accvgpr_write_b32 a67, 0\n\t"
    "v_accvgpr_write_b32 a68, 0\n\t"
    "v_accvgpr_write_b32 a69, 0\n\t"
    "v_accvgpr_write_b32 a70, 0\n\t"
    "v_accvgpr_write_b32 a71, 0\n\t"
    "v_accvgpr_write_b32 a72, 0\n\t"
    "v_accvgpr_write_b32 a73, 0\n\t"
    "v_accvgpr_write_b32 a74, 0\n\t"
    "v_accvgpr_write_b32 a75, 0\n\t"
    "v_accvgpr_write_b32 a76, 0\n\t"
    "v_accvgpr_write_b32 a77, 0\n\t"
    "v_accvgpr_write_b32 a78, 0\n\t"
    "v_accvgpr_write_b32 a79, 0\n\t"
    "v_accvgpr_write_b32 a80, 0\n\t"
    "v_accvgpr_write_b32 a81, 0\n\t"
    "v_accvgpr_write_b32 a82, 0\n\t"
    "v_accvgpr_write_b32 a83, 0\n\t"
    "v_accvgpr_write_b32 a84, 0\n\t"
    "v_accvgpr_write_b32 a85, 0\n\t"
    "v_accvgpr_write_b32 a86, 0\n\t"
    "v_accvgpr_write_b32 a87, 0\n\t"
    "v_accvgpr_write_b32 a88, 0\n\t"
    "v_accvgpr_write_b32 a89, 0\n\t"
    "v_accvgpr_write_b32 a90, 0\n\t"
    "v_accvgpr_write_b32 a91, 0\n\t"
    "v_accvgpr_write_b32 a92, 0\n\t"
    "v_accvgpr_write_b32 a93, 0\n\t"
    "v_accvgpr_write_b32 a94, 0\n\t"
    "v_accvgpr_write_b32 a95, 0\n\t"
    "v_accvgpr_write_b32 a96, 0\n\t"
    "v_accvgpr_write_b32 a97, 0\n\t"
    "v_accvgpr_write_b32 a98, 0\n\t"
    "v_accvgpr_write_b32 a99, 0\n\t"
    "v_accvgpr_write_b32 a100, 0\n\t"
    "v_accvgpr_write_b32 a101, 0\n\t"
    "v_accvgpr_write_b32 a102, 0\n\t"
    "v_accvgpr_write_b32 a103, 0\n\t"
    "v_accvgpr_write_b32 a104, 0\n\t"
    "v_accvgpr_write_b32 a105, 0\n\t"
    "v_accvgpr_write_b32 a106, 0\n\t"
    "v_accvgpr_write_b32 a107, 0\n\t"
    "v_accvgpr_write_b32 a108, 0\n\t"
    "v_accvgpr_write_b32 a109, 0\n\t"
    "v_accvgpr_write_b32 a110, 0\n\t"
    "v_accvgpr_write_b32 a111, 0\n\t"
    "v_accvgpr_write_b32 a112, 0\n\t"
    "v_accvgpr_write_b32 a113, 0\n\t"
    "v_accvgpr_write_b32 a114, 0\n\t"
    "v_accvgpr_write_b32 a115, 0\n\t"
    "v_accvgpr_write_b32 a116, 0\n\t"
    "v_accvgpr_write_b32 a117, 0\n\t"
    "v_accvgpr_write_b32 a118, 0\n\t"
    "v_accvgpr_write_b32 a119, 0\n\t"
    "v_accvgpr_write_b32 a120, 0\n\t"
    "v_accvgpr_write_b32 a121, 0\n\t"
    "v_accvgpr_write_b32 a122, 0\n\t"
    "v_accvgpr_write_b32 a123, 0\n\t"
    "v_accvgpr_write_b32 a124, 0\n\t"
    "v_accvgpr_write_b32 a125, 0\n\t"
    "v_accvgpr_write_b32 a126, 0\n\t"
    "v_accvgpr_write_b32 a127, 0\n\t"
    "v_mov_b32_e32 v20, 0\n\t"
    "v_mov_b32_e32 v22, 0\n\t"
    "v_mov_b32_e32 v21, 0\n\t"
    "v_mov_b32_e32 v23, 0\n\t"
    "s_waitcnt vmcnt(8)\n\t"
    "v_lshlrev_b32_e32 v5, 16, v96\n\t"
    "v_and_b32_e32 v6, 0xffff0000, v96\n\t"
    "v_mul_f32_e32 v5, 0x3e0293ee, v5\n\t"
    "v_mul_f32_e32 v6, 0x3e0293ee, v6\n\t"
    "v_cvt_pk_bf16_f32 v5, v5, v6\n\t"
    "v_accvgpr_write_b32 a128, v5\n\t"
    "v_lshlrev_b32_e32 v17, 16, v97\n\t"
    "v_and_b32_e32 v18, 0xffff0000, v97\n\t"
    "v_mul_f32_e32 v17, 0x3e0293ee, v17\n\t"
    "v_mul_f32_e32 v18, 0x3e0293ee, v18\n\t"
    "v_cvt_pk_bf16_f32 v17, v17, v18\n\t"
    "v_accvgpr_write_b32 a129, v17\n\t"
    "v_lshlrev_b32_e32 v5, 16, v98\n\t"
    "v_and_b32_e32 v6, 0xffff0000, v98\n\t"
    "v_mul_f32_e32 v5, 0x3e0293ee, v5\n\t"
    "v_mul_f32_e32 v6, 0x3e0293ee, v6\n\t"
    "v_cvt_pk_bf16_f32 v5, v5, v6\n\t"
    "v_accvgpr_write_b32 a130, v5\n\t"
    "v_lshlrev_b32_e32 v17, 16, v99\n\t"
    "v_and_b32_e32 v18, 0xffff0000, v99\n\t"
    "v_mul_f32_e32 v17, 0x3e0293ee, v17\n\t"
    "v_mul_f32_e32 v18, 0x3e0293ee, v18\n\t"
    "v_cvt_pk_bf16_f32 v17, v17, v18\n\t"
    "v_accvgpr_write_b32 a131, v17\n\t"
    "v_lshlrev_b32_e32 v5, 16, v100\n\t"
    "v_and_b32_e32 v6, 0xffff0000, v100\n\t"
    "v_mul_f32_e32 v5, 0x3e0293ee, v5\n\t"
    "v_mul_f32_e32 v6, 0x3e0293ee, v6\n\t"
    "v_cvt_pk_bf16_f32 v5, v5, v6\n\t"
    "v_accvgpr_write_b32 a132, v5\n\t"
    "v_lshlrev_b32_e32 v17, 16, v101\n\t"
    "v_and_b32_e32 v18, 0xffff0000, v101\n\t"
    "v_mul_f32_e32 v17, 0x3e0293ee, v17\n\t"
    "v_mul_f32_e32 v18, 0x3e0293ee, v18\n\t"
    "v_cvt_pk_bf16_f32 v17, v17, v18\n\t"
    "v_accvgpr_write_b32 a133, v17\n\t"
    "v_lshlrev_b32_e32 v5, 16, v102\n\t"
    "v_and_b32_e32 v6, 0xffff0000, v102\n\t"
    "v_mul_f32_e32 v5, 0x3e0293ee, v5\n\t"
    "v_mul_f32_e32 v6, 0x3e0293ee, v6\n\t"
    "v_cvt_pk_bf16_f32 v5, v5, v6\n\t"
    "v_accvgpr_write_b32 a134, v5\n\t"
    "v_lshlrev_b32_e32 v17, 16, v103\n\t"
    "v_and_b32_e32 v18, 0xffff0000, v103\n\t"
    "v_mul_f32_e32 v17, 0x3e0293ee, v17\n\t"
    "v_mul_f32_e32 v18, 0x3e0293ee, v18\n\t"
    "v_cvt_pk_bf16_f32 v17, v17, v18\n\t"
    "v_accvgpr_write_b32 a135, v17\n\t"
    "v_lshlrev_b32_e32 v5, 16, v104\n\t"
    "v_and_b32_e32 v6, 0xffff0000, v104\n\t"
    "v_mul_f32_e32 v5, 0x3e0293ee, v5\n\t"
    "v_mul_f32_e32 v6, 0x3e0293ee, v6\n\t"
    "v_cvt_pk_bf16_f32 v5, v5, v6\n\t"
    "v_accvgpr_write_b32 a136, v5\n\t"
    "v_lshlrev_b32_e32 v17, 16, v105\n\t"
    "v_and_b32_e32 v18, 0xffff0000, v105\n\t"
    "v_mul_f32_e32 v17, 0x3e0293ee, v17\n\t"
    "v_mul_f32_e32 v18, 0x3e0293ee, v18\n\t"
    "v_cvt_pk_bf16_f32 v17, v17, v18\n\t"
    "v_accvgpr_write_b32 a137, v17\n\t"
    "v_lshlrev_b32_e32 v5, 16, v106\n\t"
    "v_and_b32_e32 v6, 0xffff0000, v106\n\t"
    "v_mul_f32_e32 v5, 0x3e0293ee, v5\n\t"
    "v_mul_f32_e32 v6, 0x3e0293ee, v6\n\t"
    "v_cvt_pk_bf16_f32 v5, v5, v6\n\t"
    "v_accvgpr_write_b32 a138, v5\n\t"
    "v_lshlrev_b32_e32 v17, 16, v107\n\t"
    "v_and_b32_e32 v18, 0xffff0000, v107\n\t"
    "v_mul_f32_e32 v17, 0x3e0293ee, v17\n\t"
    "v_mul_f32_e32 v18, 0x3e0293ee, v18\n\t"
    "v_cvt_pk_bf16_f32 v17, v17, v18\n\t"
    "v_accvgpr_write_b32 a139, v17\n\t"
    "v_lshlrev_b32_e32 v5, 16, v108\n\t"
    "v_and_b32_e32 v6, 0xffff0000, v108\n\t"
    "v_mul_f32_e32 v5, 0x3e0293ee, v5\n\t"
    "v_mul_f32_e32 v6, 0x3e0293ee, v6\n\t"
    "v_cvt_pk_bf16_f32 v5, v5, v6\n\t"
    "v_accvgpr_write_b32 a140, v5\n\t"
    "v_lshlrev_b32_e32 v17, 16, v109\n\t"
    "v_and_b32_e32 v18, 0xffff0000, v109\n\t"
    "v_mul_f32_e32 v17, 0x3e0293ee, v17\n\t"
    "v_mul_f32_e32 v18, 0x3e0293ee, v18\n\t"
    "v_cvt_pk_bf16_f32 v17, v17, v18\n\t"
    "v_accvgpr_write_b32 a141, v17\n\t"
    "v_lshlrev_b32_e32 v5, 16, v110\n\t"
    "v_and_b32_e32 v6, 0xffff0000, v110\n\t"
    "v_mul_f32_e32 v5, 0x3e0293ee, v5\n\t"
    "v_mul_f32_e32 v6, 0x3e0293ee, v6\n\t"
    "v_cvt_pk_bf16_f32 v5, v5, v6\n\t"
    "v_accvgpr_write_b32 a142, v5\n\t"
    "v_lshlrev_b32_e32 v17, 16, v111\n\t"
    "v_and_b32_e32 v18, 0xffff0000, v111\n\t"
    "v_mul_f32_e32 v17, 0x3e0293ee, v17\n\t"
    "v_mul_f32_e32 v18, 0x3e0293ee, v18\n\t"
    "v_cvt_pk_bf16_f32 v17, v17, v18\n\t"
    "v_accvgpr_write_b32 a143, v17\n\t"
    "v_lshlrev_b32_e32 v5, 16, v112\n\t"
    "v_and_b32_e32 v6, 0xffff0000, v112\n\t"
    "v_mul_f32_e32 v5, 0x3e0293ee, v5\n\t"
    "v_mul_f32_e32 v6, 0x3e0293ee, v6\n\t"
    "v_cvt_pk_bf16_f32 v5, v5, v6\n\t"
    "v_accvgpr_write_b32 a144, v5\n\t"
    "v_lshlrev_b32_e32 v17, 16, v113\n\t"
    "v_and_b32_e32 v18, 0xffff0000, v113\n\t"
    "v_mul_f32_e32 v17, 0x3e0293ee, v17\n\t"
    "v_mul_f32_e32 v18, 0x3e0293ee, v18\n\t"
    "v_cvt_pk_bf16_f32 v17, v17, v18\n\t"
    "v_accvgpr_write_b32 a145, v17\n\t"
    "v_lshlrev_b32_e32 v5, 16, v114\n\t"
    "v_and_b32_e32 v6, 0xffff0000, v114\n\t"
    "v_mul_f32_e32 v5, 0x3e0293ee, v5\n\t"
    "v_mul_f32_e32 v6, 0x3e0293ee, v6\n\t"
    "v_cvt_pk_bf16_f32 v5, v5, v6\n\t"
    "v_accvgpr_write_b32 a146, v5\n\t"
    "v_lshlrev_b32_e32 v17, 16, v115\n\t"
    "v_and_b32_e32 v18, 0xffff0000, v115\n\t"
    "v_mul_f32_e32 v17, 0x3e0293ee, v17\n\t"
    "v_mul_f32_e32 v18, 0x3e0293ee, v18\n\t"
    "v_cvt_pk_bf16_f32 v17, v17, v18\n\t"
    "v_accvgpr_write_b32 a147, v17\n\t"
    "v_lshlrev_b32_e32 v5, 16, v116\n\t"
    "v_and_b32_e32 v6, 0xffff0000, v116\n\t"
    "v_mul_f32_e32 v5, 0x3e0293ee, v5\n\t"
    "v_mul_f32_e32 v6, 0x3e0293ee, v6\n\t"
    "v_cvt_pk_bf16_f32 v5, v5, v6\n\t"
    "v_accvgpr_write_b32 a148, v5\n\t"
    "v_lshlrev_b32_e32 v17, 16, v117\n\t"
    "v_and_b32_e32 v18, 0xffff0000, v117\n\t"
    "v_mul_f32_e32 v17, 0x3e0293ee, v17\n\t"
    "v_mul_f32_e32 v18, 0x3e0293ee, v18\n\t"
    "v_cvt_pk_bf16_f32 v17, v17, v18\n\t"
    "v_accvgpr_write_b32 a149, v17\n\t"
    "v_lshlrev_b32_e32 v5, 16, v118\n\t"
    "v_and_b32_e32 v6, 0xffff0000, v118\n\t"
    "v_mul_f32_e32 v5, 0x3e0293ee, v5\n\t"
    "v_mul_f32_e32 v6, 0x3e0293ee, v6\n\t"
    "v_cvt_pk_bf16_f32 v5, v5, v6\n\t"
    "v_accvgpr_write_b32 a150, v5\n\t"
    "v_lshlrev_b32_e32 v17, 16, v119\n\t"
    "v_and_b32_e32 v18, 0xffff0000, v119\n\t"
    "v_mul_f32_e32 v17, 0x3e0293ee, v17\n\t"
    "v_mul_f32_e32 v18, 0x3e0293ee, v18\n\t"
    "v_cvt_pk_bf16_f32 v17, v17, v18\n\t"
    "v_accvgpr_write_b32 a151, v17\n\t"
    "v_lshlrev_b32_e32 v5, 16, v120\n\t"
    "v_and_b32_e32 v6, 0xffff0000, v120\n\t"
    "v_mul_f32_e32 v5, 0x3e0293ee, v5\n\t"
    "v_mul_f32_e32 v6, 0x3e0293ee, v6\n\t"
    "v_cvt_pk_bf16_f32 v5, v5, v6\n\t"
    "v_accvgpr_write_b32 a152, v5\n\t"
    "v_lshlrev_b32_e32 v17, 16, v121\n\t"
    "v_and_b32_e32 v18, 0xffff0000, v121\n\t"
    "v_mul_f32_e32 v17, 0x3e0293ee, v17\n\t"
    "v_mul_f32_e32 v18, 0x3e0293ee, v18\n\t"
    "v_cvt_pk_bf16_f32 v17, v17, v18\n\t"
    "v_accvgpr_write_b32 a153, v17\n\t"
    "v_lshlrev_b32_e32 v5, 16, v122\n\t"
    "v_and_b32_e32 v6, 0xffff0000, v122\n\t"
    "v_mul_f32_e32 v5, 0x3e0293ee, v5\n\t"
    "v_mul_f32_e32 v6, 0x3e0293ee, v6\n\t"
    "v_cvt_pk_bf16_f32 v5, v5, v6\n\t"
    "v_accvgpr_write_b32 a154, v5\n\t"
    "v_lshlrev_b32_e32 v17, 16, v123\n\t"
    "v_and_b32_e32 v18, 0xffff0000, v123\n\t"
    "v_mul_f32_e32 v17, 0x3e0293ee, v17\n\t"
    "v_mul_f32_e32 v18, 0x3e0293ee, v18\n\t"
    "v_cvt_pk_bf16_f32 v17, v17, v18\n\t"
    "v_accvgpr_write_b32 a155, v17\n\t"
    "v_lshlrev_b32_e32 v5, 16, v124\n\t"
    "v_and_b32_e32 v6, 0xffff0000, v124\n\t"
    "v_mul_f32_e32 v5, 0x3e0293ee, v5\n\t"
    "v_mul_f32_e32 v6, 0x3e0293ee, v6\n\t"
    "v_cvt_pk_bf16_f32 v5, v5, v6\n\t"
    "v_accvgpr_write_b32 a156, v5\n\t"
    "v_lshlrev_b32_e32 v17, 16, v125\n\t"
    "v_and_b32_e32 v18, 0xffff0000, v125\n\t"
    "v_mul_f32_e32 v17, 0x3e0293ee, v17\n\t"
    "v_mul_f32_e32 v18, 0x3e0293ee, v18\n\t"
    "v_cvt_pk_bf16_f32 v17, v17, v18\n\t"
    "v_accvgpr_write_b32 a157, v17\n\t"
    "v_lshlrev_b32_e32 v5, 16, v126\n\t"
    "v_and_b32_e32 v6, 0xffff0000, v126\n\t"
    "v_mul_f32_e32 v5, 0x3e0293ee, v5\n\t"
    "v_mul_f32_e32 v6, 0x3e0293ee, v6\n\t"
    "v_cvt_pk_bf16_f32 v5, v5, v6\n\t"
    "v_accvgpr_write_b32 a158, v5\n\t"
    "v_lshlrev_b32_e32 v17, 16, v127\n\t"
    "v_and_b32_e32 v18, 0xffff0000, v127\n\t"
    "v_mul_f32_e32 v17, 0x3e0293ee, v17\n\t"
    "v_mul_f32_e32 v18, 0x3e0293ee, v18\n\t"
    "v_cvt_pk_bf16_f32 v17, v17, v18\n\t"
    "v_accvgpr_write_b32 a159, v17\n\t"
    "s_waitcnt vmcnt(0)\n\t"
    "s_add_u32 m0, s43, 0\n\t"
    "s_nop 0\n\t"
    "buffer_load_dwordx4 v15, s[28:31], s41 offen offset:0 lds\n\t"
    "buffer_load_dwordx4 v15, s[28:31], s41 offen offset:2048 lds\n\t"
    "s_add_u32 m0, s43, 896\n\t"
    "s_nop 0\n\t"
    "buffer_load_dwordx4 v15, s[28:31], s41 offen offset:128 lds\n\t"
    "buffer_load_dwordx4 v15, s[28:31], s41 offen offset:2176 lds\n\t"
    "s_add_u32 s41, s41, 16384\n\t"
    "s_add_u32 m0, s42, 16384\n\t"
    "s_nop 0\n\t"
    "buffer_load_dwordx4 v13, s[24:27], s40 offen offset:0 lds\n\t"
    "buffer_load_dwordx4 v14, s[24:27], s40 offen offset:1024 lds\n\t"
    "buffer_load_dwordx4 v13, s[24:27], s40 offen offset:2048 lds\n\t"
    "buffer_load_dwordx4 v14, s[24:27], s40 offen offset:3072 lds\n\t"
    "s_add_u32 s40, s40, 16384\n\t"
    "s_add_u32 m0, s43, 16384\n\t"
    "s_nop 0\n\t"
    "buffer_load_dwordx4 v15, s[28:31], s41 offen offset:0 lds\n\t"
    "buffer_load_dwordx4 v15, s[28:31], s41 offen offset:2048 lds\n\t"
    "s_add_u32 m0, s43, 17280\n\t"
    "s_nop 0\n\t"
    "buffer_load_dwordx4 v15, s[28:31], s41 offen offset:128 lds\n\t"
    "buffer_load_dwordx4 v15, s[28:31], s41 offen offset:2176 lds\n\t"
    "s_add_u32 s41, s41, 16384\n\t"
    "s_add_u32 m0, s42, 32768\n\t"
    "s_nop 0\n\t"
    "buffer_load_dwordx4 v13, s[24:27], s40 offen offset:0 lds\n\t"
    "buffer_load_dwordx4 v14, s[24:27], s40 offen offset:1024 lds\n\t"
    "buffer_load_dwordx4 v13, s[24:27], s40 offen offset:2048 lds\n\t"
    "buffer_load_dwordx4 v14, s[24:27], s40 offen offset:3072 lds\n\t"
    "s_add_u32 s40, s40, 16384\n\t"
    "v_lshlrev_b32_e32 v5, 16, v128\n\t"
    "v_and_b32_e32 v6, 0xffff0000, v128\n\t"
    "v_mul_f32_e32 v5, 0x3e0293ee, v5\n\t"
    "v_mul_f32_e32 v6, 0x3e0293ee, v6\n\t"
    "v_cvt_pk_bf16_f32 v5, v5, v6\n\t"
    "v_accvgpr_write_b32 a160, v5\n\t"
    "v_lshlrev_b32_e32 v17, 16, v129\n\t"
    "v_and_b32_e32 v18, 0xffff0000, v129\n\t"
    "v_mul_f32_e32 v17, 0x3e0293ee, v17\n\t"
    "v_mul_f32_e32 v18, 0x3e0293ee, v18\n\t"
    "v_cvt_pk_bf16_f32 v17, v17, v18\n\t"
    "v_accvgpr_write_b32 a161, v17\n\t"
    "v_lshlrev_b32_e32 v5, 16, v130\n\t"
    "v_and_b32_e32 v6, 0xffff0000, v130\n\t"
    "v_mul_f32_e32 v5, 0x3e0293ee, v5\n\t"
    "v_mul_f32_e32 v6, 0x3e0293ee, v6\n\t"
    "v_cvt_pk_bf16_f32 v5, v5, v6\n\t"
    "v_accvgpr_write_b32 a162, v5\n\t"
    "v_lshlrev_b32_e32 v17, 16, v131\n\t"
    "v_and_b32_e32 v18, 0xffff0000, v131\n\t"
    "v_mul_f32_e32 v17, 0x3e0293ee, v17\n\t"
    "v_mul_f32_e32 v18, 0x3e0293ee, v18\n\t"
    "v_cvt_pk_bf16_f32 v17, v17, v18\n\t"
    "v_accvgpr_write_b32 a163, v17\n\t"
    "v_lshlrev_b32_e32 v5, 16, v132\n\t"
    "v_and_b32_e32 v6, 0xffff0000, v132\n\t"
    "v_mul_f32_e32 v5, 0x3e0293ee, v5\n\t"
    "v_mul_f32_e32 v6, 0x3e0293ee, v6\n\t"
    "v_cvt_pk_bf16_f32 v5, v5, v6\n\t"
    "v_accvgpr_write_b32 a164, v5\n\t"
    "v_lshlrev_b32_e32 v17, 16, v133\n\t"
    "v_and_b32_e32 v18, 0xffff0000, v133\n\t"
    "v_mul_f32_e32 v17, 0x3e0293ee, v17\n\t"
    "v_mul_f32_e32 v18, 0x3e0293ee, v18\n\t"
    "v_cvt_pk_bf16_f32 v17, v17, v18\n\t"
    "v_accvgpr_write_b32 a165, v17\n\t"
    "v_lshlrev_b32_e32 v5, 16, v134\n\t"
    "v_and_b32_e32 v6, 0xffff0000, v134\n\t"
    "v_mul_f32_e32 v5, 0x3e0293ee, v5\n\t"
    "v_mul_f32_e32 v6, 0x3e0293ee, v6\n\t"
    "v_cvt_pk_bf16_f32 v5, v5, v6\n\t"
    "v_accvgpr_write_b32 a166, v5\n\t"
    "v_lshlrev_b32_e32 v17, 16, v135\n\t"
    "v_and_b32_e32 v18, 0xffff0000, v135\n\t"
    "v_mul_f32_e32 v17, 0x3e0293ee, v17\n\t"
    "v_mul_f32_e32 v18, 0x3e0293ee, v18\n\t"
    "v_cvt_pk_bf16_f32 v17, v17, v18\n\t"
    "v_accvgpr_write_b32 a167, v17\n\t"
    "v_lshlrev_b32_e32 v5, 16, v136\n\t"
    "v_and_b32_e32 v6, 0xffff0000, v136\n\t"
    "v_mul_f32_e32 v5, 0x3e0293ee, v5\n\t"
    "v_mul_f32_e32 v6, 0x3e0293ee, v6\n\t"
    "v_cvt_pk_bf16_f32 v5, v5, v6\n\t"
    "v_accvgpr_write_b32 a168, v5\n\t"
    "v_lshlrev_b32_e32 v17, 16, v137\n\t"
    "v_and_b32_e32 v18, 0xffff0000, v137\n\t"
    "v_mul_f32_e32 v17, 0x3e0293ee, v17\n\t"
    "v_mul_f32_e32 v18, 0x3e0293ee, v18\n\t"
    "v_cvt_pk_bf16_f32 v17, v17, v18\n\t"
    "v_accvgpr_write_b32 a169, v17\n\t"
    "v_lshlrev_b32_e32 v5, 16, v138\n\t"
    "v_and_b32_e32 v6, 0xffff0000, v138\n\t"
    "v_mul_f32_e32 v5, 0x3e0293ee, v5\n\t"
    "v_mul_f32_e32 v6, 0x3e0293ee, v6\n\t"
    "v_cvt_pk_bf16_f32 v5, v5, v6\n\t"
    "v_accvgpr_write_b32 a170, v5\n\t"
    "v_lshlrev_b32_e32 v17, 16, v139\n\t"
    "v_and_b32_e32 v18, 0xffff0000, v139\n\t"
    "v_mul_f32_e32 v17, 0x3e0293ee, v17\n\t"
    "v_mul_f32_e32 v18, 0x3e0293ee, v18\n\t"
    "v_cvt_pk_bf16_f32 v17, v17, v18\n\t"
    "v_accvgpr_write_b32 a171, v17\n\t"
    "v_lshlrev_b32_e32 v5, 16, v140\n\t"
    "v_and_b32_e32 v6, 0xffff0000, v140\n\t"
    "v_mul_f32_e32 v5, 0x3e0293ee, v5\n\t"
    "v_mul_f32_e32 v6, 0x3e0293ee, v6\n\t"
    "v_cvt_pk_bf16_f32 v5, v5, v6\n\t"
    "v_accvgpr_write_b32 a172, v5\n\t"
    "v_lshlrev_b32_e32 v17, 16, v141\n\t"
    "v_and_b32_e32 v18, 0xffff0000, v141\n\t"
    "v_mul_f32_e32 v17, 0x3e0293ee, v17\n\t"
    "v_mul_f32_e32 v18, 0x3e0293ee, v18\n\t"
    "v_cvt_pk_bf16_f32 v17, v17, v18\n\t"
    "v_accvgpr_write_b32 a173, v17\n\t"
    "v_lshlrev_b32_e32 v5, 16, v142\n\t"
    "v_and_b32_e32 v6, 0xffff0000, v142\n\t"
    "v_mul_f32_e32 v5, 0x3e0293ee, v5\n\t"
    "v_mul_f32_e32 v6, 0x3e0293ee, v6\n\t"
    "v_cvt_pk_bf16_f32 v5, v5, v6\n\t"
    "v_accvgpr_write_b32 a174, v5\n\t"
    "v_lshlrev_b32_e32 v17, 16, v143\n\t"
    "v_and_b32_e32 v18, 0xffff0000, v143\n\t"
    "v_mul_f32_e32 v17, 0x3e0293ee, v17\n\t"
    "v_mul_f32_e32 v18, 0x3e0293ee, v18\n\t"
    "v_cvt_pk_bf16_f32 v17, v17, v18\n\t"
    "v_accvgpr_write_b32 a175, v17\n\t"
    "v_lshlrev_b32_e32 v5, 16, v144\n\t"
    "v_and_b32_e32 v6, 0xffff0000, v144\n\t"
    "v_mul_f32_e32 v5, 0x3e0293ee, v5\n\t"
    "v_mul_f32_e32 v6, 0x3e0293ee, v6\n\t"
    "v_cvt_pk_bf16_f32 v5, v5, v6\n\t"
    "v_accvgpr_write_b32 a176, v5\n\t"
    "v_lshlrev_b32_e32 v17, 16, v145\n\t"
    "v_and_b32_e32 v18, 0xffff0000, v145\n\t"
    "v_mul_f32_e32 v17, 0x3e0293ee, v17\n\t"
    "v_mul_f32_e32 v18, 0x3e0293ee, v18\n\t"
    "v_cvt_pk_bf16_f32 v17, v17, v18\n\t"
    "v_accvgpr_write_b32 a177, v17\n\t"
    "v_lshlrev_b32_e32 v5, 16, v146\n\t"
    "v_and_b32_e32 v6, 0xffff0000, v146\n\t"
    "v_mul_f32_e32 v5, 0x3e0293ee, v5\n\t"
    "v_mul_f32_e32 v6, 0x3e0293ee, v6\n\t"
    "v_cvt_pk_bf16_f32 v5, v5, v6\n\t"
    "v_accvgpr_write_b32 a178, v5\n\t"
    "v_lshlrev_b32_e32 v17, 16, v147\n\t"
    "v_and_b32_e32 v18, 0xffff0000, v147\n\t"
    "v_mul_f32_e32 v17, 0x3e0293ee, v17\n\t"
    "v_mul_f32_e32 v18, 0x3e0293ee, v18\n\t"
    "v_cvt_pk_bf16_f32 v17, v17, v18\n\t"
    "v_accvgpr_write_b32 a179, v17\n\t"
    "v_lshlrev_b32_e32 v5, 16, v148\n\t"
    "v_and_b32_e32 v6, 0xffff0000, v148\n\t"
    "v_mul_f32_e32 v5, 0x3e0293ee, v5\n\t"
    "v_mul_f32_e32 v6, 0x3e0293ee, v6\n\t"
    "v_cvt_pk_bf16_f32 v5, v5, v6\n\t"
    "v_accvgpr_write_b32 a180, v5\n\t"
    "v_lshlrev_b32_e32 v17, 16, v149\n\t"
    "v_and_b32_e32 v18, 0xffff0000, v149\n\t"
    "v_mul_f32_e32 v17, 0x3e0293ee, v17\n\t"
    "v_mul_f32_e32 v18, 0x3e0293ee, v18\n\t"
    "v_cvt_pk_bf16_f32 v17, v17, v18\n\t"
    "v_accvgpr_write_b32 a181, v17\n\t"
    "v_lshlrev_b32_e32 v5, 16, v150\n\t"
    "v_and_b32_e32 v6, 0xffff0000, v150\n\t"
    "v_mul_f32_e32 v5, 0x3e0293ee, v5\n\t"
    "v_mul_f32_e32 v6, 0x3e0293ee, v6\n\t"
    "v_cvt_pk_bf16_f32 v5, v5, v6\n\t"
    "v_accvgpr_write_b32 a182, v5\n\t"
    "v_lshlrev_b32_e32 v17, 16, v151\n\t"
    "v_and_b32_e32 v18, 0xffff0000, v151\n\t"
    "v_mul_f32_e32 v17, 0x3e0293ee, v17\n\t"
    "v_mul_f32_e32 v18, 0x3e0293ee, v18\n\t"
    "v_cvt_pk_bf16_f32 v17, v17, v18\n\t"
    "v_accvgpr_write_b32 a183, v17\n\t"
    "v_lshlrev_b32_e32 v5, 16, v152\n\t"
    "v_and_b32_e32 v6, 0xffff0000, v152\n\t"
    "v_mul_f32_e32 v5, 0x3e0293ee, v5\n\t"
    "v_mul_f32_e32 v6, 0x3e0293ee, v6\n\t"
    "v_cvt_pk_bf16_f32 v5, v5, v6\n\t"
    "v_accvgpr_write_b32 a184, v5\n\t"
    "v_lshlrev_b32_e32 v17, 16, v153\n\t"
    "v_and_b32_e32 v18, 0xffff0000, v153\n\t"
    "v_mul_f32_e32 v17, 0x3e0293ee, v17\n\t"
    "v_mul_f32_e32 v18, 0x3e0293ee, v18\n\t"
    "v_cvt_pk_bf16_f32 v17, v17, v18\n\t"
    "v_accvgpr_write_b32 a185, v17\n\t"
    "v_lshlrev_b32_e32 v5, 16, v154\n\t"
    "v_and_b32_e32 v6, 0xffff0000, v154\n\t"
    "v_mul_f32_e32 v5, 0x3e0293ee, v5\n\t"
    "v_mul_f32_e32 v6, 0x3e0293ee, v6\n\t"
    "v_cvt_pk_bf16_f32 v5, v5, v6\n\t"
    "v_accvgpr_write_b32 a186, v5\n\t"
    "v_lshlrev_b32_e32 v17, 16, v155\n\t"
    "v_and_b32_e32 v18, 0xffff0000, v155\n\t"
    "v_mul_f32_e32 v17, 0x3e0293ee, v17\n\t"
    "v_mul_f32_e32 v18, 0x3e0293ee, v18\n\t"
    "v_cvt_pk_bf16_f32 v17, v17, v18\n\t"
    "v_accvgpr_write_b32 a187, v17\n\t"
    "v_lshlrev_b32_e32 v5, 16, v156\n\t"
    "v_and_b32_e32 v6, 0xffff0000, v156\n\t"
    "v_mul_f32_e32 v5, 0x3e0293ee, v5\n\t"
    "v_mul_f32_e32 v6, 0x3e0293ee, v6\n\t"
    "v_cvt_pk_bf16_f32 v5, v5, v6\n\t"
    "v_accvgpr_write_b32 a188, v5\n\t"
    "v_lshlrev_b32_e32 v17, 16, v157\n\t"
    "v_and_b32_e32 v18, 0xffff0000, v157\n\t"
    "v_mul_f32_e32 v17, 0x3e0293ee, v17\n\t"
    "v_mul_f32_e32 v18, 0x3e0293ee, v18\n\t"
    "v_cvt_pk_bf16_f32 v17, v17, v18\n\t"
    "v_accvgpr_write_b32 a189, v17\n\t"
    "v_lshlrev_b32_e32 v5, 16, v158\n\t"
    "v_and_b32_e32 v6, 0xffff0000, v158\n\t"
    "v_mul_f32_e32 v5, 0x3e0293ee, v5\n\t"
    "v_mul_f32_e32 v6, 0x3e0293ee, v6\n\t"
    "v_cvt_pk_bf16_f32 v5, v5, v6\n\t"
    "v_accvgpr_write_b32 a190, v5\n\t"
    "v_lshlrev_b32_e32 v17, 16, v159\n\t"
    "v_and_b32_e32 v18, 0xffff0000, v159\n\t"
    "v_mul_f32_e32 v17, 0x3e0293ee, v17\n\t"
    "v_mul_f32_e32 v18, 0x3e0293ee, v18\n\t"
    "v_cvt_pk_bf16_f32 v17, v17, v18\n\t"
    "v_accvgpr_write_b32 a191, v17\n\t"
    "s_barrier\n\t"
    "ds_read_b128 a[192:195], v8 offset:0\n\t"
    "ds_read_b128 a[196:199], v9 offset:0\n\t"
    "ds_read_b128 a[200:203], v10 offset:0\n\t"
    "ds_read_b128 a[204:207], v11 offset:0\n\t"
    "ds_read_b128 a[208:211], v8 offset:128\n\t"
    "ds_read_b128 a[212:215], v9 offset:128\n\t"
    "ds_read_b128 a[216:219], v10 offset:128\n\t"
    "ds_read_b128 a[220:223], v11 offset:128\n\t"
    "ds_read_b128 a[224:227], v8 offset:8192\n\t"
    "ds_read_b128 a[228:231], v9 offset:8192\n\t"
    "ds_read_b128 a[232:235], v10 offset:8192\n\t"
    "ds_read_b128 a[236:239], v11 offset:8192\n\t"
    "ds_read_b128 a[240:243], v8 offset:8320\n\t"
    "ds_read_b128 a[244:247], v9 offset:8320\n\t"
    "ds_read_b128 a[248:251], v10 offset:8320\n\t"
    "ds_read_b128 a[252:255], v11 offset:8320\n\t"
    "s_waitcnt lgkmcnt(0)\n\t"
    "s_nop 1\n\t"
    "v_mfma_f32_32x32x16_bf16 v[32:47], a[192:195], a[128:131], 0\n\t"
    "v_mfma_f32_32x32x16_bf16 v[48:63], a[192:195], a[160:163], 0\n\t"
    "v_mfma_f32_32x32x16_bf16 v[64:79], a[224:227], a[128:131], 0\n\t"
    "v_mfma_f32_32x32x16_bf16 v[80:95], a[224:227], a[160:163], 0\n\t"
    "v_mfma_f32_32x32x16_bf16 v[32:47], a[196:199], a[132:135], v[32:47]\n\t"
    "v_mfma_f32_32x32x16_bf16 v[48:63], a[196:199], a[164:167], v[48:63]\n\t"
    "v_mfma_f32_32x32x16_bf16 v[64:79], a[228:231], a[132:135], v[64:79]\n\t"
    "v_mfma_f32_32x32x16_bf16 v[80:95], a[228:231], a[164:167], v[80:95]\n\t"
    "v_mfma_f32_32x32x16_bf16 v[32:47], a[200:203], a[136:139], v[32:47]\n\t"
    "v_mfma_f32_32x32x16_bf16 v[48:63], a[200:203], a[168:171], v[48:63]\n\t"
    "v_mfma_f32_32x32x16_bf16 v[64:79], a[232:235], a[136:139], v[64:79]\n\t"
    "v_mfma_f32_32x32x16_bf16 v[80:95], a[232:235], a[168:171], v[80:95]\n\t"
    "v_mfma_f32_32x32x16_bf16 v[32:47], a[204:207], a[140:143], v[32:47]\n\t"
    "v_mfma_f32_32x32x16_bf16 v[48:63], a[204:207], a[172:175], v[48:63]\n\t"
    "v_mfma_f32_32x32x16_bf16 v[64:79], a[236:239], a[140:143], v[64:79]\n\t"
    "v_mfma_f32_32x32x16_bf16 v[80:95], a[236:239], a[172:175], v[80:95]\n\t"
    "v_mfma_f32_32x32x16_bf16 v[32:47], a[208:211], a[144:147], v[32:47]\n\t"
    "v_mfma_f32_32x32x16_bf16 v[48:63], a[208:211], a[176:179], v[48:63]\n\t"
    "v_mfma_f32_32x32x16_bf16 v[64:79], a[240:243], a[144:147], v[64:79]\n\t"
    "v_mfma_f32_32x32x16_bf16 v[80:95], a[240:243], a[176:179], v[80:95]\n\t"
    "v_mfma_f32_32x32x16_bf16 v[32:47], a[212:215], a[148:151], v[32:47]\n\t"
    "v_mfma_f32_32x32x16_bf16 v[48:63], a[212:215], a[180:183], v[48:63]\n\t"
    "v_mfma_f32_32x32x16_bf16 v[64:79], a[244:247], a[148:151], v[64:79]\n\t"
    "v_mfma_f32_32x32x16_bf16 v[80:95], a[244:247], a[180:183], v[80:95]\n\t"
    "v_mfma_f32_32x32x16_bf16 v[32:47], a[216:219], a[152:155], v[32:47]\n\t"
    "v_mfma_f32_32x32x16_bf16 v[48:63], a[216:219], a[184:187], v[48:63]\n\t"
    "v_mfma_f32_32x32x16_bf16 v[64:79], a[248:251], a[152:155], v[64:79]\n\t"
    "v_mfma_f32_32x32x16_bf16 v[80:95], a[248:251], a[184:187], v[80:95]\n\t"
    "v_mfma_f32_32x32x16_bf16 v[32:47], a[220:223], a[156:159], v[32:47]\n\t"
    "v_mfma_f32_32x32x16_bf16 v[48:63], a[220:223], a[188:191], v[48:63]\n\t"
    "v_mfma_f32_32x32x16_bf16 v[64:79], a[252:255], a[156:159], v[64:79]\n\t"
    "v_mfma_f32_32x32x16_bf16 v[80:95], a[252:255], a[188:191], v[80:95]\n\t"
    "s_waitcnt vmcnt(8) lgkmcnt(0)\n\t"
    "s_barrier\n\t"
    "s_add_u32 m0, s42, 49152\n\t"
    "s_nop 0\n\t"
    "buffer_load_dwordx4 v13, s[24:27], s40 offen offset:0 lds\n\t"
    "ds_read_b128 a[192:195], v8 offset:16384\n\t"
    "ds_read_b128 a[196:199], v9 offset:16384\n\t"
    "ds_read_b128 a[200:203], v10 offset:16384\n\t"
    "ds_read_b128 a[204:207], v11 offset:16384\n\t"
    "s_nop 1\n\t"
    "v_max3_f32 v26, v32, v33, v64\n\t"
    "v_max3_f32 v28, v34, v35, v65\n\t"
    "v_max3_f32 v26, v26, v66, v67\n\t"
    "v_max3_f32 v26, v26, v36, v37\n\t"
    "v_max3_f32 v28, v28, v38, v39\n\t"
    "buffer_load_dwordx4 v14, s[24:27], s40 offen offset:1024 lds\n\t"
    "ds_read_b128 a[208:211], v8 offset:16512\n\t"
    "v_max3_f32 v26, v26, v68, v69\n\t"
    "v_max3_f32 v28, v28, v70, v71\n\t"
    "v_max3_f32 v26, v26, v40, v41\n\t"
    "v_max3_f32 v28, v28, v42, v43\n\t"
    "ds_read_b128 a[212:215], v9 offset:16512\n\t"
    "v_max3_f32 v26, v26, v72, v73\n\t"
    "v_max3_f32 v28, v28, v74, v75\n\t"
    "v_max3_f32 v26, v26, v44, v45\n\t"
    "v_max3_f32 v28, v28, v46, v47\n\t"
    "v_max3_f32 v26, v26, v76, v77\n\t"
    "ds_read_b128 a[216:219], v10 offset:16512\n\t"
    "v_max3_f32 v28, v28, v78, v79\n\t"
    "v_max3_f32 v27, v48, v49, v80\n\t"
    "v_max3_f32 v29, v50, v51, v81\n\t"
    "v_max3_f32 v27, v27, v82, v83\n\t"
    "v_max3_f32 v27, v27, v52, v53\n\t"
    "ds_read_b128 a[220:223], v11 offset:16512\n\t"
    "v_max3_f32 v29, v29, v54, v55\n\t"
    "v_max3_f32 v27, v27, v84, v85\n\t"
    "v_max3_f32 v29, v29, v86, v87\n\t"
    "v_max3_f32 v27, v27, v56, v57\n\t"
    "v_max3_f32 v29, v29, v58, v59\n\t"
    "buffer_load_dwordx4 v13, s[24:27], s40 offen offset:2048 lds\n\t"
    "ds_read_b128 a[224:227], v8 offset:24576\n\t"
    "v_max3_f32 v27, v27, v88, v89\n\t"
    "v_max3_f32 v29, v29, v90, v91\n\t"
    "v_max3_f32 v27, v27, v60, v61\n\t"
    "v_max3_f32 v29, v29, v62, v63\n\t"
    "ds_read_b128 a[228:231], v9 offset:24576\n\t"
    "v_max3_f32 v27, v27, v92, v93\n\t"
    "v_max3_f32 v29, v29, v94, v95\n\t"
    "ds_read_b128 a[232:235], v10 offset:24576\n\t"
    "v_max_f32_e32 v30, v26, v28\n\t"
    "v_mov_b32_e32 v5, v30\n\t"
    "v_max_f32_e32 v31, v27, v29\n\t"
    "v_mov_b32_e32 v6, v31\n\t"
    "ds_read_b128 a[236:239], v11 offset:24576\n\t"
    "v_permlane32_swap_b32_e32 v30, v5\n\t"
    "v_max_f32_e32 v30, v30, v5\n\t"
    "v_permlane32_swap_b32_e32 v31, v6\n\t"
    "v_max_f32_e32 v31, v31, v6\n\t"
    "buffer_load_dwordx4 v14, s[24:27], s40 offen offset:3072 lds\n\t"
    "ds_read_b128 a[240:243], v8 offset:24704\n\t"
    "ds_read_b128 a[244:247], v9 offset:24704\n\t"
    "ds_read_b128 a[248:251], v10 offset:24704\n\t"
    "ds_read_b128 a[252:255], v11 offset:24704\n\t"
    "s_add_u32 m0, s43, 32768\n\t"
    "s_nop 0\n\t"
    "buffer_load_dwordx4 v15, s[28:31], s41 offen offset:0 lds\n\t"
    "v_mov_b32_e32 v24, v30\n\t"
    "v_sub_f32_e32 v32, v32, v30\n\t"
    "v_sub_f32_e32 v33, v33, v30\n\t"
    "v_sub_f32_e32 v34, v34, v30\n\t"
    "v_sub_f32_e32 v35, v35, v30\n\t"
    "v_sub_f32_e32 v36, v36, v30\n\t"
    "v_sub_f32_e32 v37, v37, v30\n\t"
    "v_sub_f32_e32 v38, v38, v30\n\t"
    "v_sub_f32_e32 v39, v39, v30\n\t"
    "v_sub_f32_e32 v40, v40, v30\n\t"
    "v_sub_f32_e32 v41, v41, v30\n\t"
    "v_sub_f32_e32 v42, v42, v30\n\t"
    "v_sub_f32_e32 v43, v43, v30\n\t"
    "v_sub_f32_e32 v44, v44, v30\n\t"
    "v_sub_f32_e32 v45, v45, v30\n\t"
    "v_sub_f32_e32 v46, v46, v30\n\t"
    "v_sub_f32_e32 v47, v47, v30\n\t"
    "v_sub_f32_e32 v64, v64, v30\n\t"
    "v_sub_f32_e32 v65, v65, v30\n\t"
    "v_sub_f32_e32 v66, v66, v30\n\t"
    "v_sub_f32_e32 v67, v67, v30\n\t"
    "v_sub_f32_e32 v68, v68, v30\n\t"
    "v_sub_f32_e32 v69, v69, v30\n\t"
    "v_sub_f32_e32 v70, v70, v30\n\t"
    "v_sub_f32_e32 v71, v71, v30\n\t"
    "v_sub_f32_e32 v72, v72, v30\n\t"
    "v_sub_f32_e32 v73, v73, v30\n\t"
    "v_sub_f32_e32 v74, v74, v30\n\t"
    "v_sub_f32_e32 v75, v75, v30\n\t"
    "v_sub_f32_e32 v76, v76, v30\n\t"
    "v_sub_f32_e32 v77, v77, v30\n\t"
    "v_sub_f32_e32 v78, v78, v30\n\t"
    "v_sub_f32_e32 v79, v79, v30\n\t"
    "v_sub_f32_e32 v160, 0, v30\n\t"
    "v_sub_f32_e32 v161, 0, v30\n\t"
    "v_sub_f32_e32 v162, 0, v30\n\t"
    "v_sub_f32_e32 v163, 0, v30\n\t"
    "v_sub_f32_e32 v164, 0, v30\n\t"
    "v_sub_f32_e32 v165, 0, v30\n\t"
    "v_sub_f32_e32 v166, 0, v30\n\t"
    "v_sub_f32_e32 v167, 0, v30\n\t"
    "v_sub_f32_e32 v168, 0, v30\n\t"
    "v_sub_f32_e32 v169, 0, v30\n\t"
    "v_sub_f32_e32 v170, 0, v30\n\t"
    "v_sub_f32_e32 v171, 0, v30\n\t"
    "v_sub_f32_e32 v172, 0, v30\n\t"
    "v_sub_f32_e32 v173, 0, v30\n\t"
    "v_sub_f32_e32 v174, 0, v30\n\t"
    "v_sub_f32_e32 v175, 0, v30\n\t"
    "v_mov_b32_e32 v25, v31\n\t"
    "v_sub_f32_e32 v48, v48, v31\n\t"
    "v_sub_f32_e32 v49, v49, v31\n\t"
    "v_sub_f32_e32 v50, v50, v31\n\t"
    "v_sub_f32_e32 v51, v51, v31\n\t"
    "v_sub_f32_e32 v52, v52, v31\n\t"
    "v_sub_f32_e32 v53, v53, v31\n\t"
    "v_sub_f32_e32 v54, v54, v31\n\t"
    "v_sub_f32_e32 v55, v55, v31\n\t"
    "v_sub_f32_e32 v56, v56, v31\n\t"
    "v_sub_f32_e32 v57, v57, v31\n\t"
    "v_sub_f32_e32 v58, v58, v31\n\t"
    "v_sub_f32_e32 v59, v59, v31\n\t"
    "v_sub_f32_e32 v60, v60, v31\n\t"
    "v_sub_f32_e32 v61, v61, v31\n\t"
    "v_sub_f32_e32 v62, v62, v31\n\t"
    "v_sub_f32_e32 v63, v63, v31\n\t"
    "v_sub_f32_e32 v80, v80, v31\n\t"
    "v_sub_f32_e32 v81, v81, v31\n\t"
    "v_sub_f32_e32 v82, v82, v31\n\t"
    "v_sub_f32_e32 v83, v83, v31\n\t"
    "v_sub_f32_e32 v84, v84, v31\n\t"
    "v_sub_f32_e32 v85, v85, v31\n\t"
    "v_sub_f32_e32 v86, v86, v31\n\t"
    "v_sub_f32_e32 v87, v87, v31\n\t"
    "v_sub_f32_e32 v88, v88, v31\n\t"
    "v_sub_f32_e32 v89, v89, v31\n\t"
    "v_sub_f32_e32 v90, v90, v31\n\t"
    "v_sub_f32_e32 v91, v91, v31\n\t"
    "v_sub_f32_e32 v92, v92, v31\n\t"
    "v_sub_f32_e32 v93, v93, v31\n\t"
    "v_sub_f32_e32 v94, v94, v31\n\t"
    "v_sub_f32_e32 v95, v95, v31\n\t"
    "v_sub_f32_e32 v176, 0, v31\n\t"
    "v_sub_f32_e32 v177, 0, v31\n\t"
    "v_sub_f32_e32 v178, 0, v31\n\t"
    "v_sub_f32_e32 v179, 0, v31\n\t"
    "v_sub_f32_e32 v180, 0, v31\n\t"
    "v_sub_f32_e32 v181, 0, v31\n\t"
    "v_sub_f32_e32 v182, 0, v31\n\t"
    "v_sub_f32_e32 v183, 0, v31\n\t"
    "v_sub_f32_e32 v184, 0, v31\n\t"
    "v_sub_f32_e32 v185, 0, v31\n\t"
    "v_sub_f32_e32 v186, 0, v31\n\t"
    "v_sub_f32_e32 v187, 0, v31\n\t"
    "v_sub_f32_e32 v188, 0, v31\n\t"
    "v_sub_f32_e32 v189, 0, v31\n\t"
    "v_sub_f32_e32 v190, 0, v31\n\t"
    "v_sub_f32_e32 v191, 0, v31\n\t"
    "v_exp_f32_e32 v32, v32\n\t"
    "v_exp_f32_e32 v33, v33\n\t"
    "v_add_f32_e32 v20, v20, v32\n\t"
    "v_add_f32_e32 v22, v22, v33\n\t"
    "v_exp_f32_e32 v34, v34\n\t"
    "v_exp_f32_e32 v35, v35\n\t"
    "buffer_load_dwordx4 v15, s[28:31], s41 offen offset:2048 lds\n\t"
    "v_add_f32_e32 v20, v20, v34\n\t"
    "v_add_f32_e32 v22, v22, v35\n\t"
    "v_exp_f32_e32 v36, v36\n\t"
    "s_nop 0\n\t"
    "v_add_f32_e32 v20, v20, v36\n\t"
    "v_exp_f32_e32 v37, v37\n\t"
    "v_exp_f32_e32 v38, v38\n\t"
    "v_add_f32_e32 v22, v22, v37\n\t"
    "v_add_f32_e32 v20, v20, v38\n\t"
    "v_exp_f32_e32 v39, v39\n\t"
    "v_exp_f32_e32 v40, v40\n\t"
    "v_add_f32_e32 v22, v22, v39\n\t"
    "v_add_f32_e32 v20, v20, v40\n\t"
    "v_exp_f32_e32 v41, v41\n\t"
    "v_exp_f32_e32 v42, v42\n\t"
    "s_add_u32 m0, s43, 33664\n\t"
    "s_nop 0\n\t"
    "buffer_load_dwordx4 v15, s[28:31], s41 offen offset:128 lds\n\t"
    "v_add_f32_e32 v22, v22, v41\n\t"
    "v_add_f32_e32 v20, v20, v42\n\t"
    "v_exp_f32_e32 v43, v43\n\t"
    "v_exp_f32_e32 v44, v44\n\t"
    "v_add_f32_e32 v22, v22, v43\n\t"
    "v_add_f32_e32 v20, v20, v44\n\t"
    "v_exp_f32_e32 v45, v45\n\t"
    "v_exp_f32_e32 v46, v46\n\t"
    "v_add_f32_e32 v22, v22, v45\n\t"
    "v_add_f32_e32 v20, v20, v46\n\t"
    "v_exp_f32_e32 v47, v47\n\t"
    "v_exp_f32_e32 v48, v48\n\t"
    "buffer_load_dwordx4 v15, s[28:31], s41 offen offset:2176 lds\n\t"
    "v_add_f32_e32 v22, v22, v47\n\t"
    "v_add_f32_e32 v21, v21, v48\n\t"
    "v_exp_f32_e32 v49, v49\n\t"
    "s_nop 0\n\t"
    "v_add_f32_e32 v23, v23, v49\n\t"
    "v_exp_f32_e32 v50, v50\n\t"
    "v_exp_f32_e32 v51, v51\n\t"
    "v_add_f32_e32 v21, v21, v50\n\t"
    "v_add_f32_e32 v23, v23, v51\n\t"
    "v_exp_f32_e32 v52, v52\n\t"
    "v_exp_f32_e32 v53, v53\n\t"
    "v_add_f32_e32 v21, v21, v52\n\t"
    "v_add_f32_e32 v23, v23, v53\n\t"
    "v_exp_f32_e32 v54, v54\n\t"
    "v_exp_f32_e32 v55, v55\n\t"
    "v_add_f32_e32 v21, v21, v54\n\t"
    "v_add_f32_e32 v23, v23, v55\n\t"
    "v_exp_f32_e32 v56, v56\n\t"
    "s_nop 0\n\t"
    "v_add_f32_e32 v21, v21, v56\n\t"
    "v_exp_f32_e32 v57, v57\n\t"
    "s_nop 0\n\t"
    "v_add_f32_e32 v23, v23, v57\n\t"
    "v_exp_f32_e32 v58, v58\n\t"
    "s_nop 0\n\t"
    "v_add_f32_e32 v21, v21, v58\n\t"
    "v_exp_f32_e32 v59, v59\n\t"
    "s_nop 0\n\t"
    "v_add_f32_e32 v23, v23, v59\n\t"
    "v_exp_f32_e32 v60, v60\n\t"
    "s_nop 0\n\t"
    "v_add_f32_e32 v21, v21, v60\n\t"
    "v_exp_f32_e32 v61, v61\n\t"
    "s_nop 0\n\t"
    "v_add_f32_e32 v23, v23, v61\n\t"
    "v_exp_f32_e32 v62, v62\n\t"
    "s_nop 0\n\t"
    "v_add_f32_e32 v21, v21, v62\n\t"
    "v_exp_f32_e32 v63, v63\n\t"
    "s_nop 0\n\t"
    "v_add_f32_e32 v23, v23, v63\n\t"
    "s_add_u32 s40, s40, 16384\n\t"
    "s_add_u32 s41, s41, 16384\n\t"
    "s_waitcnt lgkmcnt(0)\n\t"
    "v_mfma_f32_32x32x16_bf16 v[96:111], a[192:195], a[128:131], v[160:175]\n\t"
    "ds_read_b64_tr_b16 v[192:193], v12 offset:0\n\t"
    "v_exp_f32_e32 v64, v64\n\t"
    "v_exp_f32_e32 v65, v65\n\t"
    "v_cvt_pk_bf16_f32 v32, v32, v33\n\t"
    "v_mfma_f32_32x32x16_bf16 v[112:127], a[192:195], a[160:163], v[176:191]\n\t"
    "ds_read_b64_tr_b16 v[194:195], v12 offset:2048\n\t"
    "v_exp_f32_e32 v66, v66\n\t"
    "v_exp_f32_e32 v67, v67\n\t"
    "v_add_f32_e32 v20, v20, v64\n\t"
    "v_mfma_f32_32x32x16_bf16 v[128:143], a[224:227], a[128:131], v[160:175]\n\t"
    "ds_read_b64_tr_b16 v[208:209], v12 offset:512\n\t"
    "v_exp_f32_e32 v68, v68\n\t"
    "v_exp_f32_e32 v69, v69\n\t"
    "v_add_f32_e32 v22, v22, v65\n\t"
    "v_mfma_f32_32x32x16_bf16 v[144:159], a[224:227], a[160:163], v[176:191]\n\t"
    "ds_read_b64_tr_b16 v[210:211], v12 offset:2560\n\t"
    "v_exp_f32_e32 v70, v70\n\t"
    "v_exp_f32_e32 v71, v71\n\t"
    "v_cvt_pk_bf16_f32 v64, v64, v65\n\t"
    "v_mfma_f32_32x32x16_bf16 v[96:111], a[196:199], a[132:135], v[96:111]\n\t"
    "ds_read_b64_tr_b16 v[224:225], v12 offset:1024\n\t"
    "v_exp_f32_e32 v72, v72\n\t"
    "v_exp_f32_e32 v73, v73\n\t"
    "v_add_f32_e32 v20, v20, v66\n\t"
    "v_mfma_f32_32x32x16_bf16 v[112:127], a[196:199], a[164:167], v[112:127]\n\t"
    "ds_read_b64_tr_b16 v[226:227], v12 offset:3072\n\t"
    "v_exp_f32_e32 v74, v74\n\t"
    "v_exp_f32_e32 v75, v75\n\t"
    "v_add_f32_e32 v22, v22, v67\n\t"
    "v_mfma_f32_32x32x16_bf16 v[128:143], a[228:231], a[132:135], v[128:143]\n\t"
    "ds_read_b64_tr_b16 v[240:241], v12 offset:1536\n\t"
    "v_exp_f32_e32 v76, v76\n\t"
    "v_cvt_pk_bf16_f32 v65, v66, v67\n\t"
    "v_add_f32_e32 v20, v20, v68\n\t"
    "v_add_f32_e32 v22, v22, v69\n\t"
    "v_mfma_f32_32x32x16_bf16 v[144:159], a[228:231], a[164:167], v[144:159]\n\t"
    "ds_read_b64_tr_b16 v[242:243], v12 offset:3584\n\t"
    "v_exp_f32_e32 v77, v77\n\t"
    "v_cvt_pk_bf16_f32 v66, v68, v69\n\t"
    "v_add_f32_e32 v20, v20, v70\n\t"
    "v_add_f32_e32 v22, v22, v71\n\t"
    "v_mfma_f32_32x32x16_bf16 v[96:111], a[200:203], a[136:139], v[96:111]\n\t"
    "ds_read_b64_tr_b16 v[196:197], v12 offset:4096\n\t"
    "v_exp_f32_e32 v78, v78\n\t"
    "v_cvt_pk_bf16_f32 v67, v70, v71\n\t"
    "v_add_f32_e32 v20, v20, v72\n\t"
    "v_add_f32_e32 v22, v22, v73\n\t"
    "v_mfma_f32_32x32x16_bf16 v[112:127], a[200:203], a[168:171], v[112:127]\n\t"
    "ds_read_b64_tr_b16 v[198:199], v12 offset:6144\n\t"
    "v_exp_f32_e32 v79, v79\n\t"
    "v_cvt_pk_bf16_f32 v72, v72, v73\n\t"
    "v_add_f32_e32 v20, v20, v74\n\t"
    "v_add_f32_e32 v22, v22, v75\n\t"
    "v_mfma_f32_32x32x16_bf16 v[128:143], a[232:235], a[136:139], v[128:143]\n\t"
    "ds_read_b64_tr_b16 v[212:213], v12 offset:4608\n\t"
    "v_exp_f32_e32 v80, v80\n\t"
    "v_cvt_pk_bf16_f32 v73, v74, v75\n\t"
    "v_add_f32_e32 v20, v20, v76\n\t"
    "v_add_f32_e32 v22, v22, v77\n\t"
    "v_mfma_f32_32x32x16_bf16 v[144:159], a[232:235], a[168:171], v[144:159]\n\t"
    "ds_read_b64_tr_b16 v[214:215], v12 offset:6656\n\t"
    "v_exp_f32_e32 v81, v81\n\t"
    "v_cvt_pk_bf16_f32 v74, v76, v77\n\t"
    "v_add_f32_e32 v20, v20, v78\n\t"
    "v_add_f32_e32 v22, v22, v79\n\t"
    "v_mfma_f32_32x32x16_bf16 v[96:111], a[204:207], a[140:143], v[96:111]\n\t"
    "ds_read_b64_tr_b16 v[228:229], v12 offset:5120\n\t"
    "v_exp_f32_e32 v82, v82\n\t"
    "v_cvt_pk_bf16_f32 v75, v78, v79\n\t"
    "v_add_f32_e32 v21, v21, v80\n\t"
    "v_add_f32_e32 v23, v23, v81\n\t"
    "v_mfma_f32_32x32x16_bf16 v[112:127], a[204:207], a[172:175], v[112:127]\n\t"
    "ds_read_b64_tr_b16 v[230:231], v12 offset:7168\n\t"
    "v_exp_f32_e32 v83, v83\n\t"
    "v_cvt_pk_bf16_f32 v80, v80, v81\n\t"
    "v_add_f32_e32 v21, v21, v82\n\t"
    "v_cvt_pk_bf16_f32 v33, v34, v35\n\t"
    "v_mfma_f32_32x32x16_bf16 v[128:143], a[236:239], a[140:143], v[128:143]\n\t"
    "ds_read_b64_tr_b16 v[244:245], v12 offset:5632\n\t"
    "v_add_f32_e32 v23, v23, v83\n\t"
    "v_exp_f32_e32 v84, v84\n\t"
    "v_cvt_pk_bf16_f32 v81, v82, v83\n\t"
    "v_cvt_pk_bf16_f32 v34, v36, v37\n\t"
    "v_mfma_f32_32x32x16_bf16 v[144:159], a[236:239], a[172:175], v[144:159]\n\t"
    "ds_read_b64_tr_b16 v[246:247], v12 offset:7680\n\t"
    "v_add_f32_e32 v21, v21, v84\n\t"
    "v_exp_f32_e32 v85, v85\n\t"
    "v_exp_f32_e32 v86, v86\n\t"
    "v_mfma_f32_32x32x16_bf16 v[96:111], a[208:211], a[144:147], v[96:111]\n\t"
    "ds_read_b64_tr_b16 v[200:201], v12 offset:8192\n\t"
    "v_add_f32_e32 v23, v23, v85\n\t"
    "v_cvt_pk_bf16_f32 v82, v84, v85\n\t"
    "v_add_f32_e32 v21, v21, v86\n\t"
    "v_exp_f32_e32 v87, v87\n\t"
    "v_mfma_f32_32x32x16_bf16 v[112:127], a[208:211], a[176:179], v[112:127]\n\t"
    "ds_read_b64_tr_b16 v[202:203], v12 offset:10240\n\t"
    "v_add_f32_e32 v23, v23, v87\n\t"
    "v_cvt_pk_bf16_f32 v83, v86, v87\n\t"
    "v_exp_f32_e32 v88, v88\n\t"
    "v_cvt_pk_bf16_f32 v35, v38, v39\n\t"
    "v_mfma_f32_32x32x16_bf16 v[128:143], a[240:243], a[144:147], v[128:143]\n\t"
    "ds_read_b64_tr_b16 v[216:217], v12 offset:8704\n\t"
    "v_add_f32_e32 v21, v21, v88\n\t"
    "v_exp_f32_e32 v89, v89\n\t"
    "v_exp_f32_e32 v90, v90\n\t"
    "v_mfma_f32_32x32x16_bf16 v[144:159], a[240:243], a[176:179], v[144:159]\n\t"
    "ds_read_b64_tr_b16 v[218:219], v12 offset:10752\n\t"
    "v_add_f32_e32 v23, v23, v89\n\t"
    "v_cvt_pk_bf16_f32 v88, v88, v89\n\t"
    "v_add_f32_e32 v21, v21, v90\n\t"
    "v_exp_f32_e32 v91, v91\n\t"
    "v_mfma_f32_32x32x16_bf16 v[96:111], a[212:215], a[148:151], v[96:111]\n\t"
    "ds_read_b64_tr_b16 v[232:233], v12 offset:9216\n\t"
    "v_add_f32_e32 v23, v23, v91\n\t"
    "v_cvt_pk_bf16_f32 v89, v90, v91\n\t"
    "v_exp_f32_e32 v92, v92\n\t"
    "v_cvt_pk_bf16_f32 v40, v40, v41\n\t"
    "v_mfma_f32_32x32x16_bf16 v[112:127], a[212:215], a[180:183], v[112:127]\n\t"
    "ds_read_b64_tr_b16 v[234:235], v12 offset:11264\n\t"
    "v_add_f32_e32 v21, v21, v92\n\t"
    "v_exp_f32_e32 v93, v93\n\t"
    "v_exp_f32_e32 v94, v94\n\t"
    "v_mfma_f32_32x32x16_bf16 v[128:143], a[244:247], a[148:151], v[128:143]\n\t"
    "ds_read_b64_tr_b16 v[248:249], v12 offset:9728\n\t"
    "v_add_f32_e32 v23, v23, v93\n\t"
    "v_cvt_pk_bf16_f32 v90, v92, v93\n\t"
    "v_add_f32_e32 v21, v21, v94\n\t"
    "v_exp_f32_e32 v95, v95\n\t"
    "v_mfma_f32_32x32x16_bf16 v[144:159], a[244:247], a[180:183], v[144:159]\n\t"
    "ds_read_b64_tr_b16 v[250:251], v12 offset:11776\n\t"
    "v_add_f32_e32 v23, v23, v95\n\t"
    "v_cvt_pk_bf16_f32 v91, v94, v95\n\t"
    "v_cvt_pk_bf16_f32 v41, v42, v43\n\t"
    "v_cvt_pk_bf16_f32 v42, v44, v45\n\t"
    "v_cvt_pk_bf16_f32 v43, v46, v47\n\t"
    "v_mfma_f32_32x32x16_bf16 v[96:111], a[216:219], a[152:155], v[96:111]\n\t"
    "ds_read_b64_tr_b16 v[204:205], v12 offset:12288\n\t"
    "v_cvt_pk_bf16_f32 v48, v48, v49\n\t"
    "v_cvt_pk_bf16_f32 v49, v50, v51\n\t"
    "v_cvt_pk_bf16_f32 v50, v52, v53\n\t"
    "v_cvt_pk_bf16_f32 v51, v54, v55\n\t"
    "v_cvt_pk_bf16_f32 v56, v56, v57\n\t"
    "v_mfma_f32_32x32x16_bf16 v[112:127], a[216:219], a[184:187], v[112:127]\n\t"
    "ds_read_b64_tr_b16 v[206:207], v12 offset:14336\n\t"
    "v_cvt_pk_bf16_f32 v57, v58, v59\n\t"
    "v_cvt_pk_bf16_f32 v58, v60, v61\n\t"
    "v_cvt_pk_bf16_f32 v59, v62, v63\n\t"
    "v_mfma_f32_32x32x16_bf16 v[128:143], a[248:251], a[152:155], v[128:143]\n\t"
    "ds_read_b64_tr_b16 v[220:221], v12 offset:12800\n\t"
    "v_mfma_f32_32x32x16_bf16 v[144:159], a[248:251], a[184:187], v[144:159]\n\t"
    "ds_read_b64_tr_b16 v[222:223], v12 offset:14848\n\t"
    "v_mfma_f32_32x32x16_bf16 v[96:111], a[220:223], a[156:159], v[96:111]\n\t"
    "ds_read_b64_tr_b16 v[236:237], v12 offset:13312\n\t"
    "v_mfma_f32_32x32x16_bf16 v[112:127], a[220:223], a[188:191], v[112:127]\n\t"
    "ds_read_b64_tr_b16 v[238:239], v12 offset:15360\n\t"
    "v_mfma_f32_32x32x16_bf16 v[128:143], a[252:255], a[156:159], v[128:143]\n\t"
    "ds_read_b64_tr_b16 v[252:253], v12 offset:13824\n\t"
    "v_mfma_f32_32x32x16_bf16 v[144:159], a[252:255], a[188:191], v[144:159]\n\t"
    "ds_read_b64_tr_b16 v[254:255], v12 offset:15872\n\t"
    "s_waitcnt vmcnt(8) lgkmcnt(0)\n\t"
    "s_barrier\n\t"
    "v_mfma_f32_32x32x16_bf16 a[0:15], v[192:195], v[32:35], a[0:15]\n\t"
    "s_add_u32 m0, s42, 0\n\t"
    "s_nop 0\n\t"
    "buffer_load_dwordx4 v13, s[24:27], s40 offen offset:0 lds\n\t"
    "ds_read_b128 a[192:195], v8 offset:32768\n\t"
    "v_mfma_f32_32x32x16_bf16 a[16:31], v[192:195], v[48:51], a[16:31]\n\t"
    "ds_read_b128 a[196:199], v9 offset:32768\n\t"
    "v_mfma_f32_32x32x16_bf16 a[32:47], v[208:211], v[32:35], a[32:47]\n\t"
    "ds_read_b128 a[200:203], v10 offset:32768\n\t"
    "v_mfma_f32_32x32x16_bf16 a[48:63], v[208:211], v[48:51], a[48:63]\n\t"
    "ds_read_b128 a[204:207], v11 offset:32768\n\t"
    "v_max3_f32 v26, v96, v97, v128\n\t"
    "v_max3_f32 v28, v98, v99, v129\n\t"
    "v_max3_f32 v26, v26, v130, v131\n\t"
    "v_max3_f32 v26, v26, v100, v101\n\t"
    "v_max3_f32 v28, v28, v102, v103\n\t"
    "v_mfma_f32_32x32x16_bf16 a[64:79], v[224:227], v[32:35], a[64:79]\n\t"
    "buffer_load_dwordx4 v14, s[24:27], s40 offen offset:1024 lds\n\t"
    "ds_read_b128 a[208:211], v8 offset:32896\n\t"
    "v_max3_f32 v26, v26, v132, v133\n\t"
    "v_max3_f32 v28, v28, v134, v135\n\t"
    "v_max3_f32 v26, v26, v104, v105\n\t"
    "v_max3_f32 v28, v28, v106, v107\n\t"
    "v_mfma_f32_32x32x16_bf16 a[80:95], v[224:227], v[48:51], a[80:95]\n\t"
    "ds_read_b128 a[212:215], v9 offset:32896\n\t"
    "v_max3_f32 v26, v26, v136, v137\n\t"
    "v_max3_f32 v28, v28, v138, v139\n\t"
    "v_max3_f32 v26, v26, v108, v109\n\t"
    "v_max3_f32 v28, v28, v110, v111\n\t"
    "v_max3_f32 v26, v26, v140, v141\n\t"
    "v_mfma_f32_32x32x16_bf16 a[96:111], v[240:243], v[32:35], a[96:111]\n\t"
    "ds_read_b128 a[216:219], v10 offset:32896\n\t"
    "v_max3_f32 v28, v28, v142, v143\n\t"
    "v_max3_f32 v27, v112, v113, v144\n\t"
    "v_max3_f32 v29, v114, v115, v145\n\t"
    "v_max3_f32 v27, v27, v146, v147\n\t"
    "v_max3_f32 v27, v27, v116, v117\n\t"
    "v_mfma_f32_32x32x16_bf16 a[112:127], v[240:243], v[48:51], a[112:127]\n\t"
    "ds_read_b128 a[220:223], v11 offset:32896\n\t"
    "v_max3_f32 v29, v29, v118, v119\n\t"
    "v_max3_f32 v27, v27, v148, v149\n\t"
    "v_max3_f32 v29, v29, v150, v151\n\t"
    "v_max3_f32 v27, v27, v120, v121\n\t"
    "v_max3_f32 v29, v29, v122, v123\n\t"
    "v_mfma_f32_32x32x16_bf16 a[0:15], v[196:199], v[40:43], a[0:15]\n\t"
    "buffer_load_dwordx4 v13, s[24:27], s40 offen offset:2048 lds\n\t"
    "ds_read_b128 a[224:227], v8 offset:40960\n\t"
    "v_max3_f32 v27, v27, v152, v153\n\t"
    "v_max3_f32 v29, v29, v154, v155\n\t"
    "v_max3_f32 v27, v27, v124, v125\n\t"
    "v_max3_f32 v29, v29, v126, v127\n\t"
    "v_mfma_f32_32x32x16_bf16 a[16:31], v[196:199], v[56:59], a[16:31]\n\t"
    "ds_read_b128 a[228:231], v9 offset:40960\n\t"
    "v_max3_f32 v27, v27, v156, v157\n\t"
    "v_max3_f32 v29, v29, v158, v159\n\t"
    "v_mfma_f32_32x32x16_bf16 a[32:47], v[212:215], v[40:43], a[32:47]\n\t"
    "ds_read_b128 a[232:235], v10 offset:40960\n\t"
    "v_max_f32_e32 v30, v26, v28\n\t"
    "v_mov_b32_e32 v5, v30\n\t"
    "v_max_f32_e32 v31, v27, v29\n\t"
    "v_mov_b32_e32 v6, v31\n\t"
    "v_mfma_f32_32x32x16_bf16 a[48:63], v[212:215], v[56:59], a[48:63]\n\t"
    "ds_read_b128 a[236:239], v11 offset:40960\n\t"
    "v_permlane32_swap_b32_e32 v30, v5\n\t"
    "v_max_f32_e32 v30, v30, v5\n\t"
    "v_permlane32_swap_b32_e32 v31, v6\n\t"
    "v_max_f32_e32 v31, v31, v6\n\t"
    "v_mfma_f32_32x32x16_bf16 a[64:79], v[228:231], v[40:43], a[64:79]\n\t"
    "buffer_load_dwordx4 v14, s[24:27], s40 offen offset:3072 lds\n\t"
    "ds_read_b128 a[240:243], v8 offset:41088\n\t"
    "v_max_f32_e32 v7, v30, v31\n\t"
    "v_cmp_lt_f32_e32 vcc, s45, v7\n\t"
    "s_cbranch_vccnz L_RARE_p1\n\t"
    "L_RET_p1:\n\t"
    "v_mfma_f32_32x32x16_bf16 a[80:95], v[228:231], v[56:59], a[80:95]\n\t"
    "ds_read_b128 a[244:247], v9 offset:41088\n\t"
    "v_exp_f32_e32 v96, v96\n\t"
    "v_exp_f32_e32 v97, v97\n\t"
    "v_mfma_f32_32x32x16_bf16 a[96:111], v[244:247], v[40:43], a[96:111]\n\t"
    "ds_read_b128 a[248:251], v10 offset:41088\n\t"
    "v_add_f32_e32 v20, v20, v96\n\t"
    "v_add_f32_e32 v22, v22, v97\n\t"
    "v_exp_f32_e32 v98, v98\n\t"
    "v_mfma_f32_32x32x16_bf16 a[112:127], v[244:247], v[56:59], a[112:127]\n\t"
    "ds_read_b128 a[252:255], v11 offset:41088\n\t"
    "v_add_f32_e32 v20, v20, v98\n\t"
    "v_exp_f32_e32 v99, v99\n\t"
    "v_exp_f32_e32 v100, v100\n\t"
    "v_mfma_f32_32x32x16_bf16 a[0:15], v[200:203], v[64:67], a[0:15]\n\t"
    "s_add_u32 m0, s43, 49152\n\t"
    "s_nop 0\n\t"
    "buffer_load_dwordx4 v15, s[28:31], s41 offen offset:0 lds\n\t"
    "v_add_f32_e32 v22, v22, v99\n\t"
    "v_add_f32_e32 v20, v20, v100\n\t"
    "v_mfma_f32_32x32x16_bf16 a[16:31], v[200:203], v[80:83], a[16:31]\n\t"
    "v_exp_f32_e32 v101, v101\n\t"
    "v_exp_f32_e32 v102, v102\n\t"
    "v_mfma_f32_32x32x16_bf16 a[32:47], v[216:219], v[64:67], a[32:47]\n\t"
    "v_add_f32_e32 v22, v22, v101\n\t"
    "v_add_f32_e32 v20, v20, v102\n\t"
    "v_exp_f32_e32 v103, v103\n\t"
    "v_exp_f32_e32 v104, v104\n\t"
    "v_mfma_f32_32x32x16_bf16 a[48:63], v[216:219], v[80:83], a[48:63]\n\t"
    "v_add_f32_e32 v22, v22, v103\n\t"
    "v_add_f32_e32 v20, v20, v104\n\t"
    "v_exp_f32_e32 v105, v105\n\t"
    "v_exp_f32_e32 v106, v106\n\t"
    "v_mfma_f32_32x32x16_bf16 a[64:79], v[232:235], v[64:67], a[64:79]\n\t"
    "buffer_load_dwordx4 v15, s[28:31], s41 offen offset:2048 lds\n\t"
    "v_add_f32_e32 v22, v22, v105\n\t"
    "v_add_f32_e32 v20, v20, v106\n\t"
    "v_exp_f32_e32 v107, v107\n\t"
    "v_mfma_f32_32x32x16_bf16 a[80:95], v[232:235], v[80:83], a[80:95]\n\t"
    "v_add_f32_e32 v22, v22, v107\n\t"
    "v_exp_f32_e32 v108, v108\n\t"
    "v_exp_f32_e32 v109, v109\n\t"
    "v_mfma_f32_32x32x16_bf16 a[96:111], v[248:251], v[64:67], a[96:111]\n\t"
    "v_add_f32_e32 v20, v20, v108\n\t"
    "v_add_f32_e32 v22, v22, v109\n\t"
    "v_exp_f32_e32 v110, v110\n\t"
    "v_exp_f32_e32 v111, v111\n\t"
    "v_mfma_f32_32x32x16_bf16 a[112:127], v[248:251], v[80:83], a[112:127]\n\t"
    "v_add_f32_e32 v20, v20, v110\n\t"
    "v_add_f32_e32 v22, v22, v111\n\t"
    "v_exp_f32_e32 v112, v112\n\t"
    "v_exp_f32_e32 v113, v113\n\t"
    "v_mfma_f32_32x32x16_bf16 a[0:15], v[204:207], v[72:75], a[0:15]\n\t"
    "s_add_u32 m0, s43, 50048\n\t"
    "s_nop 0\n\t"
    "buffer_load_dwordx4 v15, s[28:31], s41 offen offset:128 lds\n\t"
    "v_add_f32_e32 v21, v21, v112\n\t"
    "v_add_f32_e32 v23, v23, v113\n\t"
    "v_mfma_f32_32x32x16_bf16 a[16:31], v[204:207], v[88:91], a[16:31]\n\t"
    "v_exp_f32_e32 v114, v114\n\t"
    "v_exp_f32_e32 v115, v115\n\t"
    "v_mfma_f32_32x32x16_bf16 a[32:47], v[220:223], v[72:75], a[32:47]\n\t"
    "v_add_f32_e32 v21, v21, v114\n\t"
    "v_add_f32_e32 v23, v23, v115\n\t"
    "v_exp_f32_e32 v116, v116\n\t"
    "v_exp_f32_e32 v117, v117\n\t"
    "v_mfma_f32_32x32x16_bf16 a[48:63], v[220:223], v[88:91], a[48:63]\n\t"
    "v_add_f32_e32 v21, v21, v116\n\t"
    "v_add_f32_e32 v23, v23, v117\n\t"
    "v_exp_f32_e32 v118, v118\n\t"
    "v_exp_f32_e32 v119, v119\n\t"
    "v_mfma_f32_32x32x16_bf16 a[64:79], v[236:239], v[72:75], a[64:79]\n\t"
    "buffer_load_dwordx4 v15, s[28:31], s41 offen offset:2176 lds\n\t"
    "v_add_f32_e32 v21, v21, v118\n\t"
    "v_add_f32_e32 v23, v23, v119\n\t"
    "v_exp_f32_e32 v120, v120\n\t"
    "v_mfma_f32_32x32x16_bf16 a[80:95], v[236:239], v[88:91], a[80:95]\n\t"
    "v_add_f32_e32 v21, v21, v120\n\t"
    "v_exp_f32_e32 v121, v121\n\t"
    "v_exp_f32_e32 v122, v122\n\t"
    "v_mfma_f32_32x32x16_bf16 a[96:111], v[252:255], v[72:75], a[96:111]\n\t"
    "v_add_f32_e32 v23, v23, v121\n\t"
    "v_add_f32_e32 v21, v21, v122\n\t"
    "v_exp_f32_e32 v123, v123\n\t"
    "v_exp_f32_e32 v124, v124\n\t"
    "v_mfma_f32_32x32x16_bf16 a[112:127], v[252:255], v[88:91], a[112:127]\n\t"
    "v_add_f32_e32 v23, v23, v123\n\t"
    "v_add_f32_e32 v21, v21, v124\n\t"
    "v_exp_f32_e32 v125, v125\n\t"
    "v_exp_f32_e32 v126, v126\n\t"
    "v_add_f32_e32 v23, v23, v125\n\t"
    "v_add_f32_e32 v21, v21, v126\n\t"
    "v_exp_f32_e32 v127, v127\n\t"
    "s_nop 0\n\t"
    "v_add_f32_e32 v23, v23, v127\n\t"
    "s_add_u32 s40, s40, 16384\n\t"
    "s_add_u32 s41, s41, 16384\n\t"
    "s_cmp_lg_u32 s47, 0\n\t"
    "s_cbranch_scc1 L_RESC_p1\n\t"
    "L_RRET_p1:\n\t"
    "s_waitcnt lgkmcnt(0)\n\t"
    "v_mfma_f32_32x32x16_bf16 v[32:47], a[192:195], a[128:131], v[160:175]\n\t"
    "ds_read_b64_tr_b16 v[192:193], v12 offset:16384\n\t"
    "v_exp_f32_e32 v128, v128\n\t"
    "v_exp_f32_e32 v129, v129\n\t"
    "v_cvt_pk_bf16_f32 v96, v96, v97\n\t"
    "v_mfma_f32_32x32x16_bf16 v[48:63], a[192:195], a[160:163], v[176:191]\n\t"
    "ds_read_b64_tr_b16 v[194:195], v12 offset:18432\n\t"
    "v_exp_f32_e32 v130, v130\n\t"
    "v_exp_f32_e32 v131, v131\n\t"
    "v_add_f32_e32 v20, v20, v128\n\t"
    "v_mfma_f32_32x32x16_bf16 v[64:79], a[224:227], a[128:131], v[160:175]\n\t"
    "ds_read_b64_tr_b16 v[208:209], v12 offset:16896\n\t"
    "v_exp_f32_e32 v132, v132\n\t"
    "v_exp_f32_e32 v133, v133\n\t"
    "v_add_f32_e32 v22, v22, v129\n\t"
    "v_mfma_f32_32x32x16_bf16 v[80:95], a[224:227], a[160:163], v[176:191]\n\t"
    "ds_read_b64_tr_b16 v[210:211], v12 offset:18944\n\t"
    "v_exp_f32_e32 v134, v134\n\t"
    "v_exp_f32_e32 v135, v135\n\t"
    "v_cvt_pk_bf16_f32 v128, v128, v129\n\t"
    "v_mfma_f32_32x32x16_bf16 v[32:47], a[196:199], a[132:135], v[32:47]\n\t"
    "ds_read_b64_tr_b16 v[224:225], v12 offset:17408\n\t"
    "v_exp_f32_e32 v136, v136\n\t"
    "v_exp_f32_e32 v137, v137\n\t"
    "v_add_f32_e32 v20, v20, v130\n\t"
    "v_mfma_f32_32x32x16_bf16 v[48:63], a[196:199], a[164:167], v[48:63]\n\t"
    "ds_read_b64_tr_b16 v[226:227], v12 offset:19456\n\t"
    "v_exp_f32_e32 v138, v138\n\t"
    "v_exp_f32_e32 v139, v139\n\t"
    "v_add_f32_e32 v22, v22, v131\n\t"
    "v_mfma_f32_32x32x16_bf16 v[64:79], a[228:231], a[132:135], v[64:79]\n\t"
    "ds_read_b64_tr_b16 v[240:241], v12 offset:17920\n\t"
    "v_exp_f32_e32 v140, v140\n\t"
    "v_cvt_pk_bf16_f32 v129, v130, v131\n\t"
    "v_add_f32_e32 v20, v20, v132\n\t"
    "v_add_f32_e32 v22, v22, v133\n\t"
    "v_mfma_f32_32x32x16_bf16 v[80:95], a[228:231], a[164:167], v[80:95]\n\t"
    "ds_read_b64_tr_b16 v[242:243], v12 offset:19968\n\t"
    "v_exp_f32_e32 v141, v141\n\t"
    "v_cvt_pk_bf16_f32 v130, v132, v133\n\t"
    "v_add_f32_e32 v20, v20, v134\n\t"
    "v_add_f32_e32 v22, v22, v135\n\t"
    "v_mfma_f32_32x32x16_bf16 v[32:47], a[200:203], a[136:139], v[32:47]\n\t"
    "ds_read_b64_tr_b16 v[196:197], v12 offset:20480\n\t"
    "v_exp_f32_e32 v142, v142\n\t"
    "v_cvt_pk_bf16_f32 v131, v134, v135\n\t"
    "v_add_f32_e32 v20, v20, v136\n\t"
    "v_add_f32_e32 v22, v22, v137\n\t"
    "v_mfma_f32_32x32x16_bf16 v[48:63], a[200:203], a[168:171], v[48:63]\n\t"
    "ds_read_b64_tr_b16 v[198:199], v12 offset:22528\n\t"
    "v_exp_f32_e32 v143, v143\n\t"
    "v_cvt_pk_bf16_f32 v136, v136, v137\n\t"
    "v_add_f32_e32 v20, v20, v138\n\t"
    "v_add_f32_e32 v22, v22, v139\n\t"
    "v_mfma_f32_32x32x16_bf16 v[64:79], a[232:235], a[136:139], v[64:79]\n\t"
    "ds_read_b64_tr_b16 v[212:213], v12 offset:20992\n\t"
    "v_exp_f32_e32 v144, v144\n\t"
    "v_cvt_pk_bf16_f32 v137, v138, v139\n\t"
    "v_add_f32_e32 v20, v20, v140\n\t"
    "v_add_f32_e32 v22, v22, v141\n\t"
    "v_mfma_f32_32x32x16_bf16 v[80:95], a[232:235], a[168:171], v[80:95]\n\t"
    "ds_read_b64_tr_b16 v[214:215], v12 offset:23040\n\t"
    "v_exp_f32_e32 v145, v145\n\t"
    "v_cvt_pk_bf16_f32 v138, v140, v141\n\t"
    "v_add_f32_e32 v20, v20, v142\n\t"
    "v_add_f32_e32 v22, v22, v143\n\t"
    "v_mfma_f32_32x32x16_bf16 v[32:47], a[204:207], a[140:143], v[32:47]\n\t"
    "ds_read_b64_tr_b16 v[228:229], v12 offset:21504\n\t"
    "v_exp_f32_e32 v146, v146\n\t"
    "v_cvt_pk_bf16_f32 v139, v142, v143\n\t"
    "v_add_f32_e32 v21, v21, v144\n\t"
    "v_add_f32_e32 v23, v23, v145\n\t"
    "v_mfma_f32_32x32x16_bf16 v[48:63], a[204:207], a[172:175], v[48:63]\n\t"
    "ds_read_b64_tr_b16 v[230:231], v12 offset:23552\n\t"
    "v_exp_f32_e32 v147, v147\n\t"
    "v_cvt_pk_bf16_f32 v144, v144, v145\n\t"
    "v_add_f32_e32 v21, v21, v146\n\t"
    "v_cvt_pk_bf16_f32 v97, v98, v99\n\t"
    "v_mfma_f32_32x32x16_bf16 v[64:79], a[236:239], a[140:143], v[64:79]\n\t"
    "ds_read_b64_tr_b16 v[244:245], v12 offset:22016\n\t"
    "v_add_f32_e32 v23, v23, v147\n\t"
    "v_exp_f32_e32 v148, v148\n\t"
    "v_cvt_pk_bf16_f32 v145, v146, v147\n\t"
    "v_cvt_pk_bf16_f32 v98, v100, v101\n\t"
    "v_mfma_f32_32x32x16_bf16 v[80:95], a[236:239], a[172:175], v[80:95]\n\t"
    "ds_read_b64_tr_b16 v[246:247], v12 offset:24064\n\t"
    "v_add_f32_e32 v21, v21, v148\n\t"
    "v_exp_f32_e32 v149, v149\n\t"
    "v_exp_f32_e32 v150, v150\n\t"
    "v_mfma_f32_32x32x16_bf16 v[32:47], a[208:211], a[144:147], v[32:47]\n\t"
    "ds_read_b64_tr_b16 v[200:201], v12 offset:24576\n\t"
    "v_add_f32_e32 v23, v23, v149\n\t"
    "v_cvt_pk_bf16_f32 v146, v148, v149\n\t"
    "v_add_f32_e32 v21, v21, v150\n\t"
    "v_exp_f32_e32 v151, v151\n\t"
    "v_mfma_f32_32x32x16_bf16 v[48:63], a[208:211], a[176:179], v[48:63]\n\t"
    "ds_read_b64_tr_b16 v[202:203], v12 offset:26624\n\t"
    "v_add_f32_e32 v23, v23, v151\n\t"
    "v_cvt_pk_bf16_f32 v147, v150, v151\n\t"
    "v_exp_f32_e32 v152, v152\n\t"
    "v_cvt_pk_bf16_f32 v99, v102, v103\n\t"
    "v_mfma_f32_32x32x16_bf16 v[64:79], a[240:243], a[144:147], v[64:79]\n\t"
    "ds_read_b64_tr_b16 v[216:217], v12 offset:25088\n\t"
    "v_add_f32_e32 v21, v21, v152\n\t"
    "v_exp_f32_e32 v153, v153\n\t"
    "v_exp_f32_e32 v154, v154\n\t"
    "v_mfma_f32_32x32x16_bf16 v[80:95], a[240:243], a[176:179], v[80:95]\n\t"
    "ds_read_b64_tr_b16 v[218:219], v12 offset:27136\n\t"
    "v_add_f32_e32 v23, v23, v153\n\t"
    "v_cvt_pk_bf16_f32 v152, v152, v153\n\t"
    "v_add_f32_e32 v21, v21, v154\n\t"
    "v_exp_f32_e32 v155, v155\n\t"
    "v_mfma_f32_32x32x16_bf16 v[32:47], a[212:215], a[148:151], v[32:47]\n\t"
    "ds_read_b64_tr_b16 v[232:233], v12 offset:25600\n\t"
    "v_add_f32_e32 v23, v23, v155\n\t"
    "v_cvt_pk_bf16_f32 v153, v154, v155\n\t"
    "v_exp_f32_e32 v156, v156\n\t"
    "v_cvt_pk_bf16_f32 v104, v104, v105\n\t"
    "v_mfma_f32_32x32x16_bf16 v[48:63], a[212:215], a[180:183], v[48:63]\n\t"
    "ds_read_b64_tr_b16 v[234:235], v12 offset:27648\n\t"
    "v_add_f32_e32 v21, v21, v156\n\t"
    "v_exp_f32_e32 v157, v157\n\t"
    "v_exp_f32_e32 v158, v158\n\t"
    "v_mfma_f32_32x32x16_bf16 v[64:79], a[244:247], a[148:151], v[64:79]\n\t"
    "ds_read_b64_tr_b16 v[248:249], v12 offset:26112\n\t"
    "v_add_f32_e32 v23, v23, v157\n\t"
    "v_cvt_pk_bf16_f32 v154, v156, v157\n\t"
    "v_add_f32_e32 v21, v21, v158\n\t"
    "v_exp_f32_e32 v159, v159\n\t"
    "v_mfma_f32_32x32x16_bf16 v[80:95], a[244:247], a[180:183], v[80:95]\n\t"
    "ds_read_b64_tr_b16 v[250:251], v12 offset:28160\n\t"
    "v_add_f32_e32 v23, v23, v159\n\t"
    "v_cvt_pk_bf16_f32 v155, v158, v159\n\t"
    "v_cvt_pk_bf16_f32 v105, v106, v107\n\t"
    "v_cvt_pk_bf16_f32 v106, v108, v109\n\t"
    "v_cvt_pk_bf16_f32 v107, v110, v111\n\t"
    "v_mfma_f32_32x32x16_bf16 v[32:47], a[216:219], a[152:155], v[32:47]\n\t"
    "ds_read_b64_tr_b16 v[204:205], v12 offset:28672\n\t"
    "v_cvt_pk_bf16_f32 v112, v112, v113\n\t"
    "v_cvt_pk_bf16_f32 v113, v114, v115\n\t"
    "v_cvt_pk_bf16_f32 v114, v116, v117\n\t"
    "v_cvt_pk_bf16_f32 v115, v118, v119\n\t"
    "v_cvt_pk_bf16_f32 v120, v120, v121\n\t"
    "v_mfma_f32_32x32x16_bf16 v[48:63], a[216:219], a[184:187], v[48:63]\n\t"
    "ds_read_b64_tr_b16 v[206:207], v12 offset:30720\n\t"
    "v_cvt_pk_bf16_f32 v121, v122, v123\n\t"
    "v_cvt_pk_bf16_f32 v122, v124, v125\n\t"
    "v_cvt_pk_bf16_f32 v123, v126, v127\n\t"
    "v_mfma_f32_32x32x16_bf16 v[64:79], a[248:251], a[152:155], v[64:79]\n\t"
    "ds_read_b64_tr_b16 v[220:221], v12 offset:29184\n\t"
    "v_mfma_f32_32x32x16_bf16 v[80:95], a[248:251], a[184:187], v[80:95]\n\t"
    "ds_read_b64_tr_b16 v[222:223], v12 offset:31232\n\t"
    "v_mfma_f32_32x32x16_bf16 v[32:47], a[220:223], a[156:159], v[32:47]\n\t"
    "ds_read_b64_tr_b16 v[236:237], v12 offset:29696\n\t"
    "v_mfma_f32_32x32x16_bf16 v[48:63], a[220:223], a[188:191], v[48:63]\n\t"
    "ds_read_b64_tr_b16 v[238:239], v12 offset:31744\n\t"
    "v_mfma_f32_32x32x16_bf16 v[64:79], a[252:255], a[156:159], v[64:79]\n\t"
    "ds_read_b64_tr_b16 v[252:253], v12 offset:30208\n\t"
    "v_mfma_f32_32x32x16_bf16 v[80:95], a[252:255], a[188:191], v[80:95]\n\t"
    "ds_read_b64_tr_b16 v[254:255], v12 offset:32256\n\t"
    "s_waitcnt vmcnt(8) lgkmcnt(0)\n\t"
    "s_barrier\n\t"
    "v_mfma_f32_32x32x16_bf16 a[0:15], v[192:195], v[96:99], a[0:15]\n\t"
    "s_add_u32 m0, s42, 16384\n\t"
    "s_nop 0\n\t"
    "buffer_load_dwordx4 v13, s[24:27], s40 offen offset:0 lds\n\t"
    "ds_read_b128 a[192:195], v8 offset:49152\n\t"
    "v_mfma_f32_32x32x16_bf16 a[16:31], v[192:195], v[112:115], a[16:31]\n\t"
    "ds_read_b128 a[196:199], v9 offset:49152\n\t"
    "v_mfma_f32_32x32x16_bf16 a[32:47], v[208:211], v[96:99], a[32:47]\n\t"
    "ds_read_b128 a[200:203], v10 offset:49152\n\t"
    "v_mfma_f32_32x32x16_bf16 a[48:63], v[208:211], v[112:115], a[48:63]\n\t"
    "ds_read_b128 a[204:207], v11 offset:49152\n\t"
    "v_max3_f32 v26, v32, v33, v64\n\t"
    "v_max3_f32 v28, v34, v35, v65\n\t"
    "v_max3_f32 v26, v26, v66, v67\n\t"
    "v_max3_f32 v26, v26, v36, v37\n\t"
    "v_max3_f32 v28, v28, v38, v39\n\t"
    "v_mfma_f32_32x32x16_bf16 a[64:79], v[224:227], v[96:99], a[64:79]\n\t"
    "buffer_load_dwordx4 v14, s[24:27], s40 offen offset:1024 lds\n\t"
    "ds_read_b128 a[208:211], v8 offset:49280\n\t"
    "v_max3_f32 v26, v26, v68, v69\n\t"
    "v_max3_f32 v28, v28, v70, v71\n\t"
    "v_max3_f32 v26, v26, v40, v41\n\t"
    "v_max3_f32 v28, v28, v42, v43\n\t"
    "v_mfma_f32_32x32x16_bf16 a[80:95], v[224:227], v[112:115], a[80:95]\n\t"
    "ds_read_b128 a[212:215], v9 offset:49280\n\t"
    "v_max3_f32 v26, v26, v72, v73\n\t"
    "v_max3_f32 v28, v28, v74, v75\n\t"
    "v_max3_f32 v26, v26, v44, v45\n\t"
    "v_max3_f32 v28, v28, v46, v47\n\t"
    "v_max3_f32 v26, v26, v76, v77\n\t"
    "v_mfma_f32_32x32x16_bf16 a[96:111], v[240:243], v[96:99], a[96:111]\n\t"
    "ds_read_b128 a[216:219], v10 offset:49280\n\t"
    "v_max3_f32 v28, v28, v78, v79\n\t"
    "v_max3_f32 v27, v48, v49, v80\n\t"
    "v_max3_f32 v29, v50, v51, v81\n\t"
    "v_max3_f32 v27, v27, v82, v83\n\t"
    "v_max3_f32 v27, v27, v52, v53\n\t"
    "v_mfma_f32_32x32x16_bf16 a[112:127], v[240:243], v[112:115], a[112:127]\n\t"
    "ds_read_b128 a[220:223], v11 offset:49280\n\t"
    "v_max3_f32 v29, v29, v54, v55\n\t"
    "v_max3_f32 v27, v27, v84, v85\n\t"
    "v_max3_f32 v29, v29, v86, v87\n\t"
    "v_max3_f32 v27, v27, v56, v57\n\t"
    "v_max3_f32 v29, v29, v58, v59\n\t"
    "v_mfma_f32_32x32x16_bf16 a[0:15], v[196:199], v[104:107], a[0:15]\n\t"
    "buffer_load_dwordx4 v13, s[24:27], s40 offen offset:2048 lds\n\t"
    "ds_read_b128 a[224:227], v8 offset:57344\n\t"
    "v_max3_f32 v27, v27, v88, v89\n\t"
    "v_max3_f32 v29, v29, v90, v91\n\t"
    "v_max3_f32 v27, v27, v60, v61\n\t"
    "v_max3_f32 v29, v29, v62, v63\n\t"
    "v_mfma_f32_32x32x16_bf16 a[16:31], v[196:199], v[120:123], a[16:31]\n\t"
    "ds_read_b128 a[228:231], v9 offset:57344\n\t"
    "v_max3_f32 v27, v27, v92, v93\n\t"
    "v_max3_f32 v29, v29, v94, v95\n\t"
    "v_mfma_f32_32x32x16_bf16 a[32:47], v[212:215], v[104:107], a[32:47]\n\t"
    "ds_read_b128 a[232:235], v10 offset:57344\n\t"
    "v_max_f32_e32 v30, v26, v28\n\t"
    "v_mov_b32_e32 v5, v30\n\t"
    "v_max_f32_e32 v31, v27, v29\n\t"
    "v_mov_b32_e32 v6, v31\n\t"
    "v_mfma_f32_32x32x16_bf16 a[48:63], v[212:215], v[120:123], a[48:63]\n\t"
    "ds_read_b128 a[236:239], v11 offset:57344\n\t"
    "v_permlane32_swap_b32_e32 v30, v5\n\t"
    "v_max_f32_e32 v30, v30, v5\n\t"
    "v_permlane32_swap_b32_e32 v31, v6\n\t"
    "v_max_f32_e32 v31, v31, v6\n\t"
    "v_mfma_f32_32x32x16_bf16 a[64:79], v[228:231], v[104:107], a[64:79]\n\t"
    "buffer_load_dwordx4 v14, s[24:27], s40 offen offset:3072 lds\n\t"
    "ds_read_b128 a[240:243], v8 offset:57472\n\t"
    "v_max_f32_e32 v7, v30, v31\n\t"
    "v_cmp_lt_f32_e32 vcc, s45, v7\n\t"
    "s_cbranch_vccnz L_RARE_p2\n\t"
    "L_RET_p2:\n\t"
    "v_mfma_f32_32x32x16_bf16 a[80:95], v[228:231], v[120:123], a[80:95]\n\t"
    "ds_read_b128 a[244:247], v9 offset:57472\n\t"
    "v_exp_f32_e32 v32, v32\n\t"
    "v_exp_f32_e32 v33, v33\n\t"
    "v_mfma_f32_32x32x16_bf16 a[96:111], v[244:247], v[104:107], a[96:111]\n\t"
    "ds_read_b128 a[248:251], v10 offset:57472\n\t"
    "v_add_f32_e32 v20, v20, v32\n\t"
    "v_add_f32_e32 v22, v22, v33\n\t"
    "v_exp_f32_e32 v34, v34\n\t"
    "v_mfma_f32_32x32x16_bf16 a[112:127], v[244:247], v[120:123], a[112:127]\n\t"
    "ds_read_b128 a[252:255], v11 offset:57472\n\t"
    "v_add_f32_e32 v20, v20, v34\n\t"
    "v_exp_f32_e32 v35, v35\n\t"
    "v_exp_f32_e32 v36, v36\n\t"
    "v_mfma_f32_32x32x16_bf16 a[0:15], v[200:203], v[128:131], a[0:15]\n\t"
    "s_add_u32 m0, s43, 0\n\t"
    "s_nop 0\n\t"
    "buffer_load_dwordx4 v15, s[28:31], s41 offen offset:0 lds\n\t"
    "v_add_f32_e32 v22, v22, v35\n\t"
    "v_add_f32_e32 v20, v20, v36\n\t"
    "v_mfma_f32_32x32x16_bf16 a[16:31], v[200:203], v[144:147], a[16:31]\n\t"
    "v_exp_f32_e32 v37, v37\n\t"
    "v_exp_f32_e32 v38, v38\n\t"
    "v_mfma_f32_32x32x16_bf16 a[32:47], v[216:219], v[128:131], a[32:47]\n\t"
    "v_add_f32_e32 v22, v22, v37\n\t"
    "v_add_f32_e32 v20, v20, v38\n\t"
    "v_exp_f32_e32 v39, v39\n\t"
    "v_exp_f32_e32 v40, v40\n\t"
    "v_mfma_f32_32x32x16_bf16 a[48:63], v[216:219], v[144:147], a[48:63]\n\t"
    "v_add_f32_e32 v22, v22, v39\n\t"
    "v_add_f32_e32 v20, v20, v40\n\t"
    "v_exp_f32_e32 v41, v41\n\t"
    "v_exp_f32_e32 v42, v42\n\t"
    "v_mfma_f32_32x32x16_bf16 a[64:79], v[232:235], v[128:131], a[64:79]\n\t"
    "buffer_load_dwordx4 v15, s[28:31], s41 offen offset:2048 lds\n\t"
    "v_add_f32_e32 v22, v22, v41\n\t"
    "v_add_f32_e32 v20, v20, v42\n\t"
    "v_exp_f32_e32 v43, v43\n\t"
    "v_mfma_f32_32x32x16_bf16 a[80:95], v[232:235], v[144:147], a[80:95]\n\t"
    "v_add_f32_e32 v22, v22, v43\n\t"
    "v_exp_f32_e32 v44, v44\n\t"
    "v_exp_f32_e32 v45, v45\n\t"
    "v_mfma_f32_32x32x16_bf16 a[96:111], v[248:251], v[128:131], a[96:111]\n\t"
    "v_add_f32_e32 v20, v20, v44\n\t"
    "v_add_f32_e32 v22, v22, v45\n\t"
    "v_exp_f32_e32 v46, v46\n\t"
    "v_exp_f32_e32 v47, v47\n\t"
    "v_mfma_f32_32x32x16_bf16 a[112:127], v[248:251], v[144:147], a[112:127]\n\t"
    "v_add_f32_e32 v20, v20, v46\n\t"
    "v_add_f32_e32 v22, v22, v47\n\t"
    "v_exp_f32_e32 v48, v48\n\t"
    "v_exp_f32_e32 v49, v49\n\t"
    "v_mfma_f32_32x32x16_bf16 a[0:15], v[204:207], v[136:139], a[0:15]\n\t"
    "s_add_u32 m0, s43, 896\n\t"
    "s_nop 0\n\t"
    "buffer_load_dwordx4 v15, s[28:31], s41 offen offset:128 lds\n\t"
    "v_add_f32_e32 v21, v21, v48\n\t"
    "v_add_f32_e32 v23, v23, v49\n\t"
    "v_mfma_f32_32x32x16_bf16 a[16:31], v[204:207], v[152:155], a[16:31]\n\t"
    "v_exp_f32_e32 v50, v50\n\t"
    "v_exp_f32_e32 v51, v51\n\t"
    "v_mfma_f32_32x32x16_bf16 a[32:47], v[220:223], v[136:139], a[32:47]\n\t"
    "v_add_f32_e32 v21, v21, v50\n\t"
    "v_add_f32_e32 v23, v23, v51\n\t"
    "v_exp_f32_e32 v52, v52\n\t"
    "v_exp_f32_e32 v53, v53\n\t"
    "v_mfma_f32_32x32x16_bf16 a[48:63], v[220:223], v[152:155], a[48:63]\n\t"
    "v_add_f32_e32 v21, v21, v52\n\t"
    "v_add_f32_e32 v23, v23, v53\n\t"
    "v_exp_f32_e32 v54, v54\n\t"
    "v_exp_f32_e32 v55, v55\n\t"
    "v_mfma_f32_32x32x16_bf16 a[64:79], v[236:239], v[136:139], a[64:79]\n\t"
    "buffer_load_dwordx4 v15, s[28:31], s41 offen offset:2176 lds\n\t"
    "v_add_f32_e32 v21, v21, v54\n\t"
    "v_add_f32_e32 v23, v23, v55\n\t"
    "v_exp_f32_e32 v56, v56\n\t"
    "v_mfma_f32_32x32x16_bf16 a[80:95], v[236:239], v[152:155], a[80:95]\n\t"
    "v_add_f32_e32 v21, v21, v56\n\t"
    "v_exp_f32_e32 v57, v57\n\t"
    "v_exp_f32_e32 v58, v58\n\t"
    "v_mfma_f32_32x32x16_bf16 a[96:111], v[252:255], v[136:139], a[96:111]\n\t"
    "v_add_f32_e32 v23, v23, v57\n\t"
    "v_add_f32_e32 v21, v21, v58\n\t"
    "v_exp_f32_e32 v59, v59\n\t"
    "v_exp_f32_e32 v60, v60\n\t"
    "v_mfma_f32_32x32x16_bf16 a[112:127], v[252:255], v[152:155], a[112:127]\n\t"
    "v_add_f32_e32 v23, v23, v59\n\t"
    "v_add_f32_e32 v21, v21, v60\n\t"
    "v_exp_f32_e32 v61, v61\n\t"
    "v_exp_f32_e32 v62, v62\n\t"
    "v_add_f32_e32 v23, v23, v61\n\t"
    "v_add_f32_e32 v21, v21, v62\n\t"
    "v_exp_f32_e32 v63, v63\n\t"
    "s_nop 0\n\t"
    "v_add_f32_e32 v23, v23, v63\n\t"
    "s_add_u32 s40, s40, 16384\n\t"
    "s_add_u32 s41, s41, 16384\n\t"
    "s_cmp_lg_u32 s47, 0\n\t"
    "s_cbranch_scc1 L_RESC_p2\n\t"
    "L_RRET_p2:\n\t"
    "s_waitcnt lgkmcnt(0)\n\t"
    "v_mfma_f32_32x32x16_bf16 v[96:111], a[192:195], a[128:131], v[160:175]\n\t"
    "ds_read_b64_tr_b16 v[192:193], v12 offset:32768\n\t"
    "v_exp_f32_e32 v64, v64\n\t"
    "v_exp_f32_e32 v65, v65\n\t"
    "v_cvt_pk_bf16_f32 v32, v32, v33\n\t"
    "v_mfma_f32_32x32x16_bf16 v[112:127], a[192:195], a[160:163], v[176:191]\n\t"
    "ds_read_b64_tr_b16 v[194:195], v12 offset:34816\n\t"
    "v_exp_f32_e32 v66, v66\n\t"
    "v_exp_f32_e32 v67, v67\n\t"
    "v_add_f32_e32 v20, v20, v64\n\t"
    "v_mfma_f32_32x32x16_bf16 v[128:143], a[224:227], a[128:131], v[160:175]\n\t"
    "ds_read_b64_tr_b16 v[208:209], v12 offset:33280\n\t"
    "v_exp_f32_e32 v68, v68\n\t"
    "v_exp_f32_e32 v69, v69\n\t"
    "v_add_f32_e32 v22, v22, v65\n\t"
    "v_mfma_f32_32x32x16_bf16 v[144:159], a[224:227], a[160:163], v[176:191]\n\t"
    "ds_read_b64_tr_b16 v[210:211], v12 offset:35328\n\t"
    "v_exp_f32_e32 v70, v70\n\t"
    "v_exp_f32_e32 v71, v71\n\t"
    "v_cvt_pk_bf16_f32 v64, v64, v65\n\t"
    "v_mfma_f32_32x32x16_bf16 v[96:111], a[196:199], a[132:135], v[96:111]\n\t"
    "ds_read_b64_tr_b16 v[224:225], v12 offset:33792\n\t"
    "v_exp_f32_e32 v72, v72\n\t"
    "v_exp_f32_e32 v73, v73\n\t"
    "v_add_f32_e32 v20, v20, v66\n\t"
    "v_mfma_f32_32x32x16_bf16 v[112:127], a[196:199], a[164:167], v[112:127]\n\t"
    "ds_read_b64_tr_b16 v[226:227], v12 offset:35840\n\t"
    "v_exp_f32_e32 v74, v74\n\t"
    "v_exp_f32_e32 v75, v75\n\t"
    "v_add_f32_e32 v22, v22, v67\n\t"
    "v_mfma_f32_32x32x16_bf16 v[128:143], a[228:231], a[132:135], v[128:143]\n\t"
    "ds_read_b64_tr_b16 v[240:241], v12 offset:34304\n\t"
    "v_exp_f32_e32 v76, v76\n\t"
    "v_cvt_pk_bf16_f32 v65, v66, v67\n\t"
    "v_add_f32_e32 v20, v20, v68\n\t"
    "v_add_f32_e32 v22, v22, v69\n\t"
    "v_mfma_f32_32x32x16_bf16 v[144:159], a[228:231], a[164:167], v[144:159]\n\t"
    "ds_read_b64_tr_b16 v[242:243], v12 offset:36352\n\t"
    "v_exp_f32_e32 v77, v77\n\t"
    "v_cvt_pk_bf16_f32 v66, v68, v69\n\t"
    "v_add_f32_e32 v20, v20, v70\n\t"
    "v_add_f32_e32 v22, v22, v71\n\t"
    "v_mfma_f32_32x32x16_bf16 v[96:111], a[200:203], a[136:139], v[96:111]\n\t"
    "ds_read_b64_tr_b16 v[196:197], v12 offset:36864\n\t"
    "v_exp_f32_e32 v78, v78\n\t"
    "v_cvt_pk_bf16_f32 v67, v70, v71\n\t"
    "v_add_f32_e32 v20, v20, v72\n\t"
    "v_add_f32_e32 v22, v22, v73\n\t"
    "v_mfma_f32_32x32x16_bf16 v[112:127], a[200:203], a[168:171], v[112:127]\n\t"
    "ds_read_b64_tr_b16 v[198:199], v12 offset:38912\n\t"
    "v_exp_f32_e32 v79, v79\n\t"
    "v_cvt_pk_bf16_f32 v72, v72, v73\n\t"
    "v_add_f32_e32 v20, v20, v74\n\t"
    "v_add_f32_e32 v22, v22, v75\n\t"
    "v_mfma_f32_32x32x16_bf16 v[128:143], a[232:235], a[136:139], v[128:143]\n\t"
    "ds_read_b64_tr_b16 v[212:213], v12 offset:37376\n\t"
    "v_exp_f32_e32 v80, v80\n\t"
    "v_cvt_pk_bf16_f32 v73, v74, v75\n\t"
    "v_add_f32_e32 v20, v20, v76\n\t"
    "v_add_f32_e32 v22, v22, v77\n\t"
    "v_mfma_f32_32x32x16_bf16 v[144:159], a[232:235], a[168:171], v[144:159]\n\t"
    "ds_read_b64_tr_b16 v[214:215], v12 offset:39424\n\t"
    "v_exp_f32_e32 v81, v81\n\t"
    "v_cvt_pk_bf16_f32 v74, v76, v77\n\t"
    "v_add_f32_e32 v20, v20, v78\n\t"
    "v_add_f32_e32 v22, v22, v79\n\t"
    "v_mfma_f32_32x32x16_bf16 v[96:111], a[204:207], a[140:143], v[96:111]\n\t"
    "ds_read_b64_tr_b16 v[228:229], v12 offset:37888\n\t"
    "v_exp_f32_e32 v82, v82\n\t"
    "v_cvt_pk_bf16_f32 v75, v78, v79\n\t"
    "v_add_f32_e32 v21, v21, v80\n\t"
    "v_add_f32_e32 v23, v23, v81\n\t"
    "v_mfma_f32_32x32x16_bf16 v[112:127], a[204:207], a[172:175], v[112:127]\n\t"
    "ds_read_b64_tr_b16 v[230:231], v12 offset:39936\n\t"
    "v_exp_f32_e32 v83, v83\n\t"
    "v_cvt_pk_bf16_f32 v80, v80, v81\n\t"
    "v_add_f32_e32 v21, v21, v82\n\t"
    "v_cvt_pk_bf16_f32 v33, v34, v35\n\t"
    "v_mfma_f32_32x32x16_bf16 v[128:143], a[236:239], a[140:143], v[128:143]\n\t"
    "ds_read_b64_tr_b16 v[244:245], v12 offset:38400\n\t"
    "v_add_f32_e32 v23, v23, v83\n\t"
    "v_exp_f32_e32 v84, v84\n\t"
    "v_cvt_pk_bf16_f32 v81, v82, v83\n\t"
    "v_cvt_pk_bf16_f32 v34, v36, v37\n\t"
    "v_mfma_f32_32x32x16_bf16 v[144:159], a[236:239], a[172:175], v[144:159]\n\t"
    "ds_read_b64_tr_b16 v[246:247], v12 offset:40448\n\t"
    "v_add_f32_e32 v21, v21, v84\n\t"
    "v_exp_f32_e32 v85, v85\n\t"
    "v_exp_f32_e32 v86, v86\n\t"
    "v_mfma_f32_32x32x16_bf16 v[96:111], a[208:211], a[144:147], v[96:111]\n\t"
    "ds_read_b64_tr_b16 v[200:201], v12 offset:40960\n\t"
    "v_add_f32_e32 v23, v23, v85\n\t"
    "v_cvt_pk_bf16_f32 v82, v84, v85\n\t"
    "v_add_f32_e32 v21, v21, v86\n\t"
    "v_exp_f32_e32 v87, v87\n\t"
    "v_mfma_f32_32x32x16_bf16 v[112:127], a[208:211], a[176:179], v[112:127]\n\t"
    "ds_read_b64_tr_b16 v[202:203], v12 offset:43008\n\t"
    "v_add_f32_e32 v23, v23, v87\n\t"
    "v_cvt_pk_bf16_f32 v83, v86, v87\n\t"
    "v_exp_f32_e32 v88, v88\n\t"
    "v_cvt_pk_bf16_f32 v35, v38, v39\n\t"
    "v_mfma_f32_32x32x16_bf16 v[128:143], a[240:243], a[144:147], v[128:143]\n\t"
    "ds_read_b64_tr_b16 v[216:217], v12 offset:41472\n\t"
    "v_add_f32_e32 v21, v21, v88\n\t"
    "v_exp_f32_e32 v89, v89\n\t"
    "v_exp_f32_e32 v90, v90\n\t"
    "v_mfma_f32_32x32x16_bf16 v[144:159], a[240:243], a[176:179], v[144:159]\n\t"
    "ds_read_b64_tr_b16 v[218:219], v12 offset:43520\n\t"
    "v_add_f32_e32 v23, v23, v89\n\t"
    "v_cvt_pk_bf16_f32 v88, v88, v89\n\t"
    "v_add_f32_e32 v21, v21, v90\n\t"
    "v_exp_f32_e32 v91, v91\n\t"
    "v_mfma_f32_32x32x16_bf16 v[96:111], a[212:215], a[148:151], v[96:111]\n\t"
    "ds_read_b64_tr_b16 v[232:233], v12 offset:41984\n\t"
    "v_add_f32_e32 v23, v23, v91\n\t"
    "v_cvt_pk_bf16_f32 v89, v90, v91\n\t"
    "v_exp_f32_e32 v92, v92\n\t"
    "v_cvt_pk_bf16_f32 v40, v40, v41\n\t"
    "v_mfma_f32_32x32x16_bf16 v[112:127], a[212:215], a[180:183], v[112:127]\n\t"
    "ds_read_b64_tr_b16 v[234:235], v12 offset:44032\n\t"
    "v_add_f32_e32 v21, v21, v92\n\t"
    "v_exp_f32_e32 v93, v93\n\t"
    "v_exp_f32_e32 v94, v94\n\t"
    "v_mfma_f32_32x32x16_bf16 v[128:143], a[244:247], a[148:151], v[128:143]\n\t"
    "ds_read_b64_tr_b16 v[248:249], v12 offset:42496\n\t"
    "v_add_f32_e32 v23, v23, v93\n\t"
    "v_cvt_pk_bf16_f32 v90, v92, v93\n\t"
    "v_add_f32_e32 v21, v21, v94\n\t"
    "v_exp_f32_e32 v95, v95\n\t"
    "v_mfma_f32_32x32x16_bf16 v[144:159], a[244:247], a[180:183], v[144:159]\n\t"
    "ds_read_b64_tr_b16 v[250:251], v12 offset:44544\n\t"
    "v_add_f32_e32 v23, v23, v95\n\t"
    "v_cvt_pk_bf16_f32 v91, v94, v95\n\t"
    "v_cvt_pk_bf16_f32 v41, v42, v43\n\t"
    "v_cvt_pk_bf16_f32 v42, v44, v45\n\t"
    "v_cvt_pk_bf16_f32 v43, v46, v47\n\t"
    "v_mfma_f32_32x32x16_bf16 v[96:111], a[216:219], a[152:155], v[96:111]\n\t"
    "ds_read_b64_tr_b16 v[204:205], v12 offset:45056\n\t"
    "v_cvt_pk_bf16_f32 v48, v48, v49\n\t"
    "v_cvt_pk_bf16_f32 v49, v50, v51\n\t"
    "v_cvt_pk_bf16_f32 v50, v52, v53\n\t"
    "v_cvt_pk_bf16_f32 v51, v54, v55\n\t"
    "v_cvt_pk_bf16_f32 v56, v56, v57\n\t"
    "v_mfma_f32_32x32x16_bf16 v[112:127], a[216:219], a[184:187], v[112:127]\n\t"
    "ds_read_b64_tr_b16 v[206:207], v12 offset:47104\n\t"
    "v_cvt_pk_bf16_f32 v57, v58, v59\n\t"
    "v_cvt_pk_bf16_f32 v58, v60, v61\n\t"
    "v_cvt_pk_bf16_f32 v59, v62, v63\n\t"
    "v_mfma_f32_32x32x16_bf16 v[128:143], a[248:251], a[152:155], v[128:143]\n\t"
    "ds_read_b64_tr_b16 v[220:221], v12 offset:45568\n\t"
    "v_mfma_f32_32x32x16_bf16 v[144:159], a[248:251], a[184:187], v[144:159]\n\t"
    "ds_read_b64_tr_b16 v[222:223], v12 offset:47616\n\t"
    "v_mfma_f32_32x32x16_bf16 v[96:111], a[220:223], a[156:159], v[96:111]\n\t"
    "ds_read_b64_tr_b16 v[236:237], v12 offset:46080\n\t"
    "v_mfma_f32_32x32x16_bf16 v[112:127], a[220:223], a[188:191], v[112:127]\n\t"
    "ds_read_b64_tr_b16 v[238:239], v12 offset:48128\n\t"
    "v_mfma_f32_32x32x16_bf16 v[128:143], a[252:255], a[156:159], v[128:143]\n\t"
    "ds_read_b64_tr_b16 v[252:253], v12 offset:46592\n\t"
    "v_mfma_f32_32x32x16_bf16 v[144:159], a[252:255], a[188:191], v[144:159]\n\t"
    "ds_read_b64_tr_b16 v[254:255], v12 offset:48640\n\t"
    "s_waitcnt vmcnt(8) lgkmcnt(0)\n\t"
    "s_barrier\n\t"
    "v_mfma_f32_32x32x16_bf16 a[0:15], v[192:195], v[32:35], a[0:15]\n\t"
    "s_add_u32 m0, s42, 32768\n\t"
    "s_nop 0\n\t"
    "buffer_load_dwordx4 v13, s[24:27], s40 offen offset:0 lds\n\t"
    "ds_read_b128 a[192:195], v8 offset:0\n\t"
    "v_mfma_f32_32x32x16_bf16 a[16:31], v[192:195], v[48:51], a[16:31]\n\t"
    "ds_read_b128 a[196:199], v9 offset:0\n\t"
    "v_mfma_f32_32x32x16_bf16 a[32:47], v[208:211], v[32:35], a[32:47]\n\t"
    "ds_read_b128 a[200:203], v10 offset:0\n\t"
    "v_mfma_f32_32x32x16_bf16 a[48:63], v[208:211], v[48:51], a[48:63]\n\t"
    "ds_read_b128 a[204:207], v11 offset:0\n\t"
    "v_max3_f32 v26, v96, v97, v128\n\t"
    "v_max3_f32 v28, v98, v99, v129\n\t"
    "v_max3_f32 v26, v26, v130, v131\n\t"
    "v_max3_f32 v26, v26, v100, v101\n\t"
    "v_max3_f32 v28, v28, v102, v103\n\t"
    "v_mfma_f32_32x32x16_bf16 a[64:79], v[224:227], v[32:35], a[64:79]\n\t"
    "buffer_load_dwordx4 v14, s[24:27], s40 offen offset:1024 lds\n\t"
    "ds_read_b128 a[208:211], v8 offset:128\n\t"
    "v_max3_f32 v26, v26, v132, v133\n\t"
    "v_max3_f32 v28, v28, v134, v135\n\t"
    "v_max3_f32 v26, v26, v104, v105\n\t"
    "v_max3_f32 v28, v28, v106, v107\n\t"
    "v_mfma_f32_32x32x16_bf16 a[80:95], v[224:227], v[48:51], a[80:95]\n\t"
    "ds_read_b128 a[212:215], v9 offset:128\n\t"
    "v_max3_f32 v26, v26, v136, v137\n\t"
    "v_max3_f32 v28, v28, v138, v139\n\t"
    "v_max3_f32 v26, v26, v108, v109\n\t"
    "v_max3_f32 v28, v28, v110, v111\n\t"
    "v_max3_f32 v26, v26, v140, v141\n\t"
    "v_mfma_f32_32x32x16_bf16 a[96:111], v[240:243], v[32:35], a[96:111]\n\t"
    "ds_read_b128 a[216:219], v10 offset:128\n\t"
    "v_max3_f32 v28, v28, v142, v143\n\t"
    "v_max3_f32 v27, v112, v113, v144\n\t"
    "v_max3_f32 v29, v114, v115, v145\n\t"
    "v_max3_f32 v27, v27, v146, v147\n\t"
    "v_max3_f32 v27, v27, v116, v117\n\t"
    "v_mfma_f32_32x32x16_bf16 a[112:127], v[240:243], v[48:51], a[112:127]\n\t"
    "ds_read_b128 a[220:223], v11 offset:128\n\t"
    "v_max3_f32 v29, v29, v118, v119\n\t"
    "v_max3_f32 v27, v27, v148, v149\n\t"
    "v_max3_f32 v29, v29, v150, v151\n\t"
    "v_max3_f32 v27, v27, v120, v121\n\t"
    "v_max3_f32 v29, v29, v122, v123\n\t"
    "v_mfma_f32_32x32x16_bf16 a[0:15], v[196:199], v[40:43], a[0:15]\n\t"
    "buffer_load_dwordx4 v13, s[24:27], s40 offen offset:2048 lds\n\t"
    "ds_read_b128 a[224:227], v8 offset:8192\n\t"
    "v_max3_f32 v27, v27, v152, v153\n\t"
    "v_max3_f32 v29, v29, v154, v155\n\t"
    "v_max3_f32 v27, v27, v124, v125\n\t"
    "v_max3_f32 v29, v29, v126, v127\n\t"
    "v_mfma_f32_32x32x16_bf16 a[16:31], v[196:199], v[56:59], a[16:31]\n\t"
    "ds_read_b128 a[228:231], v9 offset:8192\n\t"
    "v_max3_f32 v27, v27, v156, v157\n\t"
    "v_max3_f32 v29, v29, v158, v159\n\t"
    "v_mfma_f32_32x32x16_bf16 a[32:47], v[212:215], v[40:43], a[32:47]\n\t"
    "ds_read_b128 a[232:235], v10 offset:8192\n\t"
    "v_max_f32_e32 v30, v26, v28\n\t"
    "v_mov_b32_e32 v5, v30\n\t"
    "v_max_f32_e32 v31, v27, v29\n\t"
    "v_mov_b32_e32 v6, v31\n\t"
    "v_mfma_f32_32x32x16_bf16 a[48:63], v[212:215], v[56:59], a[48:63]\n\t"
    "ds_read_b128 a[236:239], v11 offset:8192\n\t"
    "v_permlane32_swap_b32_e32 v30, v5\n\t"
    "v_max_f32_e32 v30, v30, v5\n\t"
    "v_permlane32_swap_b32_e32 v31, v6\n\t"
    "v_max_f32_e32 v31, v31, v6\n\t"
    "v_mfma_f32_32x32x16_bf16 a[64:79], v[228:231], v[40:43], a[64:79]\n\t"
    "buffer_load_dwordx4 v14, s[24:27], s40 offen offset:3072 lds\n\t"
    "ds_read_b128 a[240:243], v8 offset:8320\n\t"
    "v_max_f32_e32 v7, v30, v31\n\t"
    "v_cmp_lt_f32_e32 vcc, s45, v7\n\t"
    "s_cbranch_vccnz L_RARE_p3\n\t"
    "L_RET_p3:\n\t"
    "v_mfma_f32_32x32x16_bf16 a[80:95], v[228:231], v[56:59], a[80:95]\n\t"
    "ds_read_b128 a[244:247], v9 offset:8320\n\t"
    "v_exp_f32_e32 v96, v96\n\t"
    "v_exp_f32_e32 v97, v97\n\t"
    "v_mfma_f32_32x32x16_bf16 a[96:111], v[244:247], v[40:43], a[96:111]\n\t"
    "ds_read_b128 a[248:251], v10 offset:8320\n\t"
    "v_add_f32_e32 v20, v20, v96\n\t"
    "v_add_f32_e32 v22, v22, v97\n\t"
    "v_exp_f32_e32 v98, v98\n\t"
    "v_mfma_f32_32x32x16_bf16 a[112:127], v[244:247], v[56:59], a[112:127]\n\t"
    "ds_read_b128 a[252:255], v11 offset:8320\n\t"
    "v_add_f32_e32 v20, v20, v98\n\t"
    "v_exp_f32_e32 v99, v99\n\t"
    "v_exp_f32_e32 v100, v100\n\t"
    "v_mfma_f32_32x32x16_bf16 a[0:15], v[200:203], v[64:67], a[0:15]\n\t"
    "s_add_u32 m0, s43, 16384\n\t"
    "s_nop 0\n\t"
    "buffer_load_dwordx4 v15, s[28:31], s41 offen offset:0 lds\n\t"
    "v_add_f32_e32 v22, v22, v99\n\t"
    "v_add_f32_e32 v20, v20, v100\n\t"
    "v_mfma_f32_32x32x16_bf16 a[16:31], v[200:203], v[80:83], a[16:31]\n\t"
    "v_exp_f32_e32 v101, v101\n\t"
    "v_exp_f32_e32 v102, v102\n\t"
    "v_mfma_f32_32x32x16_bf16 a[32:47], v[216:219], v[64:67], a[32:47]\n\t"
    "v_add_f32_e32 v22, v22, v101\n\t"
    "v_add_f32_e32 v20, v20, v102\n\t"
    "v_exp_f32_e32 v103, v103\n\t"
    "v_exp_f32_e32 v104, v104\n\t"
    "v_mfma_f32_32x32x16_bf16 a[48:63], v[216:219], v[80:83], a[48:63]\n\t"
    "v_add_f32_e32 v22, v22, v103\n\t"
    "v_add_f32_e32 v20, v20, v104\n\t"
    "v_exp_f32_e32 v105, v105\n\t"
    "v_exp_f32_e32 v106, v106\n\t"
    "v_mfma_f32_32x32x16_bf16 a[64:79], v[232:235], v[64:67], a[64:79]\n\t"
    "buffer_load_dwordx4 v15, s[28:31], s41 offen offset:2048 lds\n\t"
    "v_add_f32_e32 v22, v22, v105\n\t"
    "v_add_f32_e32 v20, v20, v106\n\t"
    "v_exp_f32_e32 v107, v107\n\t"
    "v_mfma_f32_32x32x16_bf16 a[80:95], v[232:235], v[80:83], a[80:95]\n\t"
    "v_add_f32_e32 v22, v22, v107\n\t"
    "v_exp_f32_e32 v108, v108\n\t"
    "v_exp_f32_e32 v109, v109\n\t"
    "v_mfma_f32_32x32x16_bf16 a[96:111], v[248:251], v[64:67], a[96:111]\n\t"
    "v_add_f32_e32 v20, v20, v108\n\t"
    "v_add_f32_e32 v22, v22, v109\n\t"
    "v_exp_f32_e32 v110, v110\n\t"
    "v_exp_f32_e32 v111, v111\n\t"
    "v_mfma_f32_32x32x16_bf16 a[112:127], v[248:251], v[80:83], a[112:127]\n\t"
    "v_add_f32_e32 v20, v20, v110\n\t"
    "v_add_f32_e32 v22, v22, v111\n\t"
    "v_exp_f32_e32 v112, v112\n\t"
    "v_exp_f32_e32 v113, v113\n\t"
    "v_mfma_f32_32x32x16_bf16 a[0:15], v[204:207], v[72:75], a[0:15]\n\t"
    "s_add_u32 m0, s43, 17280\n\t"
    "s_nop 0\n\t"
    "buffer_load_dwordx4 v15, s[28:31], s41 offen offset:128 lds\n\t"
    "v_add_f32_e32 v21, v21, v112\n\t"
    "v_add_f32_e32 v23, v23, v113\n\t"
    "v_mfma_f32_32x32x16_bf16 a[16:31], v[204:207], v[88:91], a[16:31]\n\t"
    "v_exp_f32_e32 v114, v114\n\t"
    "v_exp_f32_e32 v115, v115\n\t"
    "v_mfma_f32_32x32x16_bf16 a[32:47], v[220:223], v[72:75], a[32:47]\n\t"
    "v_add_f32_e32 v21, v21, v114\n\t"
    "v_add_f32_e32 v23, v23, v115\n\t"
    "v_exp_f32_e32 v116, v116\n\t"
    "v_exp_f32_e32 v117, v117\n\t"
    "v_mfma_f32_32x32x16_bf16 a[48:63], v[220:223], v[88:91], a[48:63]\n\t"
    "v_add_f32_e32 v21, v21, v116\n\t"
    "v_add_f32_e32 v23, v23, v117\n\t"
    "v_exp_f32_e32 v118, v118\n\t"
    "v_exp_f32_e32 v119, v119\n\t"
    "v_mfma_f32_32x32x16_bf16 a[64:79], v[236:239], v[72:75], a[64:79]\n\t"
    "buffer_load_dwordx4 v15, s[28:31], s41 offen offset:2176 lds\n\t"
    "v_add_f32_e32 v21, v21, v118\n\t"
    "v_add_f32_e32 v23, v23, v119\n\t"
    "v_exp_f32_e32 v120, v120\n\t"
    "v_mfma_f32_32x32x16_bf16 a[80:95], v[236:239], v[88:91], a[80:95]\n\t"
    "v_add_f32_e32 v21, v21, v120\n\t"
    "v_exp_f32_e32 v121, v121\n\t"
    "v_exp_f32_e32 v122, v122\n\t"
    "v_mfma_f32_32x32x16_bf16 a[96:111], v[252:255], v[72:75], a[96:111]\n\t"
    "v_add_f32_e32 v23, v23, v121\n\t"
    "v_add_f32_e32 v21, v21, v122\n\t"
    "v_exp_f32_e32 v123, v123\n\t"
    "v_exp_f32_e32 v124, v124\n\t"
    "v_mfma_f32_32x32x16_bf16 a[112:127], v[252:255], v[88:91], a[112:127]\n\t"
    "v_add_f32_e32 v23, v23, v123\n\t"
    "v_add_f32_e32 v21, v21, v124\n\t"
    "v_exp_f32_e32 v125, v125\n\t"
    "v_exp_f32_e32 v126, v126\n\t"
    "v_add_f32_e32 v23, v23, v125\n\t"
    "v_add_f32_e32 v21, v21, v126\n\t"
    "v_exp_f32_e32 v127, v127\n\t"
    "s_nop 0\n\t"
    "v_add_f32_e32 v23, v23, v127\n\t"
    "s_add_u32 s40, s40, 16384\n\t"
    "s_add_u32 s41, s41, 16384\n\t"
    "s_cmp_lg_u32 s47, 0\n\t"
    "s_cbranch_scc1 L_RESC_p3\n\t"
    "L_RRET_p3:\n\t"
    "s_waitcnt lgkmcnt(0)\n\t"
    "s_mov_b32 s46, 7\n\t"
    "L_LOOP:\n\t"
    "v_mfma_f32_32x32x16_bf16 v[32:47], a[192:195], a[128:131], v[160:175]\n\t"
    "ds_read_b64_tr_b16 v[192:193], v12 offset:49152\n\t"
    "v_exp_f32_e32 v128, v128\n\t"
    "v_exp_f32_e32 v129, v129\n\t"
    "v_cvt_pk_bf16_f32 v96, v96, v97\n\t"
    "v_mfma_f32_32x32x16_bf16 v[48:63], a[192:195], a[160:163], v[176:191]\n\t"
    "ds_read_b64_tr_b16 v[194:195], v12 offset:51200\n\t"
    "v_exp_f32_e32 v130, v130\n\t"
    "v_exp_f32_e32 v131, v131\n\t"
    "v_add_f32_e32 v20, v20, v128\n\t"
    "v_mfma_f32_32x32x16_bf16 v[64:79], a[224:227], a[128:131], v[160:175]\n\t"
    "ds_read_b64_tr_b16 v[208:209], v12 offset:49664\n\t"
    "v_exp_f32_e32 v132, v132\n\t"
    "v_exp_f32_e32 v133, v133\n\t"
    "v_add_f32_e32 v22, v22, v129\n\t"
    "v_mfma_f32_32x32x16_bf16 v[80:95], a[224:227], a[160:163], v[176:191]\n\t"
    "ds_read_b64_tr_b16 v[210:211], v12 offset:51712\n\t"
    "v_exp_f32_e32 v134, v134\n\t"
    "v_exp_f32_e32 v135, v135\n\t"
    "v_cvt_pk_bf16_f32 v128, v128, v129\n\t"
    "v_mfma_f32_32x32x16_bf16 v[32:47], a[196:199], a[132:135], v[32:47]\n\t"
    "ds_read_b64_tr_b16 v[224:225], v12 offset:50176\n\t"
    "v_exp_f32_e32 v136, v136\n\t"
    "v_exp_f32_e32 v137, v137\n\t"
    "v_add_f32_e32 v20, v20, v130\n\t"
    "v_mfma_f32_32x32x16_bf16 v[48:63], a[196:199], a[164:167], v[48:63]\n\t"
    "ds_read_b64_tr_b16 v[226:227], v12 offset:52224\n\t"
    "v_exp_f32_e32 v138, v138\n\t"
    "v_exp_f32_e32 v139, v139\n\t"
    "v_add_f32_e32 v22, v22, v131\n\t"
    "v_mfma_f32_32x32x16_bf16 v[64:79], a[228:231], a[132:135], v[64:79]\n\t"
    "ds_read_b64_tr_b16 v[240:241], v12 offset:50688\n\t"
    "v_exp_f32_e32 v140, v140\n\t"
    "v_cvt_pk_bf16_f32 v129, v130, v131\n\t"
    "v_add_f32_e32 v20, v20, v132\n\t"
    "v_add_f32_e32 v22, v22, v133\n\t"
    "v_mfma_f32_32x32x16_bf16 v[80:95], a[228:231], a[164:167], v[80:95]\n\t"
    "ds_read_b64_tr_b16 v[242:243], v12 offset:52736\n\t"
    "v_exp_f32_e32 v141, v141\n\t"
    "v_cvt_pk_bf16_f32 v130, v132, v133\n\t"
    "v_add_f32_e32 v20, v20, v134\n\t"
    "v_add_f32_e32 v22, v22, v135\n\t"
    "v_mfma_f32_32x32x16_bf16 v[32:47], a[200:203], a[136:139], v[32:47]\n\t"
    "ds_read_b64_tr_b16 v[196:197], v12 offset:53248\n\t"
    "v_exp_f32_e32 v142, v142\n\t"
    "v_cvt_pk_bf16_f32 v131, v134, v135\n\t"
    "v_add_f32_e32 v20, v20, v136\n\t"
    "v_add_f32_e32 v22, v22, v137\n\t"
    "v_mfma_f32_32x32x16_bf16 v[48:63], a[200:203], a[168:171], v[48:63]\n\t"
    "ds_read_b64_tr_b16 v[198:199], v12 offset:55296\n\t"
    "v_exp_f32_e32 v143, v143\n\t"
    "v_cvt_pk_bf16_f32 v136, v136, v137\n\t"
    "v_add_f32_e32 v20, v20, v138\n\t"
    "v_add_f32_e32 v22, v22, v139\n\t"
    "v_mfma_f32_32x32x16_bf16 v[64:79], a[232:235], a[136:139], v[64:79]\n\t"
    "ds_read_b64_tr_b16 v[212:213], v12 offset:53760\n\t"
    "v_exp_f32_e32 v144, v144\n\t"
    "v_cvt_pk_bf16_f32 v137, v138, v139\n\t"
    "v_add_f32_e32 v20, v20, v140\n\t"
    "v_add_f32_e32 v22, v22, v141\n\t"
    "v_mfma_f32_32x32x16_bf16 v[80:95], a[232:235], a[168:171], v[80:95]\n\t"
    "ds_read_b64_tr_b16 v[214:215], v12 offset:55808\n\t"
    "v_exp_f32_e32 v145, v145\n\t"
    "v_cvt_pk_bf16_f32 v138, v140, v141\n\t"
    "v_add_f32_e32 v20, v20, v142\n\t"
    "v_add_f32_e32 v22, v22, v143\n\t"
    "v_mfma_f32_32x32x16_bf16 v[32:47], a[204:207], a[140:143], v[32:47]\n\t"
    "ds_read_b64_tr_b16 v[228:229], v12 offset:54272\n\t"
    "v_exp_f32_e32 v146, v146\n\t"
    "v_cvt_pk_bf16_f32 v139, v142, v143\n\t"
    "v_add_f32_e32 v21, v21, v144\n\t"
    "v_add_f32_e32 v23, v23, v145\n\t"
    "v_mfma_f32_32x32x16_bf16 v[48:63], a[204:207], a[172:175], v[48:63]\n\t"
    "ds_read_b64_tr_b16 v[230:231], v12 offset:56320\n\t"
    "v_exp_f32_e32 v147, v147\n\t"
    "v_cvt_pk_bf16_f32 v144, v144, v145\n\t"
    "v_add_f32_e32 v21, v21, v146\n\t"
    "v_cvt_pk_bf16_f32 v97, v98, v99\n\t"
    "v_mfma_f32_32x32x16_bf16 v[64:79], a[236:239], a[140:143], v[64:79]\n\t"
    "ds_read_b64_tr_b16 v[244:245], v12 offset:54784\n\t"
    "v_add_f32_e32 v23, v23, v147\n\t"
    "v_exp_f32_e32 v148, v148\n\t"
    "v_cvt_pk_bf16_f32 v145, v146, v147\n\t"
    "v_cvt_pk_bf16_f32 v98, v100, v101\n\t"
    "v_mfma_f32_32x32x16_bf16 v[80:95], a[236:239], a[172:175], v[80:95]\n\t"
    "ds_read_b64_tr_b16 v[246:247], v12 offset:56832\n\t"
    "v_add_f32_e32 v21, v21, v148\n\t"
    "v_exp_f32_e32 v149, v149\n\t"
    "v_exp_f32_e32 v150, v150\n\t"
    "v_mfma_f32_32x32x16_bf16 v[32:47], a[208:211], a[144:147], v[32:47]\n\t"
    "ds_read_b64_tr_b16 v[200:201], v12 offset:57344\n\t"
    "v_add_f32_e32 v23, v23, v149\n\t"
    "v_cvt_pk_bf16_f32 v146, v148, v149\n\t"
    "v_add_f32_e32 v21, v21, v150\n\t"
    "v_exp_f32_e32 v151, v151\n\t"
    "v_mfma_f32_32x32x16_bf16 v[48:63], a[208:211], a[176:179], v[48:63]\n\t"
    "ds_read_b64_tr_b16 v[202:203], v12 offset:59392\n\t"
    "v_add_f32_e32 v23, v23, v151\n\t"
    "v_cvt_pk_bf16_f32 v147, v150, v151\n\t"
    "v_exp_f32_e32 v152, v152\n\t"
    "v_cvt_pk_bf16_f32 v99, v102, v103\n\t"
    "v_mfma_f32_32x32x16_bf16 v[64:79], a[240:243], a[144:147], v[64:79]\n\t"
    "ds_read_b64_tr_b16 v[216:217], v12 offset:57856\n\t"
    "v_add_f32_e32 v21, v21, v152\n\t"
    "v_exp_f32_e32 v153, v153\n\t"
    "v_exp_f32_e32 v154, v154\n\t"
    "v_mfma_f32_32x32x16_bf16 v[80:95], a[240:243], a[176:179], v[80:95]\n\t"
    "ds_read_b64_tr_b16 v[218:219], v12 offset:59904\n\t"
    "v_add_f32_e32 v23, v23, v153\n\t"
    "v_cvt_pk_bf16_f32 v152, v152, v153\n\t"
    "v_add_f32_e32 v21, v21, v154\n\t"
    "v_exp_f32_e32 v155, v155\n\t"
    "v_mfma_f32_32x32x16_bf16 v[32:47], a[212:215], a[148:151], v[32:47]\n\t"
    "ds_read_b64_tr_b16 v[232:233], v12 offset:58368\n\t"
    "v_add_f32_e32 v23, v23, v155\n\t"
    "v_cvt_pk_bf16_f32 v153, v154, v155\n\t"
    "v_exp_f32_e32 v156, v156\n\t"
    "v_cvt_pk_bf16_f32 v104, v104, v105\n\t"
    "v_mfma_f32_32x32x16_bf16 v[48:63], a[212:215], a[180:183], v[48:63]\n\t"
    "ds_read_b64_tr_b16 v[234:235], v12 offset:60416\n\t"
    "v_add_f32_e32 v21, v21, v156\n\t"
    "v_exp_f32_e32 v157, v157\n\t"
    "v_exp_f32_e32 v158, v158\n\t"
    "v_mfma_f32_32x32x16_bf16 v[64:79], a[244:247], a[148:151], v[64:79]\n\t"
    "ds_read_b64_tr_b16 v[248:249], v12 offset:58880\n\t"
    "v_add_f32_e32 v23, v23, v157\n\t"
    "v_cvt_pk_bf16_f32 v154, v156, v157\n\t"
    "v_add_f32_e32 v21, v21, v158\n\t"
    "v_exp_f32_e32 v159, v159\n\t"
    "v_mfma_f32_32x32x16_bf16 v[80:95], a[244:247], a[180:183], v[80:95]\n\t"
    "ds_read_b64_tr_b16 v[250:251], v12 offset:60928\n\t"
    "v_add_f32_e32 v23, v23, v159\n\t"
    "v_cvt_pk_bf16_f32 v155, v158, v159\n\t"
    "v_cvt_pk_bf16_f32 v105, v106, v107\n\t"
    "v_cvt_pk_bf16_f32 v106, v108, v109\n\t"
    "v_cvt_pk_bf16_f32 v107, v110, v111\n\t"
    "v_mfma_f32_32x32x16_bf16 v[32:47], a[216:219], a[152:155], v[32:47]\n\t"
    "ds_read_b64_tr_b16 v[204:205], v12 offset:61440\n\t"
    "v_cvt_pk_bf16_f32 v112, v112, v113\n\t"
    "v_cvt_pk_bf16_f32 v113, v114, v115\n\t"
    "v_cvt_pk_bf16_f32 v114, v116, v117\n\t"
    "v_cvt_pk_bf16_f32 v115, v118, v119\n\t"
    "v_cvt_pk_bf16_f32 v120, v120, v121\n\t"
    "v_mfma_f32_32x32x16_bf16 v[48:63], a[216:219], a[184:187], v[48:63]\n\t"
    "ds_read_b64_tr_b16 v[206:207], v12 offset:63488\n\t"
    "v_cvt_pk_bf16_f32 v121, v122, v123\n\t"
    "v_cvt_pk_bf16_f32 v122, v124, v125\n\t"
    "v_cvt_pk_bf16_f32 v123, v126, v127\n\t"
    "v_mfma_f32_32x32x16_bf16 v[64:79], a[248:251], a[152:155], v[64:79]\n\t"
    "ds_read_b64_tr_b16 v[220:221], v12 offset:61952\n\t"
    "v_mfma_f32_32x32x16_bf16 v[80:95], a[248:251], a[184:187], v[80:95]\n\t"
    "ds_read_b64_tr_b16 v[222:223], v12 offset:64000\n\t"
    "v_mfma_f32_32x32x16_bf16 v[32:47], a[220:223], a[156:159], v[32:47]\n\t"
    "ds_read_b64_tr_b16 v[236:237], v12 offset:62464\n\t"
    "v_mfma_f32_32x32x16_bf16 v[48:63], a[220:223], a[188:191], v[48:63]\n\t"
    "ds_read_b64_tr_b16 v[238:239], v12 offset:64512\n\t"
    "v_mfma_f32_32x32x16_bf16 v[64:79], a[252:255], a[156:159], v[64:79]\n\t"
    "ds_read_b64_tr_b16 v[252:253], v12 offset:62976\n\t"
    "v_mfma_f32_32x32x16_bf16 v[80:95], a[252:255], a[188:191], v[80:95]\n\t"
    "ds_read_b64_tr_b16 v[254:255], v12 offset:65024\n\t"
    "s_waitcnt vmcnt(8) lgkmcnt(0)\n\t"
    "s_barrier\n\t"
    "v_mfma_f32_32x32x16_bf16 a[0:15], v[192:195], v[96:99], a[0:15]\n\t"
    "s_add_u32 m0, s42, 49152\n\t"
    "s_nop 0\n\t"
    "buffer_load_dwordx4 v13, s[24:27], s40 offen offset:0 lds\n\t"
    "ds_read_b128 a[192:195], v8 offset:16384\n\t"
    "v_mfma_f32_32x32x16_bf16 a[16:31], v[192:195], v[112:115], a[16:31]\n\t"
    "ds_read_b128 a[196:199], v9 offset:16384\n\t"
    "v_mfma_f32_32x32x16_bf16 a[32:47], v[208:211], v[96:99], a[32:47]\n\t"
    "ds_read_b128 a[200:203], v10 offset:16384\n\t"
    "v_mfma_f32_32x32x16_bf16 a[48:63], v[208:211], v[112:115], a[48:63]\n\t"
    "ds_read_b128 a[204:207], v11 offset:16384\n\t"
    "v_max3_f32 v26, v32, v33, v64\n\t"
    "v_max3_f32 v28, v34, v35, v65\n\t"
    "v_max3_f32 v26, v26, v66, v67\n\t"
    "v_max3_f32 v26, v26, v36, v37\n\t"
    "v_max3_f32 v28, v28, v38, v39\n\t"
    "v_mfma_f32_32x32x16_bf16 a[64:79], v[224:227], v[96:99], a[64:79]\n\t"
    "buffer_load_dwordx4 v14, s[24:27], s40 offen offset:1024 lds\n\t"
    "ds_read_b128 a[208:211], v8 offset:16512\n\t"
    "v_max3_f32 v26, v26, v68, v69\n\t"
    "v_max3_f32 v28, v28, v70, v71\n\t"
    "v_max3_f32 v26, v26, v40, v41\n\t"
    "v_max3_f32 v28, v28, v42, v43\n\t"
    "v_mfma_f32_32x32x16_bf16 a[80:95], v[224:227], v[112:115], a[80:95]\n\t"
    "ds_read_b128 a[212:215], v9 offset:16512\n\t"
    "v_max3_f32 v26, v26, v72, v73\n\t"
    "v_max3_f32 v28, v28, v74, v75\n\t"
    "v_max3_f32 v26, v26, v44, v45\n\t"
    "v_max3_f32 v28, v28, v46, v47\n\t"
    "v_max3_f32 v26, v26, v76, v77\n\t"
    "v_mfma_f32_32x32x16_bf16 a[96:111], v[240:243], v[96:99], a[96:111]\n\t"
    "ds_read_b128 a[216:219], v10 offset:16512\n\t"
    "v_max3_f32 v28, v28, v78, v79\n\t"
    "v_max3_f32 v27, v48, v49, v80\n\t"
    "v_max3_f32 v29, v50, v51, v81\n\t"
    "v_max3_f32 v27, v27, v82, v83\n\t"
    "v_max3_f32 v27, v27, v52, v53\n\t"
    "v_mfma_f32_32x32x16_bf16 a[112:127], v[240:243], v[112:115], a[112:127]\n\t"
    "ds_read_b128 a[220:223], v11 offset:16512\n\t"
    "v_max3_f32 v29, v29, v54, v55\n\t"
    "v_max3_f32 v27, v27, v84, v85\n\t"
    "v_max3_f32 v29, v29, v86, v87\n\t"
    "v_max3_f32 v27, v27, v56, v57\n\t"
    "v_max3_f32 v29, v29, v58, v59\n\t"
    "v_mfma_f32_32x32x16_bf16 a[0:15], v[196:199], v[104:107], a[0:15]\n\t"
    "buffer_load_dwordx4 v13, s[24:27], s40 offen offset:2048 lds\n\t"
    "ds_read_b128 a[224:227], v8 offset:24576\n\t"
    "v_max3_f32 v27, v27, v88, v89\n\t"
    "v_max3_f32 v29, v29, v90, v91\n\t"
    "v_max3_f32 v27, v27, v60, v61\n\t"
    "v_max3_f32 v29, v29, v62, v63\n\t"
    "v_mfma_f32_32x32x16_bf16 a[16:31], v[196:199], v[120:123], a[16:31]\n\t"
    "ds_read_b128 a[228:231], v9 offset:24576\n\t"
    "v_max3_f32 v27, v27, v92, v93\n\t"
    "v_max3_f32 v29, v29, v94, v95\n\t"
    "v_mfma_f32_32x32x16_bf16 a[32:47], v[212:215], v[104:107], a[32:47]\n\t"
    "ds_read_b128 a[232:235], v10 offset:24576\n\t"
    "v_max_f32_e32 v30, v26, v28\n\t"
    "v_mov_b32_e32 v5, v30\n\t"
    "v_max_f32_e32 v31, v27, v29\n\t"
    "v_mov_b32_e32 v6, v31\n\t"
    "v_mfma_f32_32x32x16_bf16 a[48:63], v[212:215], v[120:123], a[48:63]\n\t"
    "ds_read_b128 a[236:239], v11 offset:24576\n\t"
    "v_permlane32_swap_b32_e32 v30, v5\n\t"
    "v_max_f32_e32 v30, v30, v5\n\t"
    "v_permlane32_swap_b32_e32 v31, v6\n\t"
    "v_max_f32_e32 v31, v31, v6\n\t"
    "v_mfma_f32_32x32x16_bf16 a[64:79], v[228:231], v[104:107], a[64:79]\n\t"
    "buffer_load_dwordx4 v14, s[24:27], s40 offen offset:3072 lds\n\t"
    "ds_read_b128 a[240:243], v8 offset:24704\n\t"
    "v_max_f32_e32 v7, v30, v31\n\t"
    "v_cmp_lt_f32_e32 vcc, s45, v7\n\t"
    "s_cbranch_vccnz L_RARE_l4\n\t"
    "L_RET_l4:\n\t"
    "v_mfma_f32_32x32x16_bf16 a[80:95], v[228:231], v[120:123], a[80:95]\n\t"
    "ds_read_b128 a[244:247], v9 offset:24704\n\t"
    "v_exp_f32_e32 v32, v32\n\t"
    "v_exp_f32_e32 v33, v33\n\t"
    "v_mfma_f32_32x32x16_bf16 a[96:111], v[244:247], v[104:107], a[96:111]\n\t"
    "ds_read_b128 a[248:251], v10 offset:24704\n\t"
    "v_add_f32_e32 v20, v20, v32\n\t"
    "v_add_f32_e32 v22, v22, v33\n\t"
    "v_exp_f32_e32 v34, v34\n\t"
    "v_mfma_f32_32x32x16_bf16 a[112:127], v[244:247], v[120:123], a[112:127]\n\t"
    "ds_read_b128 a[252:255], v11 offset:24704\n\t"
    "v_add_f32_e32 v20, v20, v34\n\t"
    "v_exp_f32_e32 v35, v35\n\t"
    "v_exp_f32_e32 v36, v36\n\t"
    "v_mfma_f32_32x32x16_bf16 a[0:15], v[200:203], v[128:131], a[0:15]\n\t"
    "s_add_u32 m0, s43, 32768\n\t"
    "s_nop 0\n\t"
    "buffer_load_dwordx4 v15, s[28:31], s41 offen offset:0 lds\n\t"
    "v_add_f32_e32 v22, v22, v35\n\t"
    "v_add_f32_e32 v20, v20, v36\n\t"
    "v_mfma_f32_32x32x16_bf16 a[16:31], v[200:203], v[144:147], a[16:31]\n\t"
    "v_exp_f32_e32 v37, v37\n\t"
    "v_exp_f32_e32 v38, v38\n\t"
    "v_mfma_f32_32x32x16_bf16 a[32:47], v[216:219], v[128:131], a[32:47]\n\t"
    "v_add_f32_e32 v22, v22, v37\n\t"
    "v_add_f32_e32 v20, v20, v38\n\t"
    "v_exp_f32_e32 v39, v39\n\t"
    "v_exp_f32_e32 v40, v40\n\t"
    "v_mfma_f32_32x32x16_bf16 a[48:63], v[216:219], v[144:147], a[48:63]\n\t"
    "v_add_f32_e32 v22, v22, v39\n\t"
    "v_add_f32_e32 v20, v20, v40\n\t"
    "v_exp_f32_e32 v41, v41\n\t"
    "v_exp_f32_e32 v42, v42\n\t"
    "v_mfma_f32_32x32x16_bf16 a[64:79], v[232:235], v[128:131], a[64:79]\n\t"
    "buffer_load_dwordx4 v15, s[28:31], s41 offen offset:2048 lds\n\t"
    "v_add_f32_e32 v22, v22, v41\n\t"
    "v_add_f32_e32 v20, v20, v42\n\t"
    "v_exp_f32_e32 v43, v43\n\t"
    "v_mfma_f32_32x32x16_bf16 a[80:95], v[232:235], v[144:147], a[80:95]\n\t"
    "v_add_f32_e32 v22, v22, v43\n\t"
    "v_exp_f32_e32 v44, v44\n\t"
    "v_exp_f32_e32 v45, v45\n\t"
    "v_mfma_f32_32x32x16_bf16 a[96:111], v[248:251], v[128:131], a[96:111]\n\t"
    "v_add_f32_e32 v20, v20, v44\n\t"
    "v_add_f32_e32 v22, v22, v45\n\t"
    "v_exp_f32_e32 v46, v46\n\t"
    "v_exp_f32_e32 v47, v47\n\t"
    "v_mfma_f32_32x32x16_bf16 a[112:127], v[248:251], v[144:147], a[112:127]\n\t"
    "v_add_f32_e32 v20, v20, v46\n\t"
    "v_add_f32_e32 v22, v22, v47\n\t"
    "v_exp_f32_e32 v48, v48\n\t"
    "v_exp_f32_e32 v49, v49\n\t"
    "v_mfma_f32_32x32x16_bf16 a[0:15], v[204:207], v[136:139], a[0:15]\n\t"
    "s_add_u32 m0, s43, 33664\n\t"
    "s_nop 0\n\t"
    "buffer_load_dwordx4 v15, s[28:31], s41 offen offset:128 lds\n\t"
    "v_add_f32_e32 v21, v21, v48\n\t"
    "v_add_f32_e32 v23, v23, v49\n\t"
    "v_mfma_f32_32x32x16_bf16 a[16:31], v[204:207], v[152:155], a[16:31]\n\t"
    "v_exp_f32_e32 v50, v50\n\t"
    "v_exp_f32_e32 v51, v51\n\t"
    "v_mfma_f32_32x32x16_bf16 a[32:47], v[220:223], v[136:139], a[32:47]\n\t"
    "v_add_f32_e32 v21, v21, v50\n\t"
    "v_add_f32_e32 v23, v23, v51\n\t"
    "v_exp_f32_e32 v52, v52\n\t"
    "v_exp_f32_e32 v53, v53\n\t"
    "v_mfma_f32_32x32x16_bf16 a[48:63], v[220:223], v[152:155], a[48:63]\n\t"
    "v_add_f32_e32 v21, v21, v52\n\t"
    "v_add_f32_e32 v23, v23, v53\n\t"
    "v_exp_f32_e32 v54, v54\n\t"
    "v_exp_f32_e32 v55, v55\n\t"
    "v_mfma_f32_32x32x16_bf16 a[64:79], v[236:239], v[136:139], a[64:79]\n\t"
    "buffer_load_dwordx4 v15, s[28:31], s41 offen offset:2176 lds\n\t"
    "v_add_f32_e32 v21, v21, v54\n\t"
    "v_add_f32_e32 v23, v23, v55\n\t"
    "v_exp_f32_e32 v56, v56\n\t"
    "v_mfma_f32_32x32x16_bf16 a[80:95], v[236:239], v[152:155], a[80:95]\n\t"
    "v_add_f32_e32 v21, v21, v56\n\t"
    "v_exp_f32_e32 v57, v57\n\t"
    "v_exp_f32_e32 v58, v58\n\t"
    "v_mfma_f32_32x32x16_bf16 a[96:111], v[252:255], v[136:139], a[96:111]\n\t"
    "v_add_f32_e32 v23, v23, v57\n\t"
    "v_add_f32_e32 v21, v21, v58\n\t"
    "v_exp_f32_e32 v59, v59\n\t"
    "v_exp_f32_e32 v60, v60\n\t"
    "v_mfma_f32_32x32x16_bf16 a[112:127], v[252:255], v[152:155], a[112:127]\n\t"
    "v_add_f32_e32 v23, v23, v59\n\t"
    "v_add_f32_e32 v21, v21, v60\n\t"
    "v_exp_f32_e32 v61, v61\n\t"
    "v_exp_f32_e32 v62, v62\n\t"
    "v_add_f32_e32 v23, v23, v61\n\t"
    "v_add_f32_e32 v21, v21, v62\n\t"
    "v_exp_f32_e32 v63, v63\n\t"
    "s_nop 0\n\t"
    "v_add_f32_e32 v23, v23, v63\n\t"
    "s_add_u32 s40, s40, 16384\n\t"
    "s_add_u32 s41, s41, 16384\n\t"
    "s_cmp_lg_u32 s47, 0\n\t"
    "s_cbranch_scc1 L_RESC_l4\n\t"
    "L_RRET_l4:\n\t"
    "s_waitcnt lgkmcnt(0)\n\t"
    "v_mfma_f32_32x32x16_bf16 v[96:111], a[192:195], a[128:131], v[160:175]\n\t"
    "ds_read_b64_tr_b16 v[192:193], v12 offset:0\n\t"
    "v_exp_f32_e32 v64, v64\n\t"
    "v_exp_f32_e32 v65, v65\n\t"
    "v_cvt_pk_bf16_f32 v32, v32, v33\n\t"
    "v_mfma_f32_32x32x16_bf16 v[112:127], a[192:195], a[160:163], v[176:191]\n\t"
    "ds_read_b64_tr_b16 v[194:195], v12 offset:2048\n\t"
    "v_exp_f32_e32 v66, v66\n\t"
    "v_exp_f32_e32 v67, v67\n\t"
    "v_add_f32_e32 v20, v20, v64\n\t"
    "v_mfma_f32_32x32x16_bf16 v[128:143], a[224:227], a[128:131], v[160:175]\n\t"
    "ds_read_b64_tr_b16 v[208:209], v12 offset:512\n\t"
    "v_exp_f32_e32 v68, v68\n\t"
    "v_exp_f32_e32 v69, v69\n\t"
    "v_add_f32_e32 v22, v22, v65\n\t"
    "v_mfma_f32_32x32x16_bf16 v[144:159], a[224:227], a[160:163], v[176:191]\n\t"
    "ds_read_b64_tr_b16 v[210:211], v12 offset:2560\n\t"
    "v_exp_f32_e32 v70, v70\n\t"
    "v_exp_f32_e32 v71, v71\n\t"
    "v_cvt_pk_bf16_f32 v64, v64, v65\n\t"
    "v_mfma_f32_32x32x16_bf16 v[96:111], a[196:199], a[132:135], v[96:111]\n\t"
    "ds_read_b64_tr_b16 v[224:225], v12 offset:1024\n\t"
    "v_exp_f32_e32 v72, v72\n\t"
    "v_exp_f32_e32 v73, v73\n\t"
    "v_add_f32_e32 v20, v20, v66\n\t"
    "v_mfma_f32_32x32x16_bf16 v[112:127], a[196:199], a[164:167], v[112:127]\n\t"
    "ds_read_b64_tr_b16 v[226:227], v12 offset:3072\n\t"
    "v_exp_f32_e32 v74, v74\n\t"
    "v_exp_f32_e32 v75, v75\n\t"
    "v_add_f32_e32 v22, v22, v67\n\t"
    "v_mfma_f32_32x32x16_bf16 v[128:143], a[228:231], a[132:135], v[128:143]\n\t"
    "ds_read_b64_tr_b16 v[240:241], v12 offset:1536\n\t"
    "v_exp_f32_e32 v76, v76\n\t"
    "v_cvt_pk_bf16_f32 v65, v66, v67\n\t"
    "v_add_f32_e32 v20, v20, v68\n\t"
    "v_add_f32_e32 v22, v22, v69\n\t"
    "v_mfma_f32_32x32x16_bf16 v[144:159], a[228:231], a[164:167], v[144:159]\n\t"
    "ds_read_b64_tr_b16 v[242:243], v12 offset:3584\n\t"
    "v_exp_f32_e32 v77, v77\n\t"
    "v_cvt_pk_bf16_f32 v66, v68, v69\n\t"
    "v_add_f32_e32 v20, v20, v70\n\t"
    "v_add_f32_e32 v22, v22, v71\n\t"
    "v_mfma_f32_32x32x16_bf16 v[96:111], a[200:203], a[136:139], v[96:111]\n\t"
    "ds_read_b64_tr_b16 v[196:197], v12 offset:4096\n\t"
    "v_exp_f32_e32 v78, v78\n\t"
    "v_cvt_pk_bf16_f32 v67, v70, v71\n\t"
    "v_add_f32_e32 v20, v20, v72\n\t"
    "v_add_f32_e32 v22, v22, v73\n\t"
    "v_mfma_f32_32x32x16_bf16 v[112:127], a[200:203], a[168:171], v[112:127]\n\t"
    "ds_read_b64_tr_b16 v[198:199], v12 offset:6144\n\t"
    "v_exp_f32_e32 v79, v79\n\t"
    "v_cvt_pk_bf16_f32 v72, v72, v73\n\t"
    "v_add_f32_e32 v20, v20, v74\n\t"
    "v_add_f32_e32 v22, v22, v75\n\t"
    "v_mfma_f32_32x32x16_bf16 v[128:143], a[232:235], a[136:139], v[128:143]\n\t"
    "ds_read_b64_tr_b16 v[212:213], v12 offset:4608\n\t"
    "v_exp_f32_e32 v80, v80\n\t"
    "v_cvt_pk_bf16_f32 v73, v74, v75\n\t"
    "v_add_f32_e32 v20, v20, v76\n\t"
    "v_add_f32_e32 v22, v22, v77\n\t"
    "v_mfma_f32_32x32x16_bf16 v[144:159], a[232:235], a[168:171], v[144:159]\n\t"
    "ds_read_b64_tr_b16 v[214:215], v12 offset:6656\n\t"
    "v_exp_f32_e32 v81, v81\n\t"
    "v_cvt_pk_bf16_f32 v74, v76, v77\n\t"
    "v_add_f32_e32 v20, v20, v78\n\t"
    "v_add_f32_e32 v22, v22, v79\n\t"
    "v_mfma_f32_32x32x16_bf16 v[96:111], a[204:207], a[140:143], v[96:111]\n\t"
    "ds_read_b64_tr_b16 v[228:229], v12 offset:5120\n\t"
    "v_exp_f32_e32 v82, v82\n\t"
    "v_cvt_pk_bf16_f32 v75, v78, v79\n\t"
    "v_add_f32_e32 v21, v21, v80\n\t"
    "v_add_f32_e32 v23, v23, v81\n\t"
    "v_mfma_f32_32x32x16_bf16 v[112:127], a[204:207], a[172:175], v[112:127]\n\t"
    "ds_read_b64_tr_b16 v[230:231], v12 offset:7168\n\t"
    "v_exp_f32_e32 v83, v83\n\t"
    "v_cvt_pk_bf16_f32 v80, v80, v81\n\t"
    "v_add_f32_e32 v21, v21, v82\n\t"
    "v_cvt_pk_bf16_f32 v33, v34, v35\n\t"
    "v_mfma_f32_32x32x16_bf16 v[128:143], a[236:239], a[140:143], v[128:143]\n\t"
    "ds_read_b64_tr_b16 v[244:245], v12 offset:5632\n\t"
    "v_add_f32_e32 v23, v23, v83\n\t"
    "v_exp_f32_e32 v84, v84\n\t"
    "v_cvt_pk_bf16_f32 v81, v82, v83\n\t"
    "v_cvt_pk_bf16_f32 v34, v36, v37\n\t"
    "v_mfma_f32_32x32x16_bf16 v[144:159], a[236:239], a[172:175], v[144:159]\n\t"
    "ds_read_b64_tr_b16 v[246:247], v12 offset:7680\n\t"
    "v_add_f32_e32 v21, v21, v84\n\t"
    "v_exp_f32_e32 v85, v85\n\t"
    "v_exp_f32_e32 v86, v86\n\t"
    "v_mfma_f32_32x32x16_bf16 v[96:111], a[208:211], a[144:147], v[96:111]\n\t"
    "ds_read_b64_tr_b16 v[200:201], v12 offset:8192\n\t"
    "v_add_f32_e32 v23, v23, v85\n\t"
    "v_cvt_pk_bf16_f32 v82, v84, v85\n\t"
    "v_add_f32_e32 v21, v21, v86\n\t"
    "v_exp_f32_e32 v87, v87\n\t"
    "v_mfma_f32_32x32x16_bf16 v[112:127], a[208:211], a[176:179], v[112:127]\n\t"
    "ds_read_b64_tr_b16 v[202:203], v12 offset:10240\n\t"
    "v_add_f32_e32 v23, v23, v87\n\t"
    "v_cvt_pk_bf16_f32 v83, v86, v87\n\t"
    "v_exp_f32_e32 v88, v88\n\t"
    "v_cvt_pk_bf16_f32 v35, v38, v39\n\t"
    "v_mfma_f32_32x32x16_bf16 v[128:143], a[240:243], a[144:147], v[128:143]\n\t"
    "ds_read_b64_tr_b16 v[216:217], v12 offset:8704\n\t"
    "v_add_f32_e32 v21, v21, v88\n\t"
    "v_exp_f32_e32 v89, v89\n\t"
    "v_exp_f32_e32 v90, v90\n\t"
    "v_mfma_f32_32x32x16_bf16 v[144:159], a[240:243], a[176:179], v[144:159]\n\t"
    "ds_read_b64_tr_b16 v[218:219], v12 offset:10752\n\t"
    "v_add_f32_e32 v23, v23, v89\n\t"
    "v_cvt_pk_bf16_f32 v88, v88, v89\n\t"
    "v_add_f32_e32 v21, v21, v90\n\t"
    "v_exp_f32_e32 v91, v91\n\t"
    "v_mfma_f32_32x32x16_bf16 v[96:111], a[212:215], a[148:151], v[96:111]\n\t"
    "ds_read_b64_tr_b16 v[232:233], v12 offset:9216\n\t"
    "v_add_f32_e32 v23, v23, v91\n\t"
    "v_cvt_pk_bf16_f32 v89, v90, v91\n\t"
    "v_exp_f32_e32 v92, v92\n\t"
    "v_cvt_pk_bf16_f32 v40, v40, v41\n\t"
    "v_mfma_f32_32x32x16_bf16 v[112:127], a[212:215], a[180:183], v[112:127]\n\t"
    "ds_read_b64_tr_b16 v[234:235], v12 offset:11264\n\t"
    "v_add_f32_e32 v21, v21, v92\n\t"
    "v_exp_f32_e32 v93, v93\n\t"
    "v_exp_f32_e32 v94, v94\n\t"
    "v_mfma_f32_32x32x16_bf16 v[128:143], a[244:247], a[148:151], v[128:143]\n\t"
    "ds_read_b64_tr_b16 v[248:249], v12 offset:9728\n\t"
    "v_add_f32_e32 v23, v23, v93\n\t"
    "v_cvt_pk_bf16_f32 v90, v92, v93\n\t"
    "v_add_f32_e32 v21, v21, v94\n\t"
    "v_exp_f32_e32 v95, v95\n\t"
    "v_mfma_f32_32x32x16_bf16 v[144:159], a[244:247], a[180:183], v[144:159]\n\t"
    "ds_read_b64_tr_b16 v[250:251], v12 offset:11776\n\t"
    "v_add_f32_e32 v23, v23, v95\n\t"
    "v_cvt_pk_bf16_f32 v91, v94, v95\n\t"
    "v_cvt_pk_bf16_f32 v41, v42, v43\n\t"
    "v_cvt_pk_bf16_f32 v42, v44, v45\n\t"
    "v_cvt_pk_bf16_f32 v43, v46, v47\n\t"
    "v_mfma_f32_32x32x16_bf16 v[96:111], a[216:219], a[152:155], v[96:111]\n\t"
    "ds_read_b64_tr_b16 v[204:205], v12 offset:12288\n\t"
    "v_cvt_pk_bf16_f32 v48, v48, v49\n\t"
    "v_cvt_pk_bf16_f32 v49, v50, v51\n\t"
    "v_cvt_pk_bf16_f32 v50, v52, v53\n\t"
    "v_cvt_pk_bf16_f32 v51, v54, v55\n\t"
    "v_cvt_pk_bf16_f32 v56, v56, v57\n\t"
    "v_mfma_f32_32x32x16_bf16 v[112:127], a[216:219], a[184:187], v[112:127]\n\t"
    "ds_read_b64_tr_b16 v[206:207], v12 offset:14336\n\t"
    "v_cvt_pk_bf16_f32 v57, v58, v59\n\t"
    "v_cvt_pk_bf16_f32 v58, v60, v61\n\t"
    "v_cvt_pk_bf16_f32 v59, v62, v63\n\t"
    "v_mfma_f32_32x32x16_bf16 v[128:143], a[248:251], a[152:155], v[128:143]\n\t"
    "ds_read_b64_tr_b16 v[220:221], v12 offset:12800\n\t"
    "v_mfma_f32_32x32x16_bf16 v[144:159], a[248:251], a[184:187], v[144:159]\n\t"
    "ds_read_b64_tr_b16 v[222:223], v12 offset:14848\n\t"
    "v_mfma_f32_32x32x16_bf16 v[96:111], a[220:223], a[156:159], v[96:111]\n\t"
    "ds_read_b64_tr_b16 v[236:237], v12 offset:13312\n\t"
    "v_mfma_f32_32x32x16_bf16 v[112:127], a[220:223], a[188:191], v[112:127]\n\t"
    "ds_read_b64_tr_b16 v[238:239], v12 offset:15360\n\t"
    "v_mfma_f32_32x32x16_bf16 v[128:143], a[252:255], a[156:159], v[128:143]\n\t"
    "ds_read_b64_tr_b16 v[252:253], v12 offset:13824\n\t"
    "v_mfma_f32_32x32x16_bf16 v[144:159], a[252:255], a[188:191], v[144:159]\n\t"
    "ds_read_b64_tr_b16 v[254:255], v12 offset:15872\n\t"
    "s_waitcnt vmcnt(8) lgkmcnt(0)\n\t"
    "s_barrier\n\t"
    "v_mfma_f32_32x32x16_bf16 a[0:15], v[192:195], v[32:35], a[0:15]\n\t"
    "s_add_u32 m0, s42, 0\n\t"
    "s_nop 0\n\t"
    "buffer_load_dwordx4 v13, s[24:27], s40 offen offset:0 lds\n\t"
    "ds_read_b128 a[192:195], v8 offset:32768\n\t"
    "v_mfma_f32_32x32x16_bf16 a[16:31], v[192:195], v[48:51], a[16:31]\n\t"
    "ds_read_b128 a[196:199], v9 offset:32768\n\t"
    "v_mfma_f32_32x32x16_bf16 a[32:47], v[208:211], v[32:35], a[32:47]\n\t"
    "ds_read_b128 a[200:203], v10 offset:32768\n\t"
    "v_mfma_f32_32x32x16_bf16 a[48:63], v[208:211], v[48:51], a[48:63]\n\t"
    "ds_read_b128 a[204:207], v11 offset:32768\n\t"
    "v_max3_f32 v26, v96, v97, v128\n\t"
    "v_max3_f32 v28, v98, v99, v129\n\t"
    "v_max3_f32 v26, v26, v130, v131\n\t"
    "v_max3_f32 v26, v26, v100, v101\n\t"
    "v_max3_f32 v28, v28, v102, v103\n\t"
    "v_mfma_f32_32x32x16_bf16 a[64:79], v[224:227], v[32:35], a[64:79]\n\t"
    "buffer_load_dwordx4 v14, s[24:27], s40 offen offset:1024 lds\n\t"
    "ds_read_b128 a[208:211], v8 offset:32896\n\t"
    "v_max3_f32 v26, v26, v132, v133\n\t"
    "v_max3_f32 v28, v28, v134, v135\n\t"
    "v_max3_f32 v26, v26, v104, v105\n\t"
    "v_max3_f32 v28, v28, v106, v107\n\t"
    "v_mfma_f32_32x32x16_bf16 a[80:95], v[224:227], v[48:51], a[80:95]\n\t"
    "ds_read_b128 a[212:215], v9 offset:32896\n\t"
    "v_max3_f32 v26, v26, v136, v137\n\t"
    "v_max3_f32 v28, v28, v138, v139\n\t"
    "v_max3_f32 v26, v26, v108, v109\n\t"
    "v_max3_f32 v28, v28, v110, v111\n\t"
    "v_max3_f32 v26, v26, v140, v141\n\t"
    "v_mfma_f32_32x32x16_bf16 a[96:111], v[240:243], v[32:35], a[96:111]\n\t"
    "ds_read_b128 a[216:219], v10 offset:32896\n\t"
    "v_max3_f32 v28, v28, v142, v143\n\t"
    "v_max3_f32 v27, v112, v113, v144\n\t"
    "v_max3_f32 v29, v114, v115, v145\n\t"
    "v_max3_f32 v27, v27, v146, v147\n\t"
    "v_max3_f32 v27, v27, v116, v117\n\t"
    "v_mfma_f32_32x32x16_bf16 a[112:127], v[240:243], v[48:51], a[112:127]\n\t"
    "ds_read_b128 a[220:223], v11 offset:32896\n\t"
    "v_max3_f32 v29, v29, v118, v119\n\t"
    "v_max3_f32 v27, v27, v148, v149\n\t"
    "v_max3_f32 v29, v29, v150, v151\n\t"
    "v_max3_f32 v27, v27, v120, v121\n\t"
    "v_max3_f32 v29, v29, v122, v123\n\t"
    "v_mfma_f32_32x32x16_bf16 a[0:15], v[196:199], v[40:43], a[0:15]\n\t"
    "buffer_load_dwordx4 v13, s[24:27], s40 offen offset:2048 lds\n\t"
    "ds_read_b128 a[224:227], v8 offset:40960\n\t"
    "v_max3_f32 v27, v27, v152, v153\n\t"
    "v_max3_f32 v29, v29, v154, v155\n\t"
    "v_max3_f32 v27, v27, v124, v125\n\t"
    "v_max3_f32 v29, v29, v126, v127\n\t"
    "v_mfma_f32_32x32x16_bf16 a[16:31], v[196:199], v[56:59], a[16:31]\n\t"
    "ds_read_b128 a[228:231], v9 offset:40960\n\t"
    "v_max3_f32 v27, v27, v156, v157\n\t"
    "v_max3_f32 v29, v29, v158, v159\n\t"
    "v_mfma_f32_32x32x16_bf16 a[32:47], v[212:215], v[40:43], a[32:47]\n\t"
    "ds_read_b128 a[232:235], v10 offset:40960\n\t"
    "v_max_f32_e32 v30, v26, v28\n\t"
    "v_mov_b32_e32 v5, v30\n\t"
    "v_max_f32_e32 v31, v27, v29\n\t"
    "v_mov_b32_e32 v6, v31\n\t"
    "v_mfma_f32_32x32x16_bf16 a[48:63], v[212:215], v[56:59], a[48:63]\n\t"
    "ds_read_b128 a[236:239], v11 offset:40960\n\t"
    "v_permlane32_swap_b32_e32 v30, v5\n\t"
    "v_max_f32_e32 v30, v30, v5\n\t"
    "v_permlane32_swap_b32_e32 v31, v6\n\t"
    "v_max_f32_e32 v31, v31, v6\n\t"
    "v_mfma_f32_32x32x16_bf16 a[64:79], v[228:231], v[40:43], a[64:79]\n\t"
    "buffer_load_dwordx4 v14, s[24:27], s40 offen offset:3072 lds\n\t"
    "ds_read_b128 a[240:243], v8 offset:41088\n\t"
    "v_max_f32_e32 v7, v30, v31\n\t"
    "v_cmp_lt_f32_e32 vcc, s45, v7\n\t"
    "s_cbranch_vccnz L_RARE_l5\n\t"
    "L_RET_l5:\n\t"
    "v_mfma_f32_32x32x16_bf16 a[80:95], v[228:231], v[56:59], a[80:95]\n\t"
    "ds_read_b128 a[244:247], v9 offset:41088\n\t"
    "v_exp_f32_e32 v96, v96\n\t"
    "v_exp_f32_e32 v97, v97\n\t"
    "v_mfma_f32_32x32x16_bf16 a[96:111], v[244:247], v[40:43], a[96:111]\n\t"
    "ds_read_b128 a[248:251], v10 offset:41088\n\t"
    "v_add_f32_e32 v20, v20, v96\n\t"
    "v_add_f32_e32 v22, v22, v97\n\t"
    "v_exp_f32_e32 v98, v98\n\t"
    "v_mfma_f32_32x32x16_bf16 a[112:127], v[244:247], v[56:59], a[112:127]\n\t"
    "ds_read_b128 a[252:255], v11 offset:41088\n\t"
    "v_add_f32_e32 v20, v20, v98\n\t"
    "v_exp_f32_e32 v99, v99\n\t"
    "v_exp_f32_e32 v100, v100\n\t"
    "v_mfma_f32_32x32x16_bf16 a[0:15], v[200:203], v[64:67], a[0:15]\n\t"
    "s_add_u32 m0, s43, 49152\n\t"
    "s_nop 0\n\t"
    "buffer_load_dwordx4 v15, s[28:31], s41 offen offset:0 lds\n\t"
    "v_add_f32_e32 v22, v22, v99\n\t"
    "v_add_f32_e32 v20, v20, v100\n\t"
    "v_mfma_f32_32x32x16_bf16 a[16:31], v[200:203], v[80:83], a[16:31]\n\t"
    "v_exp_f32_e32 v101, v101\n\t"
    "v_exp_f32_e32 v102, v102\n\t"
    "v_mfma_f32_32x32x16_bf16 a[32:47], v[216:219], v[64:67], a[32:47]\n\t"
    "v_add_f32_e32 v22, v22, v101\n\t"
    "v_add_f32_e32 v20, v20, v102\n\t"
    "v_exp_f32_e32 v103, v103\n\t"
    "v_exp_f32_e32 v104, v104\n\t"
    "v_mfma_f32_32x32x16_bf16 a[48:63], v[216:219], v[80:83], a[48:63]\n\t"
    "v_add_f32_e32 v22, v22, v103\n\t"
    "v_add_f32_e32 v20, v20, v104\n\t"
    "v_exp_f32_e32 v105, v105\n\t"
    "v_exp_f32_e32 v106, v106\n\t"
    "v_mfma_f32_32x32x16_bf16 a[64:79], v[232:235], v[64:67], a[64:79]\n\t"
    "buffer_load_dwordx4 v15, s[28:31], s41 offen offset:2048 lds\n\t"
    "v_add_f32_e32 v22, v22, v105\n\t"
    "v_add_f32_e32 v20, v20, v106\n\t"
    "v_exp_f32_e32 v107, v107\n\t"
    "v_mfma_f32_32x32x16_bf16 a[80:95], v[232:235], v[80:83], a[80:95]\n\t"
    "v_add_f32_e32 v22, v22, v107\n\t"
    "v_exp_f32_e32 v108, v108\n\t"
    "v_exp_f32_e32 v109, v109\n\t"
    "v_mfma_f32_32x32x16_bf16 a[96:111], v[248:251], v[64:67], a[96:111]\n\t"
    "v_add_f32_e32 v20, v20, v108\n\t"
    "v_add_f32_e32 v22, v22, v109\n\t"
    "v_exp_f32_e32 v110, v110\n\t"
    "v_exp_f32_e32 v111, v111\n\t"
    "v_mfma_f32_32x32x16_bf16 a[112:127], v[248:251], v[80:83], a[112:127]\n\t"
    "v_add_f32_e32 v20, v20, v110\n\t"
    "v_add_f32_e32 v22, v22, v111\n\t"
    "v_exp_f32_e32 v112, v112\n\t"
    "v_exp_f32_e32 v113, v113\n\t"
    "v_mfma_f32_32x32x16_bf16 a[0:15], v[204:207], v[72:75], a[0:15]\n\t"
    "s_add_u32 m0, s43, 50048\n\t"
    "s_nop 0\n\t"
    "buffer_load_dwordx4 v15, s[28:31], s41 offen offset:128 lds\n\t"
    "v_add_f32_e32 v21, v21, v112\n\t"
    "v_add_f32_e32 v23, v23, v113\n\t"
    "v_mfma_f32_32x32x16_bf16 a[16:31], v[204:207], v[88:91], a[16:31]\n\t"
    "v_exp_f32_e32 v114, v114\n\t"
    "v_exp_f32_e32 v115, v115\n\t"
    "v_mfma_f32_32x32x16_bf16 a[32:47], v[220:223], v[72:75], a[32:47]\n\t"
    "v_add_f32_e32 v21, v21, v114\n\t"
    "v_add_f32_e32 v23, v23, v115\n\t"
    "v_exp_f32_e32 v116, v116\n\t"
    "v_exp_f32_e32 v117, v117\n\t"
    "v_mfma_f32_32x32x16_bf16 a[48:63], v[220:223], v[88:91], a[48:63]\n\t"
    "v_add_f32_e32 v21, v21, v116\n\t"
    "v_add_f32_e32 v23, v23, v117\n\t"
    "v_exp_f32_e32 v118, v118\n\t"
    "v_exp_f32_e32 v119, v119\n\t"
    "v_mfma_f32_32x32x16_bf16 a[64:79], v[236:239], v[72:75], a[64:79]\n\t"
    "buffer_load_dwordx4 v15, s[28:31], s41 offen offset:2176 lds\n\t"
    "v_add_f32_e32 v21, v21, v118\n\t"
    "v_add_f32_e32 v23, v23, v119\n\t"
    "v_exp_f32_e32 v120, v120\n\t"
    "v_mfma_f32_32x32x16_bf16 a[80:95], v[236:239], v[88:91], a[80:95]\n\t"
    "v_add_f32_e32 v21, v21, v120\n\t"
    "v_exp_f32_e32 v121, v121\n\t"
    "v_exp_f32_e32 v122, v122\n\t"
    "v_mfma_f32_32x32x16_bf16 a[96:111], v[252:255], v[72:75], a[96:111]\n\t"
    "v_add_f32_e32 v23, v23, v121\n\t"
    "v_add_f32_e32 v21, v21, v122\n\t"
    "v_exp_f32_e32 v123, v123\n\t"
    "v_exp_f32_e32 v124, v124\n\t"
    "v_mfma_f32_32x32x16_bf16 a[112:127], v[252:255], v[88:91], a[112:127]\n\t"
    "v_add_f32_e32 v23, v23, v123\n\t"
    "v_add_f32_e32 v21, v21, v124\n\t"
    "v_exp_f32_e32 v125, v125\n\t"
    "v_exp_f32_e32 v126, v126\n\t"
    "v_add_f32_e32 v23, v23, v125\n\t"
    "v_add_f32_e32 v21, v21, v126\n\t"
    "v_exp_f32_e32 v127, v127\n\t"
    "s_nop 0\n\t"
    "v_add_f32_e32 v23, v23, v127\n\t"
    "s_add_u32 s40, s40, 16384\n\t"
    "s_add_u32 s41, s41, 16384\n\t"
    "s_cmp_lg_u32 s47, 0\n\t"
    "s_cbranch_scc1 L_RESC_l5\n\t"
    "L_RRET_l5:\n\t"
    "s_waitcnt lgkmcnt(0)\n\t"
    "v_mfma_f32_32x32x16_bf16 v[32:47], a[192:195], a[128:131], v[160:175]\n\t"
    "ds_read_b64_tr_b16 v[192:193], v12 offset:16384\n\t"
    "v_exp_f32_e32 v128, v128\n\t"
    "v_exp_f32_e32 v129, v129\n\t"
    "v_cvt_pk_bf16_f32 v96, v96, v97\n\t"
    "v_mfma_f32_32x32x16_bf16 v[48:63], a[192:195], a[160:163], v[176:191]\n\t"
    "ds_read_b64_tr_b16 v[194:195], v12 offset:18432\n\t"
    "v_exp_f32_e32 v130, v130\n\t"
    "v_exp_f32_e32 v131, v131\n\t"
    "v_add_f32_e32 v20, v20, v128\n\t"
    "v_mfma_f32_32x32x16_bf16 v[64:79], a[224:227], a[128:131], v[160:175]\n\t"
    "ds_read_b64_tr_b16 v[208:209], v12 offset:16896\n\t"
    "v_exp_f32_e32 v132, v132\n\t"
    "v_exp_f32_e32 v133, v133\n\t"
    "v_add_f32_e32 v22, v22, v129\n\t"
    "v_mfma_f32_32x32x16_bf16 v[80:95], a[224:227], a[160:163], v[176:191]\n\t"
    "ds_read_b64_tr_b16 v[210:211], v12 offset:18944\n\t"
    "v_exp_f32_e32 v134, v134\n\t"
    "v_exp_f32_e32 v135, v135\n\t"
    "v_cvt_pk_bf16_f32 v128, v128, v129\n\t"
    "v_mfma_f32_32x32x16_bf16 v[32:47], a[196:199], a[132:135], v[32:47]\n\t"
    "ds_read_b64_tr_b16 v[224:225], v12 offset:17408\n\t"
    "v_exp_f32_e32 v136, v136\n\t"
    "v_exp_f32_e32 v137, v137\n\t"
    "v_add_f32_e32 v20, v20, v130\n\t"
    "v_mfma_f32_32x32x16_bf16 v[48:63], a[196:199], a[164:167], v[48:63]\n\t"
    "ds_read_b64_tr_b16 v[226:227], v12 offset:19456\n\t"
    "v_exp_f32_e32 v138, v138\n\t"
    "v_exp_f32_e32 v139, v139\n\t"
    "v_add_f32_e32 v22, v22, v131\n\t"
    "v_mfma_f32_32x32x16_bf16 v[64:79], a[228:231], a[132:135], v[64:79]\n\t"
    "ds_read_b64_tr_b16 v[240:241], v12 offset:17920\n\t"
    "v_exp_f32_e32 v140, v140\n\t"
    "v_cvt_pk_bf16_f32 v129, v130, v131\n\t"
    "v_add_f32_e32 v20, v20, v132\n\t"
    "v_add_f32_e32 v22, v22, v133\n\t"
    "v_mfma_f32_32x32x16_bf16 v[80:95], a[228:231], a[164:167], v[80:95]\n\t"
    "ds_read_b64_tr_b16 v[242:243], v12 offset:19968\n\t"
    "v_exp_f32_e32 v141, v141\n\t"
    "v_cvt_pk_bf16_f32 v130, v132, v133\n\t"
    "v_add_f32_e32 v20, v20, v134\n\t"
    "v_add_f32_e32 v22, v22, v135\n\t"
    "v_mfma_f32_32x32x16_bf16 v[32:47], a[200:203], a[136:139], v[32:47]\n\t"
    "ds_read_b64_tr_b16 v[196:197], v12 offset:20480\n\t"
    "v_exp_f32_e32 v142, v142\n\t"
    "v_cvt_pk_bf16_f32 v131, v134, v135\n\t"
    "v_add_f32_e32 v20, v20, v136\n\t"
    "v_add_f32_e32 v22, v22, v137\n\t"
    "v_mfma_f32_32x32x16_bf16 v[48:63], a[200:203], a[168:171], v[48:63]\n\t"
    "ds_read_b64_tr_b16 v[198:199], v12 offset:22528\n\t"
    "v_exp_f32_e32 v143, v143\n\t"
    "v_cvt_pk_bf16_f32 v136, v136, v137\n\t"
    "v_add_f32_e32 v20, v20, v138\n\t"
    "v_add_f32_e32 v22, v22, v139\n\t"
    "v_mfma_f32_32x32x16_bf16 v[64:79], a[232:235], a[136:139], v[64:79]\n\t"
    "ds_read_b64_tr_b16 v[212:213], v12 offset:20992\n\t"
    "v_exp_f32_e32 v144, v144\n\t"
    "v_cvt_pk_bf16_f32 v137, v138, v139\n\t"
    "v_add_f32_e32 v20, v20, v140\n\t"
    "v_add_f32_e32 v22, v22, v141\n\t"
    "v_mfma_f32_32x32x16_bf16 v[80:95], a[232:235], a[168:171], v[80:95]\n\t"
    "ds_read_b64_tr_b16 v[214:215], v12 offset:23040\n\t"
    "v_exp_f32_e32 v145, v145\n\t"
    "v_cvt_pk_bf16_f32 v138, v140, v141\n\t"
    "v_add_f32_e32 v20, v20, v142\n\t"
    "v_add_f32_e32 v22, v22, v143\n\t"
    "v_mfma_f32_32x32x16_bf16 v[32:47], a[204:207], a[140:143], v[32:47]\n\t"
    "ds_read_b64_tr_b16 v[228:229], v12 offset:21504\n\t"
    "v_exp_f32_e32 v146, v146\n\t"
    "v_cvt_pk_bf16_f32 v139, v142, v143\n\t"
    "v_add_f32_e32 v21, v21, v144\n\t"
    "v_add_f32_e32 v23, v23, v145\n\t"
    "v_mfma_f32_32x32x16_bf16 v[48:63], a[204:207], a[172:175], v[48:63]\n\t"
    "ds_read_b64_tr_b16 v[230:231], v12 offset:23552\n\t"
    "v_exp_f32_e32 v147, v147\n\t"
    "v_cvt_pk_bf16_f32 v144, v144, v145\n\t"
    "v_add_f32_e32 v21, v21, v146\n\t"
    "v_cvt_pk_bf16_f32 v97, v98, v99\n\t"
    "v_mfma_f32_32x32x16_bf16 v[64:79], a[236:239], a[140:143], v[64:79]\n\t"
    "ds_read_b64_tr_b16 v[244:245], v12 offset:22016\n\t"
    "v_add_f32_e32 v23, v23, v147\n\t"
    "v_exp_f32_e32 v148, v148\n\t"
    "v_cvt_pk_bf16_f32 v145, v146, v147\n\t"
    "v_cvt_pk_bf16_f32 v98, v100, v101\n\t"
    "v_mfma_f32_32x32x16_bf16 v[80:95], a[236:239], a[172:175], v[80:95]\n\t"
    "ds_read_b64_tr_b16 v[246:247], v12 offset:24064\n\t"
    "v_add_f32_e32 v21, v21, v148\n\t"
    "v_exp_f32_e32 v149, v149\n\t"
    "v_exp_f32_e32 v150, v150\n\t"
    "v_mfma_f32_32x32x16_bf16 v[32:47], a[208:211], a[144:147], v[32:47]\n\t"
    "ds_read_b64_tr_b16 v[200:201], v12 offset:24576\n\t"
    "v_add_f32_e32 v23, v23, v149\n\t"
    "v_cvt_pk_bf16_f32 v146, v148, v149\n\t"
    "v_add_f32_e32 v21, v21, v150\n\t"
    "v_exp_f32_e32 v151, v151\n\t"
    "v_mfma_f32_32x32x16_bf16 v[48:63], a[208:211], a[176:179], v[48:63]\n\t"
    "ds_read_b64_tr_b16 v[202:203], v12 offset:26624\n\t"
    "v_add_f32_e32 v23, v23, v151\n\t"
    "v_cvt_pk_bf16_f32 v147, v150, v151\n\t"
    "v_exp_f32_e32 v152, v152\n\t"
    "v_cvt_pk_bf16_f32 v99, v102, v103\n\t"
    "v_mfma_f32_32x32x16_bf16 v[64:79], a[240:243], a[144:147], v[64:79]\n\t"
    "ds_read_b64_tr_b16 v[216:217], v12 offset:25088\n\t"
    "v_add_f32_e32 v21, v21, v152\n\t"
    "v_exp_f32_e32 v153, v153\n\t"
    "v_exp_f32_e32 v154, v154\n\t"
    "v_mfma_f32_32x32x16_bf16 v[80:95], a[240:243], a[176:179], v[80:95]\n\t"
    "ds_read_b64_tr_b16 v[218:219], v12 offset:27136\n\t"
    "v_add_f32_e32 v23, v23, v153\n\t"
    "v_cvt_pk_bf16_f32 v152, v152, v153\n\t"
    "v_add_f32_e32 v21, v21, v154\n\t"
    "v_exp_f32_e32 v155, v155\n\t"
    "v_mfma_f32_32x32x16_bf16 v[32:47], a[212:215], a[148:151], v[32:47]\n\t"
    "ds_read_b64_tr_b16 v[232:233], v12 offset:25600\n\t"
    "v_add_f32_e32 v23, v23, v155\n\t"
    "v_cvt_pk_bf16_f32 v153, v154, v155\n\t"
    "v_exp_f32_e32 v156, v156\n\t"
    "v_cvt_pk_bf16_f32 v104, v104, v105\n\t"
    "v_mfma_f32_32x32x16_bf16 v[48:63], a[212:215], a[180:183], v[48:63]\n\t"
    "ds_read_b64_tr_b16 v[234:235], v12 offset:27648\n\t"
    "v_add_f32_e32 v21, v21, v156\n\t"
    "v_exp_f32_e32 v157, v157\n\t"
    "v_exp_f32_e32 v158, v158\n\t"
    "v_mfma_f32_32x32x16_bf16 v[64:79], a[244:247], a[148:151], v[64:79]\n\t"
    "ds_read_b64_tr_b16 v[248:249], v12 offset:26112\n\t"
    "v_add_f32_e32 v23, v23, v157\n\t"
    "v_cvt_pk_bf16_f32 v154, v156, v157\n\t"
    "v_add_f32_e32 v21, v21, v158\n\t"
    "v_exp_f32_e32 v159, v159\n\t"
    "v_mfma_f32_32x32x16_bf16 v[80:95], a[244:247], a[180:183], v[80:95]\n\t"
    "ds_read_b64_tr_b16 v[250:251], v12 offset:28160\n\t"
    "v_add_f32_e32 v23, v23, v159\n\t"
    "v_cvt_pk_bf16_f32 v155, v158, v159\n\t"
    "v_cvt_pk_bf16_f32 v105, v106, v107\n\t"
    "v_cvt_pk_bf16_f32 v106, v108, v109\n\t"
    "v_cvt_pk_bf16_f32 v107, v110, v111\n\t"
    "v_mfma_f32_32x32x16_bf16 v[32:47], a[216:219], a[152:155], v[32:47]\n\t"
    "ds_read_b64_tr_b16 v[204:205], v12 offset:28672\n\t"
    "v_cvt_pk_bf16_f32 v112, v112, v113\n\t"
    "v_cvt_pk_bf16_f32 v113, v114, v115\n\t"
    "v_cvt_pk_bf16_f32 v114, v116, v117\n\t"
    "v_cvt_pk_bf16_f32 v115, v118, v119\n\t"
    "v_cvt_pk_bf16_f32 v120, v120, v121\n\t"
    "v_mfma_f32_32x32x16_bf16 v[48:63], a[216:219], a[184:187], v[48:63]\n\t"
    "ds_read_b64_tr_b16 v[206:207], v12 offset:30720\n\t"
    "v_cvt_pk_bf16_f32 v121, v122, v123\n\t"
    "v_cvt_pk_bf16_f32 v122, v124, v125\n\t"
    "v_cvt_pk_bf16_f32 v123, v126, v127\n\t"
    "v_mfma_f32_32x32x16_bf16 v[64:79], a[248:251], a[152:155], v[64:79]\n\t"
    "ds_read_b64_tr_b16 v[220:221], v12 offset:29184\n\t"
    "v_mfma_f32_32x32x16_bf16 v[80:95], a[248:251], a[184:187], v[80:95]\n\t"
    "ds_read_b64_tr_b16 v[222:223], v12 offset:31232\n\t"
    "v_mfma_f32_32x32x16_bf16 v[32:47], a[220:223], a[156:159], v[32:47]\n\t"
    "ds_read_b64_tr_b16 v[236:237], v12 offset:29696\n\t"
    "v_mfma_f32_32x32x16_bf16 v[48:63], a[220:223], a[188:191], v[48:63]\n\t"
    "ds_read_b64_tr_b16 v[238:239], v12 offset:31744\n\t"
    "v_mfma_f32_32x32x16_bf16 v[64:79], a[252:255], a[156:159], v[64:79]\n\t"
    "ds_read_b64_tr_b16 v[252:253], v12 offset:30208\n\t"
    "v_mfma_f32_32x32x16_bf16 v[80:95], a[252:255], a[188:191], v[80:95]\n\t"
    "ds_read_b64_tr_b16 v[254:255], v12 offset:32256\n\t"
    "s_waitcnt vmcnt(8) lgkmcnt(0)\n\t"
    "s_barrier\n\t"
    "v_mfma_f32_32x32x16_bf16 a[0:15], v[192:195], v[96:99], a[0:15]\n\t"
    "s_add_u32 m0, s42, 16384\n\t"
    "s_nop 0\n\t"
    "buffer_load_dwordx4 v13, s[24:27], s40 offen offset:0 lds\n\t"
    "ds_read_b128 a[192:195], v8 offset:49152\n\t"
    "v_mfma_f32_32x32x16_bf16 a[16:31], v[192:195], v[112:115], a[16:31]\n\t"
    "ds_read_b128 a[196:199], v9 offset:49152\n\t"
    "v_mfma_f32_32x32x16_bf16 a[32:47], v[208:211], v[96:99], a[32:47]\n\t"
    "ds_read_b128 a[200:203], v10 offset:49152\n\t"
    "v_mfma_f32_32x32x16_bf16 a[48:63], v[208:211], v[112:115], a[48:63]\n\t"
    "ds_read_b128 a[204:207], v11 offset:49152\n\t"
    "v_max3_f32 v26, v32, v33, v64\n\t"
    "v_max3_f32 v28, v34, v35, v65\n\t"
    "v_max3_f32 v26, v26, v66, v67\n\t"
    "v_max3_f32 v26, v26, v36, v37\n\t"
    "v_max3_f32 v28, v28, v38, v39\n\t"
    "v_mfma_f32_32x32x16_bf16 a[64:79], v[224:227], v[96:99], a[64:79]\n\t"
    "buffer_load_dwordx4 v14, s[24:27], s40 offen offset:1024 lds\n\t"
    "ds_read_b128 a[208:211], v8 offset:49280\n\t"
    "v_max3_f32 v26, v26, v68, v69\n\t"
    "v_max3_f32 v28, v28, v70, v71\n\t"
    "v_max3_f32 v26, v26, v40, v41\n\t"
    "v_max3_f32 v28, v28, v42, v43\n\t"
    "v_mfma_f32_32x32x16_bf16 a[80:95], v[224:227], v[112:115], a[80:95]\n\t"
    "ds_read_b128 a[212:215], v9 offset:49280\n\t"
    "v_max3_f32 v26, v26, v72, v73\n\t"
    "v_max3_f32 v28, v28, v74, v75\n\t"
    "v_max3_f32 v26, v26, v44, v45\n\t"
    "v_max3_f32 v28, v28, v46, v47\n\t"
    "v_max3_f32 v26, v26, v76, v77\n\t"
    "v_mfma_f32_32x32x16_bf16 a[96:111], v[240:243], v[96:99], a[96:111]\n\t"
    "ds_read_b128 a[216:219], v10 offset:49280\n\t"
    "v_max3_f32 v28, v28, v78, v79\n\t"
    "v_max3_f32 v27, v48, v49, v80\n\t"
    "v_max3_f32 v29, v50, v51, v81\n\t"
    "v_max3_f32 v27, v27, v82, v83\n\t"
    "v_max3_f32 v27, v27, v52, v53\n\t"
    "v_mfma_f32_32x32x16_bf16 a[112:127], v[240:243], v[112:115], a[112:127]\n\t"
    "ds_read_b128 a[220:223], v11 offset:49280\n\t"
    "v_max3_f32 v29, v29, v54, v55\n\t"
    "v_max3_f32 v27, v27, v84, v85\n\t"
    "v_max3_f32 v29, v29, v86, v87\n\t"
    "v_max3_f32 v27, v27, v56, v57\n\t"
    "v_max3_f32 v29, v29, v58, v59\n\t"
    "v_mfma_f32_32x32x16_bf16 a[0:15], v[196:199], v[104:107], a[0:15]\n\t"
    "buffer_load_dwordx4 v13, s[24:27], s40 offen offset:2048 lds\n\t"
    "ds_read_b128 a[224:227], v8 offset:57344\n\t"
    "v_max3_f32 v27, v27, v88, v89\n\t"
    "v_max3_f32 v29, v29, v90, v91\n\t"
    "v_max3_f32 v27, v27, v60, v61\n\t"
    "v_max3_f32 v29, v29, v62, v63\n\t"
    "v_mfma_f32_32x32x16_bf16 a[16:31], v[196:199], v[120:123], a[16:31]\n\t"
    "ds_read_b128 a[228:231], v9 offset:57344\n\t"
    "v_max3_f32 v27, v27, v92, v93\n\t"
    "v_max3_f32 v29, v29, v94, v95\n\t"
    "v_mfma_f32_32x32x16_bf16 a[32:47], v[212:215], v[104:107], a[32:47]\n\t"
    "ds_read_b128 a[232:235], v10 offset:57344\n\t"
    "v_max_f32_e32 v30, v26, v28\n\t"
    "v_mov_b32_e32 v5, v30\n\t"
    "v_max_f32_e32 v31, v27, v29\n\t"
    "v_mov_b32_e32 v6, v31\n\t"
    "v_mfma_f32_32x32x16_bf16 a[48:63], v[212:215], v[120:123], a[48:63]\n\t"
    "ds_read_b128 a[236:239], v11 offset:57344\n\t"
    "v_permlane32_swap_b32_e32 v30, v5\n\t"
    "v_max_f32_e32 v30, v30, v5\n\t"
    "v_permlane32_swap_b32_e32 v31, v6\n\t"
    "v_max_f32_e32 v31, v31, v6\n\t"
    "v_mfma_f32_32x32x16_bf16 a[64:79], v[228:231], v[104:107], a[64:79]\n\t"
    "buffer_load_dwordx4 v14, s[24:27], s40 offen offset:3072 lds\n\t"
    "ds_read_b128 a[240:243], v8 offset:57472\n\t"
    "v_max_f32_e32 v7, v30, v31\n\t"
    "v_cmp_lt_f32_e32 vcc, s45, v7\n\t"
    "s_cbranch_vccnz L_RARE_l6\n\t"
    "L_RET_l6:\n\t"
    "v_mfma_f32_32x32x16_bf16 a[80:95], v[228:231], v[120:123], a[80:95]\n\t"
    "ds_read_b128 a[244:247], v9 offset:57472\n\t"
    "v_exp_f32_e32 v32, v32\n\t"
    "v_exp_f32_e32 v33, v33\n\t"
    "v_mfma_f32_32x32x16_bf16 a[96:111], v[244:247], v[104:107], a[96:111]\n\t"
    "ds_read_b128 a[248:251], v10 offset:57472\n\t"
    "v_add_f32_e32 v20, v20, v32\n\t"
    "v_add_f32_e32 v22, v22, v33\n\t"
    "v_exp_f32_e32 v34, v34\n\t"
    "v_mfma_f32_32x32x16_bf16 a[112:127], v[244:247], v[120:123], a[112:127]\n\t"
    "ds_read_b128 a[252:255], v11 offset:57472\n\t"
    "v_add_f32_e32 v20, v20, v34\n\t"
    "v_exp_f32_e32 v35, v35\n\t"
    "v_exp_f32_e32 v36, v36\n\t"
    "v_mfma_f32_32x32x16_bf16 a[0:15], v[200:203], v[128:131], a[0:15]\n\t"
    "s_add_u32 m0, s43, 0\n\t"
    "s_nop 0\n\t"
    "buffer_load_dwordx4 v15, s[28:31], s41 offen offset:0 lds\n\t"
    "v_add_f32_e32 v22, v22, v35\n\t"
    "v_add_f32_e32 v20, v20, v36\n\t"
    "v_mfma_f32_32x32x16_bf16 a[16:31], v[200:203], v[144:147], a[16:31]\n\t"
    "v_exp_f32_e32 v37, v37\n\t"
    "v_exp_f32_e32 v38, v38\n\t"
    "v_mfma_f32_32x32x16_bf16 a[32:47], v[216:219], v[128:131], a[32:47]\n\t"
    "v_add_f32_e32 v22, v22, v37\n\t"
    "v_add_f32_e32 v20, v20, v38\n\t"
    "v_exp_f32_e32 v39, v39\n\t"
    "v_exp_f32_e32 v40, v40\n\t"
    "v_mfma_f32_32x32x16_bf16 a[48:63], v[216:219], v[144:147], a[48:63]\n\t"
    "v_add_f32_e32 v22, v22, v39\n\t"
    "v_add_f32_e32 v20, v20, v40\n\t"
    "v_exp_f32_e32 v41, v41\n\t"
    "v_exp_f32_e32 v42, v42\n\t"
    "v_mfma_f32_32x32x16_bf16 a[64:79], v[232:235], v[128:131], a[64:79]\n\t"
    "buffer_load_dwordx4 v15, s[28:31], s41 offen offset:2048 lds\n\t"
    "v_add_f32_e32 v22, v22, v41\n\t"
    "v_add_f32_e32 v20, v20, v42\n\t"
    "v_exp_f32_e32 v43, v43\n\t"
    "v_mfma_f32_32x32x16_bf16 a[80:95], v[232:235], v[144:147], a[80:95]\n\t"
    "v_add_f32_e32 v22, v22, v43\n\t"
    "v_exp_f32_e32 v44, v44\n\t"
    "v_exp_f32_e32 v45, v45\n\t"
    "v_mfma_f32_32x32x16_bf16 a[96:111], v[248:251], v[128:131], a[96:111]\n\t"
    "v_add_f32_e32 v20, v20, v44\n\t"
    "v_add_f32_e32 v22, v22, v45\n\t"
    "v_exp_f32_e32 v46, v46\n\t"
    "v_exp_f32_e32 v47, v47\n\t"
    "v_mfma_f32_32x32x16_bf16 a[112:127], v[248:251], v[144:147], a[112:127]\n\t"
    "v_add_f32_e32 v20, v20, v46\n\t"
    "v_add_f32_e32 v22, v22, v47\n\t"
    "v_exp_f32_e32 v48, v48\n\t"
    "v_exp_f32_e32 v49, v49\n\t"
    "v_mfma_f32_32x32x16_bf16 a[0:15], v[204:207], v[136:139], a[0:15]\n\t"
    "s_add_u32 m0, s43, 896\n\t"
    "s_nop 0\n\t"
    "buffer_load_dwordx4 v15, s[28:31], s41 offen offset:128 lds\n\t"
    "v_add_f32_e32 v21, v21, v48\n\t"
    "v_add_f32_e32 v23, v23, v49\n\t"
    "v_mfma_f32_32x32x16_bf16 a[16:31], v[204:207], v[152:155], a[16:31]\n\t"
    "v_exp_f32_e32 v50, v50\n\t"
    "v_exp_f32_e32 v51, v51\n\t"
    "v_mfma_f32_32x32x16_bf16 a[32:47], v[220:223], v[136:139], a[32:47]\n\t"
    "v_add_f32_e32 v21, v21, v50\n\t"
    "v_add_f32_e32 v23, v23, v51\n\t"
    "v_exp_f32_e32 v52, v52\n\t"
    "v_exp_f32_e32 v53, v53\n\t"
    "v_mfma_f32_32x32x16_bf16 a[48:63], v[220:223], v[152:155], a[48:63]\n\t"
    "v_add_f32_e32 v21, v21, v52\n\t"
    "v_add_f32_e32 v23, v23, v53\n\t"
    "v_exp_f32_e32 v54, v54\n\t"
    "v_exp_f32_e32 v55, v55\n\t"
    "v_mfma_f32_32x32x16_bf16 a[64:79], v[236:239], v[136:139], a[64:79]\n\t"
    "buffer_load_dwordx4 v15, s[28:31], s41 offen offset:2176 lds\n\t"
    "v_add_f32_e32 v21, v21, v54\n\t"
    "v_add_f32_e32 v23, v23, v55\n\t"
    "v_exp_f32_e32 v56, v56\n\t"
    "v_mfma_f32_32x32x16_bf16 a[80:95], v[236:239], v[152:155], a[80:95]\n\t"
    "v_add_f32_e32 v21, v21, v56\n\t"
    "v_exp_f32_e32 v57, v57\n\t"
    "v_exp_f32_e32 v58, v58\n\t"
    "v_mfma_f32_32x32x16_bf16 a[96:111], v[252:255], v[136:139], a[96:111]\n\t"
    "v_add_f32_e32 v23, v23, v57\n\t"
    "v_add_f32_e32 v21, v21, v58\n\t"
    "v_exp_f32_e32 v59, v59\n\t"
    "v_exp_f32_e32 v60, v60\n\t"
    "v_mfma_f32_32x32x16_bf16 a[112:127], v[252:255], v[152:155], a[112:127]\n\t"
    "v_add_f32_e32 v23, v23, v59\n\t"
    "v_add_f32_e32 v21, v21, v60\n\t"
    "v_exp_f32_e32 v61, v61\n\t"
    "v_exp_f32_e32 v62, v62\n\t"
    "v_add_f32_e32 v23, v23, v61\n\t"
    "v_add_f32_e32 v21, v21, v62\n\t"
    "v_exp_f32_e32 v63, v63\n\t"
    "s_nop 0\n\t"
    "v_add_f32_e32 v23, v23, v63\n\t"
    "s_add_u32 s40, s40, 16384\n\t"
    "s_add_u32 s41, s41, 16384\n\t"
    "s_cmp_lg_u32 s47, 0\n\t"
    "s_cbranch_scc1 L_RESC_l6\n\t"
    "L_RRET_l6:\n\t"
    "s_waitcnt lgkmcnt(0)\n\t"
    "v_mfma_f32_32x32x16_bf16 v[96:111], a[192:195], a[128:131], v[160:175]\n\t"
    "ds_read_b64_tr_b16 v[192:193], v12 offset:32768\n\t"
    "v_exp_f32_e32 v64, v64\n\t"
    "v_exp_f32_e32 v65, v65\n\t"
    "v_cvt_pk_bf16_f32 v32, v32, v33\n\t"
    "v_mfma_f32_32x32x16_bf16 v[112:127], a[192:195], a[160:163], v[176:191]\n\t"
    "ds_read_b64_tr_b16 v[194:195], v12 offset:34816\n\t"
    "v_exp_f32_e32 v66, v66\n\t"
    "v_exp_f32_e32 v67, v67\n\t"
    "v_add_f32_e32 v20, v20, v64\n\t"
    "v_mfma_f32_32x32x16_bf16 v[128:143], a[224:227], a[128:131], v[160:175]\n\t"
    "ds_read_b64_tr_b16 v[208:209], v12 offset:33280\n\t"
    "v_exp_f32_e32 v68, v68\n\t"
    "v_exp_f32_e32 v69, v69\n\t"
    "v_add_f32_e32 v22, v22, v65\n\t"
    "v_mfma_f32_32x32x16_bf16 v[144:159], a[224:227], a[160:163], v[176:191]\n\t"
    "ds_read_b64_tr_b16 v[210:211], v12 offset:35328\n\t"
    "v_exp_f32_e32 v70, v70\n\t"
    "v_exp_f32_e32 v71, v71\n\t"
    "v_cvt_pk_bf16_f32 v64, v64, v65\n\t"
    "v_mfma_f32_32x32x16_bf16 v[96:111], a[196:199], a[132:135], v[96:111]\n\t"
    "ds_read_b64_tr_b16 v[224:225], v12 offset:33792\n\t"
    "v_exp_f32_e32 v72, v72\n\t"
    "v_exp_f32_e32 v73, v73\n\t"
    "v_add_f32_e32 v20, v20, v66\n\t"
    "v_mfma_f32_32x32x16_bf16 v[112:127], a[196:199], a[164:167], v[112:127]\n\t"
    "ds_read_b64_tr_b16 v[226:227], v12 offset:35840\n\t"
    "v_exp_f32_e32 v74, v74\n\t"
    "v_exp_f32_e32 v75, v75\n\t"
    "v_add_f32_e32 v22, v22, v67\n\t"
    "v_mfma_f32_32x32x16_bf16 v[128:143], a[228:231], a[132:135], v[128:143]\n\t"
    "ds_read_b64_tr_b16 v[240:241], v12 offset:34304\n\t"
    "v_exp_f32_e32 v76, v76\n\t"
    "v_cvt_pk_bf16_f32 v65, v66, v67\n\t"
    "v_add_f32_e32 v20, v20, v68\n\t"
    "v_add_f32_e32 v22, v22, v69\n\t"
    "v_mfma_f32_32x32x16_bf16 v[144:159], a[228:231], a[164:167], v[144:159]\n\t"
    "ds_read_b64_tr_b16 v[242:243], v12 offset:36352\n\t"
    "v_exp_f32_e32 v77, v77\n\t"
    "v_cvt_pk_bf16_f32 v66, v68, v69\n\t"
    "v_add_f32_e32 v20, v20, v70\n\t"
    "v_add_f32_e32 v22, v22, v71\n\t"
    "v_mfma_f32_32x32x16_bf16 v[96:111], a[200:203], a[136:139], v[96:111]\n\t"
    "ds_read_b64_tr_b16 v[196:197], v12 offset:36864\n\t"
    "v_exp_f32_e32 v78, v78\n\t"
    "v_cvt_pk_bf16_f32 v67, v70, v71\n\t"
    "v_add_f32_e32 v20, v20, v72\n\t"
    "v_add_f32_e32 v22, v22, v73\n\t"
    "v_mfma_f32_32x32x16_bf16 v[112:127], a[200:203], a[168:171], v[112:127]\n\t"
    "ds_read_b64_tr_b16 v[198:199], v12 offset:38912\n\t"
    "v_exp_f32_e32 v79, v79\n\t"
    "v_cvt_pk_bf16_f32 v72, v72, v73\n\t"
    "v_add_f32_e32 v20, v20, v74\n\t"
    "v_add_f32_e32 v22, v22, v75\n\t"
    "v_mfma_f32_32x32x16_bf16 v[128:143], a[232:235], a[136:139], v[128:143]\n\t"
    "ds_read_b64_tr_b16 v[212:213], v12 offset:37376\n\t"
    "v_exp_f32_e32 v80, v80\n\t"
    "v_cvt_pk_bf16_f32 v73, v74, v75\n\t"
    "v_add_f32_e32 v20, v20, v76\n\t"
    "v_add_f32_e32 v22, v22, v77\n\t"
    "v_mfma_f32_32x32x16_bf16 v[144:159], a[232:235], a[168:171], v[144:159]\n\t"
    "ds_read_b64_tr_b16 v[214:215], v12 offset:39424\n\t"
    "v_exp_f32_e32 v81, v81\n\t"
    "v_cvt_pk_bf16_f32 v74, v76, v77\n\t"
    "v_add_f32_e32 v20, v20, v78\n\t"
    "v_add_f32_e32 v22, v22, v79\n\t"
    "v_mfma_f32_32x32x16_bf16 v[96:111], a[204:207], a[140:143], v[96:111]\n\t"
    "ds_read_b64_tr_b16 v[228:229], v12 offset:37888\n\t"
    "v_exp_f32_e32 v82, v82\n\t"
    "v_cvt_pk_bf16_f32 v75, v78, v79\n\t"
    "v_add_f32_e32 v21, v21, v80\n\t"
    "v_add_f32_e32 v23, v23, v81\n\t"
    "v_mfma_f32_32x32x16_bf16 v[112:127], a[204:207], a[172:175], v[112:127]\n\t"
    "ds_read_b64_tr_b16 v[230:231], v12 offset:39936\n\t"
    "v_exp_f32_e32 v83, v83\n\t"
    "v_cvt_pk_bf16_f32 v80, v80, v81\n\t"
    "v_add_f32_e32 v21, v21, v82\n\t"
    "v_cvt_pk_bf16_f32 v33, v34, v35\n\t"
    "v_mfma_f32_32x32x16_bf16 v[128:143], a[236:239], a[140:143], v[128:143]\n\t"
    "ds_read_b64_tr_b16 v[244:245], v12 offset:38400\n\t"
    "v_add_f32_e32 v23, v23, v83\n\t"
    "v_exp_f32_e32 v84, v84\n\t"
    "v_cvt_pk_bf16_f32 v81, v82, v83\n\t"
    "v_cvt_pk_bf16_f32 v34, v36, v37\n\t"
    "v_mfma_f32_32x32x16_bf16 v[144:159], a[236:239], a[172:175], v[144:159]\n\t"
    "ds_read_b64_tr_b16 v[246:247], v12 offset:40448\n\t"
    "v_add_f32_e32 v21, v21, v84\n\t"
    "v_exp_f32_e32 v85, v85\n\t"
    "v_exp_f32_e32 v86, v86\n\t"
    "v_mfma_f32_32x32x16_bf16 v[96:111], a[208:211], a[144:147], v[96:111]\n\t"
    "ds_read_b64_tr_b16 v[200:201], v12 offset:40960\n\t"
    "v_add_f32_e32 v23, v23, v85\n\t"
    "v_cvt_pk_bf16_f32 v82, v84, v85\n\t"
    "v_add_f32_e32 v21, v21, v86\n\t"
    "v_exp_f32_e32 v87, v87\n\t"
    "v_mfma_f32_32x32x16_bf16 v[112:127], a[208:211], a[176:179], v[112:127]\n\t"
    "ds_read_b64_tr_b16 v[202:203], v12 offset:43008\n\t"
    "v_add_f32_e32 v23, v23, v87\n\t"
    "v_cvt_pk_bf16_f32 v83, v86, v87\n\t"
    "v_exp_f32_e32 v88, v88\n\t"
    "v_cvt_pk_bf16_f32 v35, v38, v39\n\t"
    "v_mfma_f32_32x32x16_bf16 v[128:143], a[240:243], a[144:147], v[128:143]\n\t"
    "ds_read_b64_tr_b16 v[216:217], v12 offset:41472\n\t"
    "v_add_f32_e32 v21, v21, v88\n\t"
    "v_exp_f32_e32 v89, v89\n\t"
    "v_exp_f32_e32 v90, v90\n\t"
    "v_mfma_f32_32x32x16_bf16 v[144:159], a[240:243], a[176:179], v[144:159]\n\t"
    "ds_read_b64_tr_b16 v[218:219], v12 offset:43520\n\t"
    "v_add_f32_e32 v23, v23, v89\n\t"
    "v_cvt_pk_bf16_f32 v88, v88, v89\n\t"
    "v_add_f32_e32 v21, v21, v90\n\t"
    "v_exp_f32_e32 v91, v91\n\t"
    "v_mfma_f32_32x32x16_bf16 v[96:111], a[212:215], a[148:151], v[96:111]\n\t"
    "ds_read_b64_tr_b16 v[232:233], v12 offset:41984\n\t"
    "v_add_f32_e32 v23, v23, v91\n\t"
    "v_cvt_pk_bf16_f32 v89, v90, v91\n\t"
    "v_exp_f32_e32 v92, v92\n\t"
    "v_cvt_pk_bf16_f32 v40, v40, v41\n\t"
    "v_mfma_f32_32x32x16_bf16 v[112:127], a[212:215], a[180:183], v[112:127]\n\t"
    "ds_read_b64_tr_b16 v[234:235], v12 offset:44032\n\t"
    "v_add_f32_e32 v21, v21, v92\n\t"
    "v_exp_f32_e32 v93, v93\n\t"
    "v_exp_f32_e32 v94, v94\n\t"
    "v_mfma_f32_32x32x16_bf16 v[128:143], a[244:247], a[148:151], v[128:143]\n\t"
    "ds_read_b64_tr_b16 v[248:249], v12 offset:42496\n\t"
    "v_add_f32_e32 v23, v23, v93\n\t"
    "v_cvt_pk_bf16_f32 v90, v92, v93\n\t"
    "v_add_f32_e32 v21, v21, v94\n\t"
    "v_exp_f32_e32 v95, v95\n\t"
    "v_mfma_f32_32x32x16_bf16 v[144:159], a[244:247], a[180:183], v[144:159]\n\t"
    "ds_read_b64_tr_b16 v[250:251], v12 offset:44544\n\t"
    "v_add_f32_e32 v23, v23, v95\n\t"
    "v_cvt_pk_bf16_f32 v91, v94, v95\n\t"
    "v_cvt_pk_bf16_f32 v41, v42, v43\n\t"
    "v_cvt_pk_bf16_f32 v42, v44, v45\n\t"
    "v_cvt_pk_bf16_f32 v43, v46, v47\n\t"
    "v_mfma_f32_32x32x16_bf16 v[96:111], a[216:219], a[152:155], v[96:111]\n\t"
    "ds_read_b64_tr_b16 v[204:205], v12 offset:45056\n\t"
    "v_cvt_pk_bf16_f32 v48, v48, v49\n\t"
    "v_cvt_pk_bf16_f32 v49, v50, v51\n\t"
    "v_cvt_pk_bf16_f32 v50, v52, v53\n\t"
    "v_cvt_pk_bf16_f32 v51, v54, v55\n\t"
    "v_cvt_pk_bf16_f32 v56, v56, v57\n\t"
    "v_mfma_f32_32x32x16_bf16 v[112:127], a[216:219], a[184:187], v[112:127]\n\t"
    "ds_read_b64_tr_b16 v[206:207], v12 offset:47104\n\t"
    "v_cvt_pk_bf16_f32 v57, v58, v59\n\t"
    "v_cvt_pk_bf16_f32 v58, v60, v61\n\t"
    "v_cvt_pk_bf16_f32 v59, v62, v63\n\t"
    "v_mfma_f32_32x32x16_bf16 v[128:143], a[248:251], a[152:155], v[128:143]\n\t"
    "ds_read_b64_tr_b16 v[220:221], v12 offset:45568\n\t"
    "v_mfma_f32_32x32x16_bf16 v[144:159], a[248:251], a[184:187], v[144:159]\n\t"
    "ds_read_b64_tr_b16 v[222:223], v12 offset:47616\n\t"
    "v_mfma_f32_32x32x16_bf16 v[96:111], a[220:223], a[156:159], v[96:111]\n\t"
    "ds_read_b64_tr_b16 v[236:237], v12 offset:46080\n\t"
    "v_mfma_f32_32x32x16_bf16 v[112:127], a[220:223], a[188:191], v[112:127]\n\t"
    "ds_read_b64_tr_b16 v[238:239], v12 offset:48128\n\t"
    "v_mfma_f32_32x32x16_bf16 v[128:143], a[252:255], a[156:159], v[128:143]\n\t"
    "ds_read_b64_tr_b16 v[252:253], v12 offset:46592\n\t"
    "v_mfma_f32_32x32x16_bf16 v[144:159], a[252:255], a[188:191], v[144:159]\n\t"
    "ds_read_b64_tr_b16 v[254:255], v12 offset:48640\n\t"
    "s_waitcnt vmcnt(8) lgkmcnt(0)\n\t"
    "s_barrier\n\t"
    "v_mfma_f32_32x32x16_bf16 a[0:15], v[192:195], v[32:35], a[0:15]\n\t"
    "s_add_u32 m0, s42, 32768\n\t"
    "s_nop 0\n\t"
    "buffer_load_dwordx4 v13, s[24:27], s40 offen offset:0 lds\n\t"
    "ds_read_b128 a[192:195], v8 offset:0\n\t"
    "v_mfma_f32_32x32x16_bf16 a[16:31], v[192:195], v[48:51], a[16:31]\n\t"
    "ds_read_b128 a[196:199], v9 offset:0\n\t"
    "v_mfma_f32_32x32x16_bf16 a[32:47], v[208:211], v[32:35], a[32:47]\n\t"
    "ds_read_b128 a[200:203], v10 offset:0\n\t"
    "v_mfma_f32_32x32x16_bf16 a[48:63], v[208:211], v[48:51], a[48:63]\n\t"
    "ds_read_b128 a[204:207], v11 offset:0\n\t"
    "v_max3_f32 v26, v96, v97, v128\n\t"
    "v_max3_f32 v28, v98, v99, v129\n\t"
    "v_max3_f32 v26, v26, v130, v131\n\t"
    "v_max3_f32 v26, v26, v100, v101\n\t"
    "v_max3_f32 v28, v28, v102, v103\n\t"
    "v_mfma_f32_32x32x16_bf16 a[64:79], v[224:227], v[32:35], a[64:79]\n\t"
    "buffer_load_dwordx4 v14, s[24:27], s40 offen offset:1024 lds\n\t"
    "ds_read_b128 a[208:211], v8 offset:128\n\t"
    "v_max3_f32 v26, v26, v132, v133\n\t"
    "v_max3_f32 v28, v28, v134, v135\n\t"
    "v_max3_f32 v26, v26, v104, v105\n\t"
    "v_max3_f32 v28, v28, v106, v107\n\t"
    "v_mfma_f32_32x32x16_bf16 a[80:95], v[224:227], v[48:51], a[80:95]\n\t"
    "ds_read_b128 a[212:215], v9 offset:128\n\t"
    "v_max3_f32 v26, v26, v136, v137\n\t"
    "v_max3_f32 v28, v28, v138, v139\n\t"
    "v_max3_f32 v26, v26, v108, v109\n\t"
    "v_max3_f32 v28, v28, v110, v111\n\t"
    "v_max3_f32 v26, v26, v140, v141\n\t"
    "v_mfma_f32_32x32x16_bf16 a[96:111], v[240:243], v[32:35], a[96:111]\n\t"
    "ds_read_b128 a[216:219], v10 offset:128\n\t"
    "v_max3_f32 v28, v28, v142, v143\n\t"
    "v_max3_f32 v27, v112, v113, v144\n\t"
    "v_max3_f32 v29, v114, v115, v145\n\t"
    "v_max3_f32 v27, v27, v146, v147\n\t"
    "v_max3_f32 v27, v27, v116, v117\n\t"
    "v_mfma_f32_32x32x16_bf16 a[112:127], v[240:243], v[48:51], a[112:127]\n\t"
    "ds_read_b128 a[220:223], v11 offset:128\n\t"
    "v_max3_f32 v29, v29, v118, v119\n\t"
    "v_max3_f32 v27, v27, v148, v149\n\t"
    "v_max3_f32 v29, v29, v150, v151\n\t"
    "v_max3_f32 v27, v27, v120, v121\n\t"
    "v_max3_f32 v29, v29, v122, v123\n\t"
    "v_mfma_f32_32x32x16_bf16 a[0:15], v[196:199], v[40:43], a[0:15]\n\t"
    "buffer_load_dwordx4 v13, s[24:27], s40 offen offset:2048 lds\n\t"
    "ds_read_b128 a[224:227], v8 offset:8192\n\t"
    "v_max3_f32 v27, v27, v152, v153\n\t"
    "v_max3_f32 v29, v29, v154, v155\n\t"
    "v_max3_f32 v27, v27, v124, v125\n\t"
    "v_max3_f32 v29, v29, v126, v127\n\t"
    "v_mfma_f32_32x32x16_bf16 a[16:31], v[196:199], v[56:59], a[16:31]\n\t"
    "ds_read_b128 a[228:231], v9 offset:8192\n\t"
    "v_max3_f32 v27, v27, v156, v157\n\t"
    "v_max3_f32 v29, v29, v158, v159\n\t"
    "v_mfma_f32_32x32x16_bf16 a[32:47], v[212:215], v[40:43], a[32:47]\n\t"
    "ds_read_b128 a[232:235], v10 offset:8192\n\t"
    "v_max_f32_e32 v30, v26, v28\n\t"
    "v_mov_b32_e32 v5, v30\n\t"
    "v_max_f32_e32 v31, v27, v29\n\t"
    "v_mov_b32_e32 v6, v31\n\t"
    "v_mfma_f32_32x32x16_bf16 a[48:63], v[212:215], v[56:59], a[48:63]\n\t"
    "ds_read_b128 a[236:239], v11 offset:8192\n\t"
    "v_permlane32_swap_b32_e32 v30, v5\n\t"
    "v_max_f32_e32 v30, v30, v5\n\t"
    "v_permlane32_swap_b32_e32 v31, v6\n\t"
    "v_max_f32_e32 v31, v31, v6\n\t"
    "v_mfma_f32_32x32x16_bf16 a[64:79], v[228:231], v[40:43], a[64:79]\n\t"
    "buffer_load_dwordx4 v14, s[24:27], s40 offen offset:3072 lds\n\t"
    "ds_read_b128 a[240:243], v8 offset:8320\n\t"
    "v_max_f32_e32 v7, v30, v31\n\t"
    "v_cmp_lt_f32_e32 vcc, s45, v7\n\t"
    "s_cbranch_vccnz L_RARE_l7\n\t"
    "L_RET_l7:\n\t"
    "v_mfma_f32_32x32x16_bf16 a[80:95], v[228:231], v[56:59], a[80:95]\n\t"
    "ds_read_b128 a[244:247], v9 offset:8320\n\t"
    "v_exp_f32_e32 v96, v96\n\t"
    "v_exp_f32_e32 v97, v97\n\t"
    "v_mfma_f32_32x32x16_bf16 a[96:111], v[244:247], v[40:43], a[96:111]\n\t"
    "ds_read_b128 a[248:251], v10 offset:8320\n\t"
    "v_add_f32_e32 v20, v20, v96\n\t"
    "v_add_f32_e32 v22, v22, v97\n\t"
    "v_exp_f32_e32 v98, v98\n\t"
    "v_mfma_f32_32x32x16_bf16 a[112:127], v[244:247], v[56:59], a[112:127]\n\t"
    "ds_read_b128 a[252:255], v11 offset:8320\n\t"
    "v_add_f32_e32 v20, v20, v98\n\t"
    "v_exp_f32_e32 v99, v99\n\t"
    "v_exp_f32_e32 v100, v100\n\t"
    "v_mfma_f32_32x32x16_bf16 a[0:15], v[200:203], v[64:67], a[0:15]\n\t"
    "s_add_u32 m0, s43, 16384\n\t"
    "s_nop 0\n\t"
    "buffer_load_dwordx4 v15, s[28:31], s41 offen offset:0 lds\n\t"
    "v_add_f32_e32 v22, v22, v99\n\t"
    "v_add_f32_e32 v20, v20, v100\n\t"
    "v_mfma_f32_32x32x16_bf16 a[16:31], v[200:203], v[80:83], a[16:31]\n\t"
    "v_exp_f32_e32 v101, v101\n\t"
    "v_exp_f32_e32 v102, v102\n\t"
    "v_mfma_f32_32x32x16_bf16 a[32:47], v[216:219], v[64:67], a[32:47]\n\t"
    "v_add_f32_e32 v22, v22, v101\n\t"
    "v_add_f32_e32 v20, v20, v102\n\t"
    "v_exp_f32_e32 v103, v103\n\t"
    "v_exp_f32_e32 v104, v104\n\t"
    "v_mfma_f32_32x32x16_bf16 a[48:63], v[216:219], v[80:83], a[48:63]\n\t"
    "v_add_f32_e32 v22, v22, v103\n\t"
    "v_add_f32_e32 v20, v20, v104\n\t"
    "v_exp_f32_e32 v105, v105\n\t"
    "v_exp_f32_e32 v106, v106\n\t"
    "v_mfma_f32_32x32x16_bf16 a[64:79], v[232:235], v[64:67], a[64:79]\n\t"
    "buffer_load_dwordx4 v15, s[28:31], s41 offen offset:2048 lds\n\t"
    "v_add_f32_e32 v22, v22, v105\n\t"
    "v_add_f32_e32 v20, v20, v106\n\t"
    "v_exp_f32_e32 v107, v107\n\t"
    "v_mfma_f32_32x32x16_bf16 a[80:95], v[232:235], v[80:83], a[80:95]\n\t"
    "v_add_f32_e32 v22, v22, v107\n\t"
    "v_exp_f32_e32 v108, v108\n\t"
    "v_exp_f32_e32 v109, v109\n\t"
    "v_mfma_f32_32x32x16_bf16 a[96:111], v[248:251], v[64:67], a[96:111]\n\t"
    "v_add_f32_e32 v20, v20, v108\n\t"
    "v_add_f32_e32 v22, v22, v109\n\t"
    "v_exp_f32_e32 v110, v110\n\t"
    "v_exp_f32_e32 v111, v111\n\t"
    "v_mfma_f32_32x32x16_bf16 a[112:127], v[248:251], v[80:83], a[112:127]\n\t"
    "v_add_f32_e32 v20, v20, v110\n\t"
    "v_add_f32_e32 v22, v22, v111\n\t"
    "v_exp_f32_e32 v112, v112\n\t"
    "v_exp_f32_e32 v113, v113\n\t"
    "v_mfma_f32_32x32x16_bf16 a[0:15], v[204:207], v[72:75], a[0:15]\n\t"
    "s_add_u32 m0, s43, 17280\n\t"
    "s_nop 0\n\t"
    "buffer_load_dwordx4 v15, s[28:31], s41 offen offset:128 lds\n\t"
    "v_add_f32_e32 v21, v21, v112\n\t"
    "v_add_f32_e32 v23, v23, v113\n\t"
    "v_mfma_f32_32x32x16_bf16 a[16:31], v[204:207], v[88:91], a[16:31]\n\t"
    "v_exp_f32_e32 v114, v114\n\t"
    "v_exp_f32_e32 v115, v115\n\t"
    "v_mfma_f32_32x32x16_bf16 a[32:47], v[220:223], v[72:75], a[32:47]\n\t"
    "v_add_f32_e32 v21, v21, v114\n\t"
    "v_add_f32_e32 v23, v23, v115\n\t"
    "v_exp_f32_e32 v116, v116\n\t"
    "v_exp_f32_e32 v117, v117\n\t"
    "v_mfma_f32_32x32x16_bf16 a[48:63], v[220:223], v[88:91], a[48:63]\n\t"
    "v_add_f32_e32 v21, v21, v116\n\t"
    "v_add_f32_e32 v23, v23, v117\n\t"
    "v_exp_f32_e32 v118, v118\n\t"
    "v_exp_f32_e32 v119, v119\n\t"
    "v_mfma_f32_32x32x16_bf16 a[64:79], v[236:239], v[72:75], a[64:79]\n\t"
    "buffer_load_dwordx4 v15, s[28:31], s41 offen offset:2176 lds\n\t"
    "v_add_f32_e32 v21, v21, v118\n\t"
    "v_add_f32_e32 v23, v23, v119\n\t"
    "v_exp_f32_e32 v120, v120\n\t"
    "v_mfma_f32_32x32x16_bf16 a[80:95], v[236:239], v[88:91], a[80:95]\n\t"
    "v_add_f32_e32 v21, v21, v120\n\t"
    "v_exp_f32_e32 v121, v121\n\t"
    "v_exp_f32_e32 v122, v122\n\t"
    "v_mfma_f32_32x32x16_bf16 a[96:111], v[252:255], v[72:75], a[96:111]\n\t"
    "v_add_f32_e32 v23, v23, v121\n\t"
    "v_add_f32_e32 v21, v21, v122\n\t"
    "v_exp_f32_e32 v123, v123\n\t"
    "v_exp_f32_e32 v124, v124\n\t"
    "v_mfma_f32_32x32x16_bf16 a[112:127], v[252:255], v[88:91], a[112:127]\n\t"
    "v_add_f32_e32 v23, v23, v123\n\t"
    "v_add_f32_e32 v21, v21, v124\n\t"
    "v_exp_f32_e32 v125, v125\n\t"
    "v_exp_f32_e32 v126, v126\n\t"
    "v_add_f32_e32 v23, v23, v125\n\t"
    "v_add_f32_e32 v21, v21, v126\n\t"
    "v_exp_f32_e32 v127, v127\n\t"
    "s_nop 0\n\t"
    "v_add_f32_e32 v23, v23, v127\n\t"
    "s_add_u32 s40, s40, 16384\n\t"
    "s_add_u32 s41, s41, 16384\n\t"
    "s_cmp_lg_u32 s47, 0\n\t"
    "s_cbranch_scc1 L_RESC_l7\n\t"
    "L_RRET_l7:\n\t"
    "s_waitcnt lgkmcnt(0)\n\t"
    "s_sub_u32 s46, s46, 1\n\t"
    "s_cmp_lg_u32 s46, 0\n\t"
    "s_cbranch_scc1 L_LOOP\n\t"
    "ds_read_b64_tr_b16 v[192:193], v12 offset:49152\n\t"
    "ds_read_b64_tr_b16 v[194:195], v12 offset:51200\n\t"
    "ds_read_b64_tr_b16 v[208:209], v12 offset:49664\n\t"
    "ds_read_b64_tr_b16 v[210:211], v12 offset:51712\n\t"
    "ds_read_b64_tr_b16 v[224:225], v12 offset:50176\n\t"
    "ds_read_b64_tr_b16 v[226:227], v12 offset:52224\n\t"
    "ds_read_b64_tr_b16 v[240:241], v12 offset:50688\n\t"
    "ds_read_b64_tr_b16 v[242:243], v12 offset:52736\n\t"
    "ds_read_b64_tr_b16 v[196:197], v12 offset:53248\n\t"
    "ds_read_b64_tr_b16 v[198:199], v12 offset:55296\n\t"
    "ds_read_b64_tr_b16 v[212:213], v12 offset:53760\n\t"
    "ds_read_b64_tr_b16 v[214:215], v12 offset:55808\n\t"
    "ds_read_b64_tr_b16 v[228:229], v12 offset:54272\n\t"
    "ds_read_b64_tr_b16 v[230:231], v12 offset:56320\n\t"
    "ds_read_b64_tr_b16 v[244:245], v12 offset:54784\n\t"
    "ds_read_b64_tr_b16 v[246:247], v12 offset:56832\n\t"
    "ds_read_b64_tr_b16 v[200:201], v12 offset:57344\n\t"
    "ds_read_b64_tr_b16 v[202:203], v12 offset:59392\n\t"
    "ds_read_b64_tr_b16 v[216:217], v12 offset:57856\n\t"
    "ds_read_b64_tr_b16 v[218:219], v12 offset:59904\n\t"
    "ds_read_b64_tr_b16 v[232:233], v12 offset:58368\n\t"
    "ds_read_b64_tr_b16 v[234:235], v12 offset:60416\n\t"
    "ds_read_b64_tr_b16 v[248:249], v12 offset:58880\n\t"
    "ds_read_b64_tr_b16 v[250:251], v12 offset:60928\n\t"
    "ds_read_b64_tr_b16 v[204:205], v12 offset:61440\n\t"
    "ds_read_b64_tr_b16 v[206:207], v12 offset:63488\n\t"
    "ds_read_b64_tr_b16 v[220:221], v12 offset:61952\n\t"
    "ds_read_b64_tr_b16 v[222:223], v12 offset:64000\n\t"
    "ds_read_b64_tr_b16 v[236:237], v12 offset:62464\n\t"
    "ds_read_b64_tr_b16 v[238:239], v12 offset:64512\n\t"
    "ds_read_b64_tr_b16 v[252:253], v12 offset:62976\n\t"
    "ds_read_b64_tr_b16 v[254:255], v12 offset:65024\n\t"
    "v_exp_f32_e32 v128, v128\n\t"
    "v_exp_f32_e32 v129, v129\n\t"
    "v_exp_f32_e32 v130, v130\n\t"
    "v_exp_f32_e32 v131, v131\n\t"
    "v_exp_f32_e32 v132, v132\n\t"
    "v_exp_f32_e32 v133, v133\n\t"
    "v_exp_f32_e32 v134, v134\n\t"
    "v_exp_f32_e32 v135, v135\n\t"
    "v_exp_f32_e32 v136, v136\n\t"
    "v_exp_f32_e32 v137, v137\n\t"
    "v_exp_f32_e32 v138, v138\n\t"
    "v_exp_f32_e32 v139, v139\n\t"
    "v_exp_f32_e32 v140, v140\n\t"
    "v_exp_f32_e32 v141, v141\n\t"
    "v_exp_f32_e32 v142, v142\n\t"
    "v_exp_f32_e32 v143, v143\n\t"
    "v_exp_f32_e32 v144, v144\n\t"
    "v_exp_f32_e32 v145, v145\n\t"
    "v_exp_f32_e32 v146, v146\n\t"
    "v_exp_f32_e32 v147, v147\n\t"
    "v_exp_f32_e32 v148, v148\n\t"
    "v_exp_f32_e32 v149, v149\n\t"
    "v_exp_f32_e32 v150, v150\n\t"
    "v_exp_f32_e32 v151, v151\n\t"
    "v_exp_f32_e32 v152, v152\n\t"
    "v_exp_f32_e32 v153, v153\n\t"
    "v_exp_f32_e32 v154, v154\n\t"
    "v_exp_f32_e32 v155, v155\n\t"
    "v_exp_f32_e32 v156, v156\n\t"
    "v_exp_f32_e32 v157, v157\n\t"
    "v_exp_f32_e32 v158, v158\n\t"
    "v_exp_f32_e32 v159, v159\n\t"
    "v_cvt_pk_bf16_f32 v96, v96, v97\n\t"
    "v_cvt_pk_bf16_f32 v97, v98, v99\n\t"
    "v_cvt_pk_bf16_f32 v98, v100, v101\n\t"
    "v_cvt_pk_bf16_f32 v99, v102, v103\n\t"
    "v_cvt_pk_bf16_f32 v104, v104, v105\n\t"
    "v_cvt_pk_bf16_f32 v105, v106, v107\n\t"
    "v_cvt_pk_bf16_f32 v106, v108, v109\n\t"
    "v_cvt_pk_bf16_f32 v107, v110, v111\n\t"
    "v_cvt_pk_bf16_f32 v112, v112, v113\n\t"
    "v_cvt_pk_bf16_f32 v113, v114, v115\n\t"
    "v_cvt_pk_bf16_f32 v114, v116, v117\n\t"
    "v_cvt_pk_bf16_f32 v115, v118, v119\n\t"
    "v_cvt_pk_bf16_f32 v120, v120, v121\n\t"
    "v_cvt_pk_bf16_f32 v121, v122, v123\n\t"
    "v_cvt_pk_bf16_f32 v122, v124, v125\n\t"
    "v_cvt_pk_bf16_f32 v123, v126, v127\n\t"
    "v_add_f32_e32 v20, v20, v128\n\t"
    "v_add_f32_e32 v22, v22, v129\n\t"
    "v_cvt_pk_bf16_f32 v128, v128, v129\n\t"
    "v_add_f32_e32 v20, v20, v130\n\t"
    "v_add_f32_e32 v22, v22, v131\n\t"
    "v_cvt_pk_bf16_f32 v129, v130, v131\n\t"
    "v_add_f32_e32 v20, v20, v132\n\t"
    "v_add_f32_e32 v22, v22, v133\n\t"
    "v_cvt_pk_bf16_f32 v130, v132, v133\n\t"
    "v_add_f32_e32 v20, v20, v134\n\t"
    "v_add_f32_e32 v22, v22, v135\n\t"
    "v_cvt_pk_bf16_f32 v131, v134, v135\n\t"
    "v_add_f32_e32 v20, v20, v136\n\t"
    "v_add_f32_e32 v22, v22, v137\n\t"
    "v_cvt_pk_bf16_f32 v136, v136, v137\n\t"
    "v_add_f32_e32 v20, v20, v138\n\t"
    "v_add_f32_e32 v22, v22, v139\n\t"
    "v_cvt_pk_bf16_f32 v137, v138, v139\n\t"
    "v_add_f32_e32 v20, v20, v140\n\t"
    "v_add_f32_e32 v22, v22, v141\n\t"
    "v_cvt_pk_bf16_f32 v138, v140, v141\n\t"
    "v_add_f32_e32 v20, v20, v142\n\t"
    "v_add_f32_e32 v22, v22, v143\n\t"
    "v_cvt_pk_bf16_f32 v139, v142, v143\n\t"
    "v_add_f32_e32 v21, v21, v144\n\t"
    "v_add_f32_e32 v23, v23, v145\n\t"
    "v_cvt_pk_bf16_f32 v144, v144, v145\n\t"
    "v_add_f32_e32 v21, v21, v146\n\t"
    "v_add_f32_e32 v23, v23, v147\n\t"
    "v_cvt_pk_bf16_f32 v145, v146, v147\n\t"
    "v_add_f32_e32 v21, v21, v148\n\t"
    "v_add_f32_e32 v23, v23, v149\n\t"
    "v_cvt_pk_bf16_f32 v146, v148, v149\n\t"
    "v_add_f32_e32 v21, v21, v150\n\t"
    "v_add_f32_e32 v23, v23, v151\n\t"
    "v_cvt_pk_bf16_f32 v147, v150, v151\n\t"
    "v_add_f32_e32 v21, v21, v152\n\t"
    "v_add_f32_e32 v23, v23, v153\n\t"
    "v_cvt_pk_bf16_f32 v152, v152, v153\n\t"
    "v_add_f32_e32 v21, v21, v154\n\t"
    "v_add_f32_e32 v23, v23, v155\n\t"
    "v_cvt_pk_bf16_f32 v153, v154, v155\n\t"
    "v_add_f32_e32 v21, v21, v156\n\t"
    "v_add_f32_e32 v23, v23, v157\n\t"
    "v_cvt_pk_bf16_f32 v154, v156, v157\n\t"
    "v_add_f32_e32 v21, v21, v158\n\t"
    "v_add_f32_e32 v23, v23, v159\n\t"
    "v_cvt_pk_bf16_f32 v155, v158, v159\n\t"
    "s_waitcnt lgkmcnt(0)\n\t"
    "s_nop 1\n\t"
    "v_mfma_f32_32x32x16_bf16 a[0:15], v[192:195], v[96:99], a[0:15]\n\t"
    "v_mfma_f32_32x32x16_bf16 a[16:31], v[192:195], v[112:115], a[16:31]\n\t"
    "v_mfma_f32_32x32x16_bf16 a[32:47], v[208:211], v[96:99], a[32:47]\n\t"
    "v_mfma_f32_32x32x16_bf16 a[48:63], v[208:211], v[112:115], a[48:63]\n\t"
    "v_mfma_f32_32x32x16_bf16 a[64:79], v[224:227], v[96:99], a[64:79]\n\t"
    "v_mfma_f32_32x32x16_bf16 a[80:95], v[224:227], v[112:115], a[80:95]\n\t"
    "v_mfma_f32_32x32x16_bf16 a[96:111], v[240:243], v[96:99], a[96:111]\n\t"
    "v_mfma_f32_32x32x16_bf16 a[112:127], v[240:243], v[112:115], a[112:127]\n\t"
    "v_mfma_f32_32x32x16_bf16 a[0:15], v[196:199], v[104:107], a[0:15]\n\t"
    "v_mfma_f32_32x32x16_bf16 a[16:31], v[196:199], v[120:123], a[16:31]\n\t"
    "v_mfma_f32_32x32x16_bf16 a[32:47], v[212:215], v[104:107], a[32:47]\n\t"
    "v_mfma_f32_32x32x16_bf16 a[48:63], v[212:215], v[120:123], a[48:63]\n\t"
    "v_mfma_f32_32x32x16_bf16 a[64:79], v[228:231], v[104:107], a[64:79]\n\t"
    "v_mfma_f32_32x32x16_bf16 a[80:95], v[228:231], v[120:123], a[80:95]\n\t"
    "v_mfma_f32_32x32x16_bf16 a[96:111], v[244:247], v[104:107], a[96:111]\n\t"
    "v_mfma_f32_32x32x16_bf16 a[112:127], v[244:247], v[120:123], a[112:127]\n\t"
    "v_mfma_f32_32x32x16_bf16 a[0:15], v[200:203], v[128:131], a[0:15]\n\t"
    "v_mfma_f32_32x32x16_bf16 a[16:31], v[200:203], v[144:147], a[16:31]\n\t"
    "v_mfma_f32_32x32x16_bf16 a[32:47], v[216:219], v[128:131], a[32:47]\n\t"
    "v_mfma_f32_32x32x16_bf16 a[48:63], v[216:219], v[144:147], a[48:63]\n\t"
    "v_mfma_f32_32x32x16_bf16 a[64:79], v[232:235], v[128:131], a[64:79]\n\t"
    "v_mfma_f32_32x32x16_bf16 a[80:95], v[232:235], v[144:147], a[80:95]\n\t"
    "v_mfma_f32_32x32x16_bf16 a[96:111], v[248:251], v[128:131], a[96:111]\n\t"
    "v_mfma_f32_32x32x16_bf16 a[112:127], v[248:251], v[144:147], a[112:127]\n\t"
    "v_mfma_f32_32x32x16_bf16 a[0:15], v[204:207], v[136:139], a[0:15]\n\t"
    "v_mfma_f32_32x32x16_bf16 a[16:31], v[204:207], v[152:155], a[16:31]\n\t"
    "v_mfma_f32_32x32x16_bf16 a[32:47], v[220:223], v[136:139], a[32:47]\n\t"
    "v_mfma_f32_32x32x16_bf16 a[48:63], v[220:223], v[152:155], a[48:63]\n\t"
    "v_mfma_f32_32x32x16_bf16 a[64:79], v[236:239], v[136:139], a[64:79]\n\t"
    "v_mfma_f32_32x32x16_bf16 a[80:95], v[236:239], v[152:155], a[80:95]\n\t"
    "v_mfma_f32_32x32x16_bf16 a[96:111], v[252:255], v[136:139], a[96:111]\n\t"
    "v_mfma_f32_32x32x16_bf16 a[112:127], v[252:255], v[152:155], a[112:127]\n\t"
    "v_add_f32_e32 v30, v20, v22\n\t"
    "v_mov_b32_e32 v5, v30\n\t"
    "s_nop 1\n\t"
    "v_permlane32_swap_b32_e32 v30, v5\n\t"
    "v_add_f32_e32 v30, v30, v5\n\t"
    "v_rcp_f32_e32 v30, v30\n\t"
    "v_add_f32_e32 v31, v21, v23\n\t"
    "v_mov_b32_e32 v6, v31\n\t"
    "s_nop 1\n\t"
    "v_permlane32_swap_b32_e32 v31, v6\n\t"
    "v_add_f32_e32 v31, v31, v6\n\t"
    "v_rcp_f32_e32 v31, v31\n\t"
    "s_nop 7\n\t"
    "s_nop 7\n\t"
    "s_lshl_b32 s52, s44, 13\n\t"
    "s_add_u32 s52, s52, 131072\n\t"
    "s_add_u32 s52, s52, %5\n\t"
    "v_lshlrev_b32_e32 v17, 3, v4\n\t"
    "v_lshl_add_u32 v17, v3, 8, v17\n\t"
    "v_add_u32_e32 v17, s52, v17\n\t"
    "v_and_b32_e32 v18, 15, v3\n\t"
    "v_lshrrev_b32_e32 v19, 4, v1\n\t"
    "v_and_b32_e32 v2, 15, v1\n\t"
    "v_lshlrev_b32_e32 v16, 4, v2\n\t"
    "v_lshl_add_u32 v16, v19, 8, v16\n\t"
    "v_accvgpr_read_b32 v32, a0\n\t"
    "v_accvgpr_read_b32 v33, a1\n\t"
    "v_accvgpr_read_b32 v34, a2\n\t"
    "v_accvgpr_read_b32 v35, a3\n\t"
    "v_mul_f32_e32 v32, v30, v32\n\t"
    "v_mul_f32_e32 v33, v30, v33\n\t"
    "v_mul_f32_e32 v34, v30, v34\n\t"
    "v_mul_f32_e32 v35, v30, v35\n\t"
    "v_cvt_pk_bf16_f32 v36, v32, v33\n\t"
    "v_cvt_pk_bf16_f32 v37, v34, v35\n\t"
    "v_xor_b32_e32 v38, 0, v18\n\t"
    "v_lshl_add_u32 v38, v38, 4, v17\n\t"
    "ds_write_b64 v38, v[36:37]\n\t"
    "v_accvgpr_read_b32 v32, a4\n\t"
    "v_accvgpr_read_b32 v33, a5\n\t"
    "v_accvgpr_read_b32 v34, a6\n\t"
    "v_accvgpr_read_b32 v35, a7\n\t"
    "v_mul_f32_e32 v32, v30, v32\n\t"
    "v_mul_f32_e32 v33, v30, v33\n\t"
    "v_mul_f32_e32 v34, v30, v34\n\t"
    "v_mul_f32_e32 v35, v30, v35\n\t"
    "v_cvt_pk_bf16_f32 v36, v32, v33\n\t"
    "v_cvt_pk_bf16_f32 v37, v34, v35\n\t"
    "v_xor_b32_e32 v38, 1, v18\n\t"
    "v_lshl_add_u32 v38, v38, 4, v17\n\t"
    "ds_write_b64 v38, v[36:37]\n\t"
    "v_accvgpr_read_b32 v32, a8\n\t"
    "v_accvgpr_read_b32 v33, a9\n\t"
    "v_accvgpr_read_b32 v34, a10\n\t"
    "v_accvgpr_read_b32 v35, a11\n\t"
    "v_mul_f32_e32 v32, v30, v32\n\t"
    "v_mul_f32_e32 v33, v30, v33\n\t"
    "v_mul_f32_e32 v34, v30, v34\n\t"
    "v_mul_f32_e32 v35, v30, v35\n\t"
    "v_cvt_pk_bf16_f32 v36, v32, v33\n\t"
    "v_cvt_pk_bf16_f32 v37, v34, v35\n\t"
    "v_xor_b32_e32 v38, 2, v18\n\t"
    "v_lshl_add_u32 v38, v38, 4, v17\n\t"
    "ds_write_b64 v38, v[36:37]\n\t"
    "v_accvgpr_read_b32 v32, a12\n\t"
    "v_accvgpr_read_b32 v33, a13\n\t"
    "v_accvgpr_read_b32 v34, a14\n\t"
    "v_accvgpr_read_b32 v35, a15\n\t"
    "v_mul_f32_e32 v32, v30, v32\n\t"
    "v_mul_f32_e32 v33, v30, v33\n\t"
    "v_mul_f32_e32 v34, v30, v34\n\t"
    "v_mul_f32_e32 v35, v30, v35\n\t"
    "v_cvt_pk_bf16_f32 v36, v32, v33\n\t"
    "v_cvt_pk_bf16_f32 v37, v34, v35\n\t"
    "v_xor_b32_e32 v38, 3, v18\n\t"
    "v_lshl_add_u32 v38, v38, 4, v17\n\t"
    "ds_write_b64 v38, v[36:37]\n\t"
    "v_accvgpr_read_b32 v32, a32\n\t"
    "v_accvgpr_read_b32 v33, a33\n\t"
    "v_accvgpr_read_b32 v34, a34\n\t"
    "v_accvgpr_read_b32 v35, a35\n\t"
    "v_mul_f32_e32 v32, v30, v32\n\t"
    "v_mul_f32_e32 v33, v30, v33\n\t"
    "v_mul_f32_e32 v34, v30, v34\n\t"
    "v_mul_f32_e32 v35, v30, v35\n\t"
    "v_cvt_pk_bf16_f32 v36, v32, v33\n\t"
    "v_cvt_pk_bf16_f32 v37, v34, v35\n\t"
    "v_xor_b32_e32 v38, 4, v18\n\t"
    "v_lshl_add_u32 v38, v38, 4, v17\n\t"
    "ds_write_b64 v38, v[36:37]\n\t"
    "v_accvgpr_read_b32 v32, a36\n\t"
    "v_accvgpr_read_b32 v33, a37\n\t"
    "v_accvgpr_read_b32 v34, a38\n\t"
    "v_accvgpr_read_b32 v35, a39\n\t"
    "v_mul_f32_e32 v32, v30, v32\n\t"
    "v_mul_f32_e32 v33, v30, v33\n\t"
    "v_mul_f32_e32 v34, v30, v34\n\t"
    "v_mul_f32_e32 v35, v30, v35\n\t"
    "v_cvt_pk_bf16_f32 v36, v32, v33\n\t"
    "v_cvt_pk_bf16_f32 v37, v34, v35\n\t"
    "v_xor_b32_e32 v38, 5, v18\n\t"
    "v_lshl_add_u32 v38, v38, 4, v17\n\t"
    "ds_write_b64 v38, v[36:37]\n\t"
    "v_accvgpr_read_b32 v32, a40\n\t"
    "v_accvgpr_read_b32 v33, a41\n\t"
    "v_accvgpr_read_b32 v34, a42\n\t"
    "v_accvgpr_read_b32 v35, a43\n\t"
    "v_mul_f32_e32 v32, v30, v32\n\t"
    "v_mul_f32_e32 v33, v30, v33\n\t"
    "v_mul_f32_e32 v34, v30, v34\n\t"
    "v_mul_f32_e32 v35, v30, v35\n\t"
    "v_cvt_pk_bf16_f32 v36, v32, v33\n\t"
    "v_cvt_pk_bf16_f32 v37, v34, v35\n\t"
    "v_xor_b32_e32 v38, 6, v18\n\t"
    "v_lshl_add_u32 v38, v38, 4, v17\n\t"
    "ds_write_b64 v38, v[36:37]\n\t"
    "v_accvgpr_read_b32 v32, a44\n\t"
    "v_accvgpr_read_b32 v33, a45\n\t"
    "v_accvgpr_read_b32 v34, a46\n\t"
    "v_accvgpr_read_b32 v35, a47\n\t"
    "v_mul_f32_e32 v32, v30, v32\n\t"
    "v_mul_f32_e32 v33, v30, v33\n\t"
    "v_mul_f32_e32 v34, v30, v34\n\t"
    "v_mul_f32_e32 v35, v30, v35\n\t"
    "v_cvt_pk_bf16_f32 v36, v32, v33\n\t"
    "v_cvt_pk_bf16_f32 v37, v34, v35\n\t"
    "v_xor_b32_e32 v38, 7, v18\n\t"
    "v_lshl_add_u32 v38, v38, 4, v17\n\t"
    "ds_write_b64 v38, v[36:37]\n\t"
    "v_accvgpr_read_b32 v32, a64\n\t"
    "v_accvgpr_read_b32 v33, a65\n\t"
    "v_accvgpr_read_b32 v34, a66\n\t"
    "v_accvgpr_read_b32 v35, a67\n\t"
    "v_mul_f32_e32 v32, v30, v32\n\t"
    "v_mul_f32_e32 v33, v30, v33\n\t"
    "v_mul_f32_e32 v34, v30, v34\n\t"
    "v_mul_f32_e32 v35, v30, v35\n\t"
    "v_cvt_pk_bf16_f32 v36, v32, v33\n\t"
    "v_cvt_pk_bf16_f32 v37, v34, v35\n\t"
    "v_xor_b32_e32 v38, 8, v18\n\t"
    "v_lshl_add_u32 v38, v38, 4, v17\n\t"
    "ds_write_b64 v38, v[36:37]\n\t"
    "v_accvgpr_read_b32 v32, a68\n\t"
    "v_accvgpr_read_b32 v33, a69\n\t"
    "v_accvgpr_read_b32 v34, a70\n\t"
    "v_accvgpr_read_b32 v35, a71\n\t"
    "v_mul_f32_e32 v32, v30, v32\n\t"
    "v_mul_f32_e32 v33, v30, v33\n\t"
    "v_mul_f32_e32 v34, v30, v34\n\t"
    "v_mul_f32_e32 v35, v30, v35\n\t"
    "v_cvt_pk_bf16_f32 v36, v32, v33\n\t"
    "v_cvt_pk_bf16_f32 v37, v34, v35\n\t"
    "v_xor_b32_e32 v38, 9, v18\n\t"
    "v_lshl_add_u32 v38, v38, 4, v17\n\t"
    "ds_write_b64 v38, v[36:37]\n\t"
    "v_accvgpr_read_b32 v32, a72\n\t"
    "v_accvgpr_read_b32 v33, a73\n\t"
    "v_accvgpr_read_b32 v34, a74\n\t"
    "v_accvgpr_read_b32 v35, a75\n\t"
    "v_mul_f32_e32 v32, v30, v32\n\t"
    "v_mul_f32_e32 v33, v30, v33\n\t"
    "v_mul_f32_e32 v34, v30, v34\n\t"
    "v_mul_f32_e32 v35, v30, v35\n\t"
    "v_cvt_pk_bf16_f32 v36, v32, v33\n\t"
    "v_cvt_pk_bf16_f32 v37, v34, v35\n\t"
    "v_xor_b32_e32 v38, 10, v18\n\t"
    "v_lshl_add_u32 v38, v38, 4, v17\n\t"
    "ds_write_b64 v38, v[36:37]\n\t"
    "v_accvgpr_read_b32 v32, a76\n\t"
    "v_accvgpr_read_b32 v33, a77\n\t"
    "v_accvgpr_read_b32 v34, a78\n\t"
    "v_accvgpr_read_b32 v35, a79\n\t"
    "v_mul_f32_e32 v32, v30, v32\n\t"
    "v_mul_f32_e32 v33, v30, v33\n\t"
    "v_mul_f32_e32 v34, v30, v34\n\t"
    "v_mul_f32_e32 v35, v30, v35\n\t"
    "v_cvt_pk_bf16_f32 v36, v32, v33\n\t"
    "v_cvt_pk_bf16_f32 v37, v34, v35\n\t"
    "v_xor_b32_e32 v38, 11, v18\n\t"
    "v_lshl_add_u32 v38, v38, 4, v17\n\t"
    "ds_write_b64 v38, v[36:37]\n\t"
    "v_accvgpr_read_b32 v32, a96\n\t"
    "v_accvgpr_read_b32 v33, a97\n\t"
    "v_accvgpr_read_b32 v34, a98\n\t"
    "v_accvgpr_read_b32 v35, a99\n\t"
    "v_mul_f32_e32 v32, v30, v32\n\t"
    "v_mul_f32_e32 v33, v30, v33\n\t"
    "v_mul_f32_e32 v34, v30, v34\n\t"
    "v_mul_f32_e32 v35, v30, v35\n\t"
    "v_cvt_pk_bf16_f32 v36, v32, v33\n\t"
    "v_cvt_pk_bf16_f32 v37, v34, v35\n\t"
    "v_xor_b32_e32 v38, 12, v18\n\t"
    "v_lshl_add_u32 v38, v38, 4, v17\n\t"
    "ds_write_b64 v38, v[36:37]\n\t"
    "v_accvgpr_read_b32 v32, a100\n\t"
    "v_accvgpr_read_b32 v33, a101\n\t"
    "v_accvgpr_read_b32 v34, a102\n\t"
    "v_accvgpr_read_b32 v35, a103\n\t"
    "v_mul_f32_e32 v32, v30, v32\n\t"
    "v_mul_f32_e32 v33, v30, v33\n\t"
    "v_mul_f32_e32 v34, v30, v34\n\t"
    "v_mul_f32_e32 v35, v30, v35\n\t"
    "v_cvt_pk_bf16_f32 v36, v32, v33\n\t"
    "v_cvt_pk_bf16_f32 v37, v34, v35\n\t"
    "v_xor_b32_e32 v38, 13, v18\n\t"
    "v_lshl_add_u32 v38, v38, 4, v17\n\t"
    "ds_write_b64 v38, v[36:37]\n\t"
    "v_accvgpr_read_b32 v32, a104\n\t"
    "v_accvgpr_read_b32 v33, a105\n\t"
    "v_accvgpr_read_b32 v34, a106\n\t"
    "v_accvgpr_read_b32 v35, a107\n\t"
    "v_mul_f32_e32 v32, v30, v32\n\t"
    "v_mul_f32_e32 v33, v30, v33\n\t"
    "v_mul_f32_e32 v34, v30, v34\n\t"
    "v_mul_f32_e32 v35, v30, v35\n\t"
    "v_cvt_pk_bf16_f32 v36, v32, v33\n\t"
    "v_cvt_pk_bf16_f32 v37, v34, v35\n\t"
    "v_xor_b32_e32 v38, 14, v18\n\t"
    "v_lshl_add_u32 v38, v38, 4, v17\n\t"
    "ds_write_b64 v38, v[36:37]\n\t"
    "v_accvgpr_read_b32 v32, a108\n\t"
    "v_accvgpr_read_b32 v33, a109\n\t"
    "v_accvgpr_read_b32 v34, a110\n\t"
    "v_accvgpr_read_b32 v35, a111\n\t"
    "v_mul_f32_e32 v32, v30, v32\n\t"
    "v_mul_f32_e32 v33, v30, v33\n\t"
    "v_mul_f32_e32 v34, v30, v34\n\t"
    "v_mul_f32_e32 v35, v30, v35\n\t"
    "v_cvt_pk_bf16_f32 v36, v32, v33\n\t"
    "v_cvt_pk_bf16_f32 v37, v34, v35\n\t"
    "v_xor_b32_e32 v38, 15, v18\n\t"
    "v_lshl_add_u32 v38, v38, 4, v17\n\t"
    "ds_write_b64 v38, v[36:37]\n\t"
    "s_waitcnt lgkmcnt(0)\n\t"
    "v_add_u32_e32 v38, 0, v19\n\t"
    "v_and_b32_e32 v39, 15, v38\n\t"
    "v_xor_b32_e32 v39, v39, v2\n\t"
    "v_lshlrev_b32_e32 v39, 4, v39\n\t"
    "v_lshl_add_u32 v38, v38, 8, v39\n\t"
    "v_add_u32_e32 v38, s52, v38\n\t"
    "ds_read_b128 v[40:43], v38\n\t"
    "v_add_u32_e32 v38, 4, v19\n\t"
    "v_and_b32_e32 v39, 15, v38\n\t"
    "v_xor_b32_e32 v39, v39, v2\n\t"
    "v_lshlrev_b32_e32 v39, 4, v39\n\t"
    "v_lshl_add_u32 v38, v38, 8, v39\n\t"
    "v_add_u32_e32 v38, s52, v38\n\t"
    "ds_read_b128 v[44:47], v38\n\t"
    "v_add_u32_e32 v38, 8, v19\n\t"
    "v_and_b32_e32 v39, 15, v38\n\t"
    "v_xor_b32_e32 v39, v39, v2\n\t"
    "v_lshlrev_b32_e32 v39, 4, v39\n\t"
    "v_lshl_add_u32 v38, v38, 8, v39\n\t"
    "v_add_u32_e32 v38, s52, v38\n\t"
    "ds_read_b128 v[48:51], v38\n\t"
    "v_add_u32_e32 v38, 12, v19\n\t"
    "v_and_b32_e32 v39, 15, v38\n\t"
    "v_xor_b32_e32 v39, v39, v2\n\t"
    "v_lshlrev_b32_e32 v39, 4, v39\n\t"
    "v_lshl_add_u32 v38, v38, 8, v39\n\t"
    "v_add_u32_e32 v38, s52, v38\n\t"
    "ds_read_b128 v[52:55], v38\n\t"
    "v_add_u32_e32 v38, 16, v19\n\t"
    "v_and_b32_e32 v39, 15, v38\n\t"
    "v_xor_b32_e32 v39, v39, v2\n\t"
    "v_lshlrev_b32_e32 v39, 4, v39\n\t"
    "v_lshl_add_u32 v38, v38, 8, v39\n\t"
    "v_add_u32_e32 v38, s52, v38\n\t"
    "ds_read_b128 v[56:59], v38\n\t"
    "v_add_u32_e32 v38, 20, v19\n\t"
    "v_and_b32_e32 v39, 15, v38\n\t"
    "v_xor_b32_e32 v39, v39, v2\n\t"
    "v_lshlrev_b32_e32 v39, 4, v39\n\t"
    "v_lshl_add_u32 v38, v38, 8, v39\n\t"
    "v_add_u32_e32 v38, s52, v38\n\t"
    "ds_read_b128 v[60:63], v38\n\t"
    "v_add_u32_e32 v38, 24, v19\n\t"
    "v_and_b32_e32 v39, 15, v38\n\t"
    "v_xor_b32_e32 v39, v39, v2\n\t"
    "v_lshlrev_b32_e32 v39, 4, v39\n\t"
    "v_lshl_add_u32 v38, v38, 8, v39\n\t"
    "v_add_u32_e32 v38, s52, v38\n\t"
    "ds_read_b128 v[64:67], v38\n\t"
    "v_add_u32_e32 v38, 28, v19\n\t"
    "v_and_b32_e32 v39, 15, v38\n\t"
    "v_xor_b32_e32 v39, v39, v2\n\t"
    "v_lshlrev_b32_e32 v39, 4, v39\n\t"
    "v_lshl_add_u32 v38, v38, 8, v39\n\t"
    "v_add_u32_e32 v38, s52, v38\n\t"
    "ds_read_b128 v[68:71], v38\n\t"
    "s_add_u32 s53, s50, 0\n\t"
    "s_add_u32 s51, s50, 4096\n\t"
    "s_waitcnt lgkmcnt(7)\n\t"
    "buffer_store_dwordx4 v[40:43], v16, s[36:39], s53 offen offset:0\n\t"
    "s_waitcnt lgkmcnt(6)\n\t"
    "buffer_store_dwordx4 v[44:47], v16, s[36:39], s53 offen offset:1024\n\t"
    "s_waitcnt lgkmcnt(5)\n\t"
    "buffer_store_dwordx4 v[48:51], v16, s[36:39], s53 offen offset:2048\n\t"
    "s_waitcnt lgkmcnt(4)\n\t"
    "buffer_store_dwordx4 v[52:55], v16, s[36:39], s53 offen offset:3072\n\t"
    "s_waitcnt lgkmcnt(3)\n\t"
    "buffer_store_dwordx4 v[56:59], v16, s[36:39], s51 offen offset:0\n\t"
    "s_waitcnt lgkmcnt(2)\n\t"
    "buffer_store_dwordx4 v[60:63], v16, s[36:39], s51 offen offset:1024\n\t"
    "s_waitcnt lgkmcnt(1)\n\t"
    "buffer_store_dwordx4 v[64:67], v16, s[36:39], s51 offen offset:2048\n\t"
    "s_waitcnt lgkmcnt(0)\n\t"
    "buffer_store_dwordx4 v[68:71], v16, s[36:39], s51 offen offset:3072\n\t"
    "s_nop 1\n\t"
    "v_accvgpr_read_b32 v32, a16\n\t"
    "v_accvgpr_read_b32 v33, a17\n\t"
    "v_accvgpr_read_b32 v34, a18\n\t"
    "v_accvgpr_read_b32 v35, a19\n\t"
    "v_mul_f32_e32 v32, v31, v32\n\t"
    "v_mul_f32_e32 v33, v31, v33\n\t"
    "v_mul_f32_e32 v34, v31, v34\n\t"
    "v_mul_f32_e32 v35, v31, v35\n\t"
    "v_cvt_pk_bf16_f32 v36, v32, v33\n\t"
    "v_cvt_pk_bf16_f32 v37, v34, v35\n\t"
    "v_xor_b32_e32 v38, 0, v18\n\t"
    "v_lshl_add_u32 v38, v38, 4, v17\n\t"
    "ds_write_b64 v38, v[36:37]\n\t"
    "v_accvgpr_read_b32 v32, a20\n\t"
    "v_accvgpr_read_b32 v33, a21\n\t"
    "v_accvgpr_read_b32 v34, a22\n\t"
    "v_accvgpr_read_b32 v35, a23\n\t"
    "v_mul_f32_e32 v32, v31, v32\n\t"
    "v_mul_f32_e32 v33, v31, v33\n\t"
    "v_mul_f32_e32 v34, v31, v34\n\t"
    "v_mul_f32_e32 v35, v31, v35\n\t"
    "v_cvt_pk_bf16_f32 v36, v32, v33\n\t"
    "v_cvt_pk_bf16_f32 v37, v34, v35\n\t"
    "v_xor_b32_e32 v38, 1, v18\n\t"
    "v_lshl_add_u32 v38, v38, 4, v17\n\t"
    "ds_write_b64 v38, v[36:37]\n\t"
    "v_accvgpr_read_b32 v32, a24\n\t"
    "v_accvgpr_read_b32 v33, a25\n\t"
    "v_accvgpr_read_b32 v34, a26\n\t"
    "v_accvgpr_read_b32 v35, a27\n\t"
    "v_mul_f32_e32 v32, v31, v32\n\t"
    "v_mul_f32_e32 v33, v31, v33\n\t"
    "v_mul_f32_e32 v34, v31, v34\n\t"
    "v_mul_f32_e32 v35, v31, v35\n\t"
    "v_cvt_pk_bf16_f32 v36, v32, v33\n\t"
    "v_cvt_pk_bf16_f32 v37, v34, v35\n\t"
    "v_xor_b32_e32 v38, 2, v18\n\t"
    "v_lshl_add_u32 v38, v38, 4, v17\n\t"
    "ds_write_b64 v38, v[36:37]\n\t"
    "v_accvgpr_read_b32 v32, a28\n\t"
    "v_accvgpr_read_b32 v33, a29\n\t"
    "v_accvgpr_read_b32 v34, a30\n\t"
    "v_accvgpr_read_b32 v35, a31\n\t"
    "v_mul_f32_e32 v32, v31, v32\n\t"
    "v_mul_f32_e32 v33, v31, v33\n\t"
    "v_mul_f32_e32 v34, v31, v34\n\t"
    "v_mul_f32_e32 v35, v31, v35\n\t"
    "v_cvt_pk_bf16_f32 v36, v32, v33\n\t"
    "v_cvt_pk_bf16_f32 v37, v34, v35\n\t"
    "v_xor_b32_e32 v38, 3, v18\n\t"
    "v_lshl_add_u32 v38, v38, 4, v17\n\t"
    "ds_write_b64 v38, v[36:37]\n\t"
    "v_accvgpr_read_b32 v32, a48\n\t"
    "v_accvgpr_read_b32 v33, a49\n\t"
    "v_accvgpr_read_b32 v34, a50\n\t"
    "v_accvgpr_read_b32 v35, a51\n\t"
    "v_mul_f32_e32 v32, v31, v32\n\t"
    "v_mul_f32_e32 v33, v31, v33\n\t"
    "v_mul_f32_e32 v34, v31, v34\n\t"
    "v_mul_f32_e32 v35, v31, v35\n\t"
    "v_cvt_pk_bf16_f32 v36, v32, v33\n\t"
    "v_cvt_pk_bf16_f32 v37, v34, v35\n\t"
    "v_xor_b32_e32 v38, 4, v18\n\t"
    "v_lshl_add_u32 v38, v38, 4, v17\n\t"
    "ds_write_b64 v38, v[36:37]\n\t"
    "v_accvgpr_read_b32 v32, a52\n\t"
    "v_accvgpr_read_b32 v33, a53\n\t"
    "v_accvgpr_read_b32 v34, a54\n\t"
    "v_accvgpr_read_b32 v35, a55\n\t"
    "v_mul_f32_e32 v32, v31, v32\n\t"
    "v_mul_f32_e32 v33, v31, v33\n\t"
    "v_mul_f32_e32 v34, v31, v34\n\t"
    "v_mul_f32_e32 v35, v31, v35\n\t"
    "v_cvt_pk_bf16_f32 v36, v32, v33\n\t"
    "v_cvt_pk_bf16_f32 v37, v34, v35\n\t"
    "v_xor_b32_e32 v38, 5, v18\n\t"
    "v_lshl_add_u32 v38, v38, 4, v17\n\t"
    "ds_write_b64 v38, v[36:37]\n\t"
    "v_accvgpr_read_b32 v32, a56\n\t"
    "v_accvgpr_read_b32 v33, a57\n\t"
    "v_accvgpr_read_b32 v34, a58\n\t"
    "v_accvgpr_read_b32 v35, a59\n\t"
    "v_mul_f32_e32 v32, v31, v32\n\t"
    "v_mul_f32_e32 v33, v31, v33\n\t"
    "v_mul_f32_e32 v34, v31, v34\n\t"
    "v_mul_f32_e32 v35, v31, v35\n\t"
    "v_cvt_pk_bf16_f32 v36, v32, v33\n\t"
    "v_cvt_pk_bf16_f32 v37, v34, v35\n\t"
    "v_xor_b32_e32 v38, 6, v18\n\t"
    "v_lshl_add_u32 v38, v38, 4, v17\n\t"
    "ds_write_b64 v38, v[36:37]\n\t"
    "v_accvgpr_read_b32 v32, a60\n\t"
    "v_accvgpr_read_b32 v33, a61\n\t"
    "v_accvgpr_read_b32 v34, a62\n\t"
    "v_accvgpr_read_b32 v35, a63\n\t"
    "v_mul_f32_e32 v32, v31, v32\n\t"
    "v_mul_f32_e32 v33, v31, v33\n\t"
    "v_mul_f32_e32 v34, v31, v34\n\t"
    "v_mul_f32_e32 v35, v31, v35\n\t"
    "v_cvt_pk_bf16_f32 v36, v32, v33\n\t"
    "v_cvt_pk_bf16_f32 v37, v34, v35\n\t"
    "v_xor_b32_e32 v38, 7, v18\n\t"
    "v_lshl_add_u32 v38, v38, 4, v17\n\t"
    "ds_write_b64 v38, v[36:37]\n\t"
    "v_accvgpr_read_b32 v32, a80\n\t"
    "v_accvgpr_read_b32 v33, a81\n\t"
    "v_accvgpr_read_b32 v34, a82\n\t"
    "v_accvgpr_read_b32 v35, a83\n\t"
    "v_mul_f32_e32 v32, v31, v32\n\t"
    "v_mul_f32_e32 v33, v31, v33\n\t"
    "v_mul_f32_e32 v34, v31, v34\n\t"
    "v_mul_f32_e32 v35, v31, v35\n\t"
    "v_cvt_pk_bf16_f32 v36, v32, v33\n\t"
    "v_cvt_pk_bf16_f32 v37, v34, v35\n\t"
    "v_xor_b32_e32 v38, 8, v18\n\t"
    "v_lshl_add_u32 v38, v38, 4, v17\n\t"
    "ds_write_b64 v38, v[36:37]\n\t"
    "v_accvgpr_read_b32 v32, a84\n\t"
    "v_accvgpr_read_b32 v33, a85\n\t"
    "v_accvgpr_read_b32 v34, a86\n\t"
    "v_accvgpr_read_b32 v35, a87\n\t"
    "v_mul_f32_e32 v32, v31, v32\n\t"
    "v_mul_f32_e32 v33, v31, v33\n\t"
    "v_mul_f32_e32 v34, v31, v34\n\t"
    "v_mul_f32_e32 v35, v31, v35\n\t"
    "v_cvt_pk_bf16_f32 v36, v32, v33\n\t"
    "v_cvt_pk_bf16_f32 v37, v34, v35\n\t"
    "v_xor_b32_e32 v38, 9, v18\n\t"
    "v_lshl_add_u32 v38, v38, 4, v17\n\t"
    "ds_write_b64 v38, v[36:37]\n\t"
    "v_accvgpr_read_b32 v32, a88\n\t"
    "v_accvgpr_read_b32 v33, a89\n\t"
    "v_accvgpr_read_b32 v34, a90\n\t"
    "v_accvgpr_read_b32 v35, a91\n\t"
    "v_mul_f32_e32 v32, v31, v32\n\t"
    "v_mul_f32_e32 v33, v31, v33\n\t"
    "v_mul_f32_e32 v34, v31, v34\n\t"
    "v_mul_f32_e32 v35, v31, v35\n\t"
    "v_cvt_pk_bf16_f32 v36, v32, v33\n\t"
    "v_cvt_pk_bf16_f32 v37, v34, v35\n\t"
    "v_xor_b32_e32 v38, 10, v18\n\t"
    "v_lshl_add_u32 v38, v38, 4, v17\n\t"
    "ds_write_b64 v38, v[36:37]\n\t"
    "v_accvgpr_read_b32 v32, a92\n\t"
    "v_accvgpr_read_b32 v33, a93\n\t"
    "v_accvgpr_read_b32 v34, a94\n\t"
    "v_accvgpr_read_b32 v35, a95\n\t"
    "v_mul_f32_e32 v32, v31, v32\n\t"
    "v_mul_f32_e32 v33, v31, v33\n\t"
    "v_mul_f32_e32 v34, v31, v34\n\t"
    "v_mul_f32_e32 v35, v31, v35\n\t"
    "v_cvt_pk_bf16_f32 v36, v32, v33\n\t"
    "v_cvt_pk_bf16_f32 v37, v34, v35\n\t"
    "v_xor_b32_e32 v38, 11, v18\n\t"
    "v_lshl_add_u32 v38, v38, 4, v17\n\t"
    "ds_write_b64 v38, v[36:37]\n\t"
    "v_accvgpr_read_b32 v32, a112\n\t"
    "v_accvgpr_read_b32 v33, a113\n\t"
    "v_accvgpr_read_b32 v34, a114\n\t"
    "v_accvgpr_read_b32 v35, a115\n\t"
    "v_mul_f32_e32 v32, v31, v32\n\t"
    "v_mul_f32_e32 v33, v31, v33\n\t"
    "v_mul_f32_e32 v34, v31, v34\n\t"
    "v_mul_f32_e32 v35, v31, v35\n\t"
    "v_cvt_pk_bf16_f32 v36, v32, v33\n\t"
    "v_cvt_pk_bf16_f32 v37, v34, v35\n\t"
    "v_xor_b32_e32 v38, 12, v18\n\t"
    "v_lshl_add_u32 v38, v38, 4, v17\n\t"
    "ds_write_b64 v38, v[36:37]\n\t"
    "v_accvgpr_read_b32 v32, a116\n\t"
    "v_accvgpr_read_b32 v33, a117\n\t"
    "v_accvgpr_read_b32 v34, a118\n\t"
    "v_accvgpr_read_b32 v35, a119\n\t"
    "v_mul_f32_e32 v32, v31, v32\n\t"
    "v_mul_f32_e32 v33, v31, v33\n\t"
    "v_mul_f32_e32 v34, v31, v34\n\t"
    "v_mul_f32_e32 v35, v31, v35\n\t"
    "v_cvt_pk_bf16_f32 v36, v32, v33\n\t"
    "v_cvt_pk_bf16_f32 v37, v34, v35\n\t"
    "v_xor_b32_e32 v38, 13, v18\n\t"
    "v_lshl_add_u32 v38, v38, 4, v17\n\t"
    "ds_write_b64 v38, v[36:37]\n\t"
    "v_accvgpr_read_b32 v32, a120\n\t"
    "v_accvgpr_read_b32 v33, a121\n\t"
    "v_accvgpr_read_b32 v34, a122\n\t"
    "v_accvgpr_read_b32 v35, a123\n\t"
    "v_mul_f32_e32 v32, v31, v32\n\t"
    "v_mul_f32_e32 v33, v31, v33\n\t"
    "v_mul_f32_e32 v34, v31, v34\n\t"
    "v_mul_f32_e32 v35, v31, v35\n\t"
    "v_cvt_pk_bf16_f32 v36, v32, v33\n\t"
    "v_cvt_pk_bf16_f32 v37, v34, v35\n\t"
    "v_xor_b32_e32 v38, 14, v18\n\t"
    "v_lshl_add_u32 v38, v38, 4, v17\n\t"
    "ds_write_b64 v38, v[36:37]\n\t"
    "v_accvgpr_read_b32 v32, a124\n\t"
    "v_accvgpr_read_b32 v33, a125\n\t"
    "v_accvgpr_read_b32 v34, a126\n\t"
    "v_accvgpr_read_b32 v35, a127\n\t"
    "v_mul_f32_e32 v32, v31, v32\n\t"
    "v_mul_f32_e32 v33, v31, v33\n\t"
    "v_mul_f32_e32 v34, v31, v34\n\t"
    "v_mul_f32_e32 v35, v31, v35\n\t"
    "v_cvt_pk_bf16_f32 v36, v32, v33\n\t"
    "v_cvt_pk_bf16_f32 v37, v34, v35\n\t"
    "v_xor_b32_e32 v38, 15, v18\n\t"
    "v_lshl_add_u32 v38, v38, 4, v17\n\t"
    "ds_write_b64 v38, v[36:37]\n\t"
    "s_waitcnt lgkmcnt(0)\n\t"
    "v_add_u32_e32 v38, 0, v19\n\t"
    "v_and_b32_e32 v39, 15, v38\n\t"
    "v_xor_b32_e32 v39, v39, v2\n\t"
    "v_lshlrev_b32_e32 v39, 4, v39\n\t"
    "v_lshl_add_u32 v38, v38, 8, v39\n\t"
    "v_add_u32_e32 v38, s52, v38\n\t"
    "ds_read_b128 v[40:43], v38\n\t"
    "v_add_u32_e32 v38, 4, v19\n\t"
    "v_and_b32_e32 v39, 15, v38\n\t"
    "v_xor_b32_e32 v39, v39, v2\n\t"
    "v_lshlrev_b32_e32 v39, 4, v39\n\t"
    "v_lshl_add_u32 v38, v38, 8, v39\n\t"
    "v_add_u32_e32 v38, s52, v38\n\t"
    "ds_read_b128 v[44:47], v38\n\t"
    "v_add_u32_e32 v38, 8, v19\n\t"
    "v_and_b32_e32 v39, 15, v38\n\t"
    "v_xor_b32_e32 v39, v39, v2\n\t"
    "v_lshlrev_b32_e32 v39, 4, v39\n\t"
    "v_lshl_add_u32 v38, v38, 8, v39\n\t"
    "v_add_u32_e32 v38, s52, v38\n\t"
    "ds_read_b128 v[48:51], v38\n\t"
    "v_add_u32_e32 v38, 12, v19\n\t"
    "v_and_b32_e32 v39, 15, v38\n\t"
    "v_xor_b32_e32 v39, v39, v2\n\t"
    "v_lshlrev_b32_e32 v39, 4, v39\n\t"
    "v_lshl_add_u32 v38, v38, 8, v39\n\t"
    "v_add_u32_e32 v38, s52, v38\n\t"
    "ds_read_b128 v[52:55], v38\n\t"
    "v_add_u32_e32 v38, 16, v19\n\t"
    "v_and_b32_e32 v39, 15, v38\n\t"
    "v_xor_b32_e32 v39, v39, v2\n\t"
    "v_lshlrev_b32_e32 v39, 4, v39\n\t"
    "v_lshl_add_u32 v38, v38, 8, v39\n\t"
    "v_add_u32_e32 v38, s52, v38\n\t"
    "ds_read_b128 v[56:59], v38\n\t"
    "v_add_u32_e32 v38, 20, v19\n\t"
    "v_and_b32_e32 v39, 15, v38\n\t"
    "v_xor_b32_e32 v39, v39, v2\n\t"
    "v_lshlrev_b32_e32 v39, 4, v39\n\t"
    "v_lshl_add_u32 v38, v38, 8, v39\n\t"
    "v_add_u32_e32 v38, s52, v38\n\t"
    "ds_read_b128 v[60:63], v38\n\t"
    "v_add_u32_e32 v38, 24, v19\n\t"
    "v_and_b32_e32 v39, 15, v38\n\t"
    "v_xor_b32_e32 v39, v39, v2\n\t"
    "v_lshlrev_b32_e32 v39, 4, v39\n\t"
    "v_lshl_add_u32 v38, v38, 8, v39\n\t"
    "v_add_u32_e32 v38, s52, v38\n\t"
    "ds_read_b128 v[64:67], v38\n\t"
    "v_add_u32_e32 v38, 28, v19\n\t"
    "v_and_b32_e32 v39, 15, v38\n\t"
    "v_xor_b32_e32 v39, v39, v2\n\t"
    "v_lshlrev_b32_e32 v39, 4, v39\n\t"
    "v_lshl_add_u32 v38, v38, 8, v39\n\t"
    "v_add_u32_e32 v38, s52, v38\n\t"
    "ds_read_b128 v[68:71], v38\n\t"
    "s_add_u32 s53, s50, 8192\n\t"
    "s_add_u32 s51, s50, 12288\n\t"
    "s_waitcnt lgkmcnt(7)\n\t"
    "buffer_store_dwordx4 v[40:43], v16, s[36:39], s53 offen offset:0\n\t"
    "s_waitcnt lgkmcnt(6)\n\t"
    "buffer_store_dwordx4 v[44:47], v16, s[36:39], s53 offen offset:1024\n\t"
    "s_waitcnt lgkmcnt(5)\n\t"
    "buffer_store_dwordx4 v[48:51], v16, s[36:39], s53 offen offset:2048\n\t"
    "s_waitcnt lgkmcnt(4)\n\t"
    "buffer_store_dwordx4 v[52:55], v16, s[36:39], s53 offen offset:3072\n\t"
    "s_waitcnt lgkmcnt(3)\n\t"
    "buffer_store_dwordx4 v[56:59], v16, s[36:39], s51 offen offset:0\n\t"
    "s_waitcnt lgkmcnt(2)\n\t"
    "buffer_store_dwordx4 v[60:63], v16, s[36:39], s51 offen offset:1024\n\t"
    "s_waitcnt lgkmcnt(1)\n\t"
    "buffer_store_dwordx4 v[64:67], v16, s[36:39], s51 offen offset:2048\n\t"
    "s_waitcnt lgkmcnt(0)\n\t"
    "buffer_store_dwordx4 v[68:71], v16, s[36:39], s51 offen offset:3072\n\t"
    "s_nop 1\n\t"
    "s_waitcnt vmcnt(0)\n\t"
    "s_branch L_END\n\t"
    "L_RARE_p1:\n\t"
    "s_nop 1\n\t"
    "v_max_f32_e32 v17, 0, v30\n\t"
    "v_add_f32_e32 v24, v24, v17\n\t"
    "v_sub_f32_e32 v96, v96, v17\n\t"
    "v_sub_f32_e32 v97, v97, v17\n\t"
    "v_sub_f32_e32 v98, v98, v17\n\t"
    "v_sub_f32_e32 v99, v99, v17\n\t"
    "v_sub_f32_e32 v100, v100, v17\n\t"
    "v_sub_f32_e32 v101, v101, v17\n\t"
    "v_sub_f32_e32 v102, v102, v17\n\t"
    "v_sub_f32_e32 v103, v103, v17\n\t"
    "v_sub_f32_e32 v104, v104, v17\n\t"
    "v_sub_f32_e32 v105, v105, v17\n\t"
    "v_sub_f32_e32 v106, v106, v17\n\t"
    "v_sub_f32_e32 v107, v107, v17\n\t"
    "v_sub_f32_e32 v108, v108, v17\n\t"
    "v_sub_f32_e32 v109, v109, v17\n\t"
    "v_sub_f32_e32 v110, v110, v17\n\t"
    "v_sub_f32_e32 v111, v111, v17\n\t"
    "v_sub_f32_e32 v128, v128, v17\n\t"
    "v_sub_f32_e32 v129, v129, v17\n\t"
    "v_sub_f32_e32 v130, v130, v17\n\t"
    "v_sub_f32_e32 v131, v131, v17\n\t"
    "v_sub_f32_e32 v132, v132, v17\n\t"
    "v_sub_f32_e32 v133, v133, v17\n\t"
    "v_sub_f32_e32 v134, v134, v17\n\t"
    "v_sub_f32_e32 v135, v135, v17\n\t"
    "v_sub_f32_e32 v136, v136, v17\n\t"
    "v_sub_f32_e32 v137, v137, v17\n\t"
    "v_sub_f32_e32 v138, v138, v17\n\t"
    "v_sub_f32_e32 v139, v139, v17\n\t"
    "v_sub_f32_e32 v140, v140, v17\n\t"
    "v_sub_f32_e32 v141, v141, v17\n\t"
    "v_sub_f32_e32 v142, v142, v17\n\t"
    "v_sub_f32_e32 v143, v143, v17\n\t"
    "v_sub_f32_e32 v160, 0, v24\n\t"
    "v_sub_f32_e32 v161, 0, v24\n\t"
    "v_sub_f32_e32 v162, 0, v24\n\t"
    "v_sub_f32_e32 v163, 0, v24\n\t"
    "v_sub_f32_e32 v164, 0, v24\n\t"
    "v_sub_f32_e32 v165, 0, v24\n\t"
    "v_sub_f32_e32 v166, 0, v24\n\t"
    "v_sub_f32_e32 v167, 0, v24\n\t"
    "v_sub_f32_e32 v168, 0, v24\n\t"
    "v_sub_f32_e32 v169, 0, v24\n\t"
    "v_sub_f32_e32 v170, 0, v24\n\t"
    "v_sub_f32_e32 v171, 0, v24\n\t"
    "v_sub_f32_e32 v172, 0, v24\n\t"
    "v_sub_f32_e32 v173, 0, v24\n\t"
    "v_sub_f32_e32 v174, 0, v24\n\t"
    "v_sub_f32_e32 v175, 0, v24\n\t"
    "v_sub_f32_e32 v17, 0, v17\n\t"
    "v_exp_f32_e32 v17, v17\n\t"
    "s_nop 0\n\t"
    "v_mul_f32_e32 v20, v17, v20\n\t"
    "v_mul_f32_e32 v22, v17, v22\n\t"
    "v_max_f32_e32 v18, 0, v31\n\t"
    "v_add_f32_e32 v25, v25, v18\n\t"
    "v_sub_f32_e32 v112, v112, v18\n\t"
    "v_sub_f32_e32 v113, v113, v18\n\t"
    "v_sub_f32_e32 v114, v114, v18\n\t"
    "v_sub_f32_e32 v115, v115, v18\n\t"
    "v_sub_f32_e32 v116, v116, v18\n\t"
    "v_sub_f32_e32 v117, v117, v18\n\t"
    "v_sub_f32_e32 v118, v118, v18\n\t"
    "v_sub_f32_e32 v119, v119, v18\n\t"
    "v_sub_f32_e32 v120, v120, v18\n\t"
    "v_sub_f32_e32 v121, v121, v18\n\t"
    "v_sub_f32_e32 v122, v122, v18\n\t"
    "v_sub_f32_e32 v123, v123, v18\n\t"
    "v_sub_f32_e32 v124, v124, v18\n\t"
    "v_sub_f32_e32 v125, v125, v18\n\t"
    "v_sub_f32_e32 v126, v126, v18\n\t"
    "v_sub_f32_e32 v127, v127, v18\n\t"
    "v_sub_f32_e32 v144, v144, v18\n\t"
    "v_sub_f32_e32 v145, v145, v18\n\t"
    "v_sub_f32_e32 v146, v146, v18\n\t"
    "v_sub_f32_e32 v147, v147, v18\n\t"
    "v_sub_f32_e32 v148, v148, v18\n\t"
    "v_sub_f32_e32 v149, v149, v18\n\t"
    "v_sub_f32_e32 v150, v150, v18\n\t"
    "v_sub_f32_e32 v151, v151, v18\n\t"
    "v_sub_f32_e32 v152, v152, v18\n\t"
    "v_sub_f32_e32 v153, v153, v18\n\t"
    "v_sub_f32_e32 v154, v154, v18\n\t"
    "v_sub_f32_e32 v155, v155, v18\n\t"
    "v_sub_f32_e32 v156, v156, v18\n\t"
    "v_sub_f32_e32 v157, v157, v18\n\t"
    "v_sub_f32_e32 v158, v158, v18\n\t"
    "v_sub_f32_e32 v159, v159, v18\n\t"
    "v_sub_f32_e32 v176, 0, v25\n\t"
    "v_sub_f32_e32 v177, 0, v25\n\t"
    "v_sub_f32_e32 v178, 0, v25\n\t"
    "v_sub_f32_e32 v179, 0, v25\n\t"
    "v_sub_f32_e32 v180, 0, v25\n\t"
    "v_sub_f32_e32 v181, 0, v25\n\t"
    "v_sub_f32_e32 v182, 0, v25\n\t"
    "v_sub_f32_e32 v183, 0, v25\n\t"
    "v_sub_f32_e32 v184, 0, v25\n\t"
    "v_sub_f32_e32 v185, 0, v25\n\t"
    "v_sub_f32_e32 v186, 0, v25\n\t"
    "v_sub_f32_e32 v187, 0, v25\n\t"
    "v_sub_f32_e32 v188, 0, v25\n\t"
    "v_sub_f32_e32 v189, 0, v25\n\t"
    "v_sub_f32_e32 v190, 0, v25\n\t"
    "v_sub_f32_e32 v191, 0, v25\n\t"
    "v_sub_f32_e32 v18, 0, v18\n\t"
    "v_exp_f32_e32 v18, v18\n\t"
    "s_nop 0\n\t"
    "v_mul_f32_e32 v21, v18, v21\n\t"
    "v_mul_f32_e32 v23, v18, v23\n\t"
    "s_mov_b32 s47, 1\n\t"
    "s_nop 1\n\t"
    "s_branch L_RET_p1\n\t"
    "L_RESC_p1:\n\t"
    "s_nop 7\n\t"
    "s_nop 7\n\t"
    "v_accvgpr_read_b32 v5, a0\n\t"
    "v_mul_f32_e32 v5, v17, v5\n\t"
    "v_accvgpr_write_b32 a0, v5\n\t"
    "v_accvgpr_read_b32 v6, a1\n\t"
    "v_mul_f32_e32 v6, v17, v6\n\t"
    "v_accvgpr_write_b32 a1, v6\n\t"
    "v_accvgpr_read_b32 v5, a2\n\t"
    "v_mul_f32_e32 v5, v17, v5\n\t"
    "v_accvgpr_write_b32 a2, v5\n\t"
    "v_accvgpr_read_b32 v6, a3\n\t"
    "v_mul_f32_e32 v6, v17, v6\n\t"
    "v_accvgpr_write_b32 a3, v6\n\t"
    "v_accvgpr_read_b32 v5, a4\n\t"
    "v_mul_f32_e32 v5, v17, v5\n\t"
    "v_accvgpr_write_b32 a4, v5\n\t"
    "v_accvgpr_read_b32 v6, a5\n\t"
    "v_mul_f32_e32 v6, v17, v6\n\t"
    "v_accvgpr_write_b32 a5, v6\n\t"
    "v_accvgpr_read_b32 v5, a6\n\t"
    "v_mul_f32_e32 v5, v17, v5\n\t"
    "v_accvgpr_write_b32 a6, v5\n\t"
    "v_accvgpr_read_b32 v6, a7\n\t"
    "v_mul_f32_e32 v6, v17, v6\n\t"
    "v_accvgpr_write_b32 a7, v6\n\t"
    "v_accvgpr_read_b32 v5, a8\n\t"
    "v_mul_f32_e32 v5, v17, v5\n\t"
    "v_accvgpr_write_b32 a8, v5\n\t"
    "v_accvgpr_read_b32 v6, a9\n\t"
    "v_mul_f32_e32 v6, v17, v6\n\t"
    "v_accvgpr_write_b32 a9, v6\n\t"
    "v_accvgpr_read_b32 v5, a10\n\t"
    "v_mul_f32_e32 v5, v17, v5\n\t"
    "v_accvgpr_write_b32 a10, v5\n\t"
    "v_accvgpr_read_b32 v6, a11\n\t"
    "v_mul_f32_e32 v6, v17, v6\n\t"
    "v_accvgpr_write_b32 a11, v6\n\t"
    "v_accvgpr_read_b32 v5, a12\n\t"
    "v_mul_f32_e32 v5, v17, v5\n\t"
    "v_accvgpr_write_b32 a12, v5\n\t"
    "v_accvgpr_read_b32 v6, a13\n\t"
    "v_mul_f32_e32 v6, v17, v6\n\t"
    "v_accvgpr_write_b32 a13, v6\n\t"
    "v_accvgpr_read_b32 v5, a14\n\t"
    "v_mul_f32_e32 v5, v17, v5\n\t"
    "v_accvgpr_write_b32 a14, v5\n\t"
    "v_accvgpr_read_b32 v6, a15\n\t"
    "v_mul_f32_e32 v6, v17, v6\n\t"
    "v_accvgpr_write_b32 a15, v6\n\t"
    "v_accvgpr_read_b32 v5, a16\n\t"
    "v_mul_f32_e32 v5, v18, v5\n\t"
    "v_accvgpr_write_b32 a16, v5\n\t"
    "v_accvgpr_read_b32 v6, a17\n\t"
    "v_mul_f32_e32 v6, v18, v6\n\t"
    "v_accvgpr_write_b32 a17, v6\n\t"
    "v_accvgpr_read_b32 v5, a18\n\t"
    "v_mul_f32_e32 v5, v18, v5\n\t"
    "v_accvgpr_write_b32 a18, v5\n\t"
    "v_accvgpr_read_b32 v6, a19\n\t"
    "v_mul_f32_e32 v6, v18, v6\n\t"
    "v_accvgpr_write_b32 a19, v6\n\t"
    "v_accvgpr_read_b32 v5, a20\n\t"
    "v_mul_f32_e32 v5, v18, v5\n\t"
    "v_accvgpr_write_b32 a20, v5\n\t"
    "v_accvgpr_read_b32 v6, a21\n\t"
    "v_mul_f32_e32 v6, v18, v6\n\t"
    "v_accvgpr_write_b32 a21, v6\n\t"
    "v_accvgpr_read_b32 v5, a22\n\t"
    "v_mul_f32_e32 v5, v18, v5\n\t"
    "v_accvgpr_write_b32 a22, v5\n\t"
    "v_accvgpr_read_b32 v6, a23\n\t"
    "v_mul_f32_e32 v6, v18, v6\n\t"
    "v_accvgpr_write_b32 a23, v6\n\t"
    "v_accvgpr_read_b32 v5, a24\n\t"
    "v_mul_f32_e32 v5, v18, v5\n\t"
    "v_accvgpr_write_b32 a24, v5\n\t"
    "v_accvgpr_read_b32 v6, a25\n\t"
    "v_mul_f32_e32 v6, v18, v6\n\t"
    "v_accvgpr_write_b32 a25, v6\n\t"
    "v_accvgpr_read_b32 v5, a26\n\t"
    "v_mul_f32_e32 v5, v18, v5\n\t"
    "v_accvgpr_write_b32 a26, v5\n\t"
    "v_accvgpr_read_b32 v6, a27\n\t"
    "v_mul_f32_e32 v6, v18, v6\n\t"
    "v_accvgpr_write_b32 a27, v6\n\t"
    "v_accvgpr_read_b32 v5, a28\n\t"
    "v_mul_f32_e32 v5, v18, v5\n\t"
    "v_accvgpr_write_b32 a28, v5\n\t"
    "v_accvgpr_read_b32 v6, a29\n\t"
    "v_mul_f32_e32 v6, v18, v6\n\t"
    "v_accvgpr_write_b32 a29, v6\n\t"
    "v_accvgpr_read_b32 v5, a30\n\t"
    "v_mul_f32_e32 v5, v18, v5\n\t"
    "v_accvgpr_write_b32 a30, v5\n\t"
    "v_accvgpr_read_b32 v6, a31\n\t"
    "v_mul_f32_e32 v6, v18, v6\n\t"
    "v_accvgpr_write_b32 a31, v6\n\t"
    "v_accvgpr_read_b32 v5, a32\n\t"
    "v_mul_f32_e32 v5, v17, v5\n\t"
    "v_accvgpr_write_b32 a32, v5\n\t"
    "v_accvgpr_read_b32 v6, a33\n\t"
    "v_mul_f32_e32 v6, v17, v6\n\t"
    "v_accvgpr_write_b32 a33, v6\n\t"
    "v_accvgpr_read_b32 v5, a34\n\t"
    "v_mul_f32_e32 v5, v17, v5\n\t"
    "v_accvgpr_write_b32 a34, v5\n\t"
    "v_accvgpr_read_b32 v6, a35\n\t"
    "v_mul_f32_e32 v6, v17, v6\n\t"
    "v_accvgpr_write_b32 a35, v6\n\t"
    "v_accvgpr_read_b32 v5, a36\n\t"
    "v_mul_f32_e32 v5, v17, v5\n\t"
    "v_accvgpr_write_b32 a36, v5\n\t"
    "v_accvgpr_read_b32 v6, a37\n\t"
    "v_mul_f32_e32 v6, v17, v6\n\t"
    "v_accvgpr_write_b32 a37, v6\n\t"
    "v_accvgpr_read_b32 v5, a38\n\t"
    "v_mul_f32_e32 v5, v17, v5\n\t"
    "v_accvgpr_write_b32 a38, v5\n\t"
    "v_accvgpr_read_b32 v6, a39\n\t"
    "v_mul_f32_e32 v6, v17, v6\n\t"
    "v_accvgpr_write_b32 a39, v6\n\t"
    "v_accvgpr_read_b32 v5, a40\n\t"
    "v_mul_f32_e32 v5, v17, v5\n\t"
    "v_accvgpr_write_b32 a40, v5\n\t"
    "v_accvgpr_read_b32 v6, a41\n\t"
    "v_mul_f32_e32 v6, v17, v6\n\t"
    "v_accvgpr_write_b32 a41, v6\n\t"
    "v_accvgpr_read_b32 v5, a42\n\t"
    "v_mul_f32_e32 v5, v17, v5\n\t"
    "v_accvgpr_write_b32 a42, v5\n\t"
    "v_accvgpr_read_b32 v6, a43\n\t"
    "v_mul_f32_e32 v6, v17, v6\n\t"
    "v_accvgpr_write_b32 a43, v6\n\t"
    "v_accvgpr_read_b32 v5, a44\n\t"
    "v_mul_f32_e32 v5, v17, v5\n\t"
    "v_accvgpr_write_b32 a44, v5\n\t"
    "v_accvgpr_read_b32 v6, a45\n\t"
    "v_mul_f32_e32 v6, v17, v6\n\t"
    "v_accvgpr_write_b32 a45, v6\n\t"
    "v_accvgpr_read_b32 v5, a46\n\t"
    "v_mul_f32_e32 v5, v17, v5\n\t"
    "v_accvgpr_write_b32 a46, v5\n\t"
    "v_accvgpr_read_b32 v6, a47\n\t"
    "v_mul_f32_e32 v6, v17, v6\n\t"
    "v_accvgpr_write_b32 a47, v6\n\t"
    "v_accvgpr_read_b32 v5, a48\n\t"
    "v_mul_f32_e32 v5, v18, v5\n\t"
    "v_accvgpr_write_b32 a48, v5\n\t"
    "v_accvgpr_read_b32 v6, a49\n\t"
    "v_mul_f32_e32 v6, v18, v6\n\t"
    "v_accvgpr_write_b32 a49, v6\n\t"
    "v_accvgpr_read_b32 v5, a50\n\t"
    "v_mul_f32_e32 v5, v18, v5\n\t"
    "v_accvgpr_write_b32 a50, v5\n\t"
    "v_accvgpr_read_b32 v6, a51\n\t"
    "v_mul_f32_e32 v6, v18, v6\n\t"
    "v_accvgpr_write_b32 a51, v6\n\t"
    "v_accvgpr_read_b32 v5, a52\n\t"
    "v_mul_f32_e32 v5, v18, v5\n\t"
    "v_accvgpr_write_b32 a52, v5\n\t"
    "v_accvgpr_read_b32 v6, a53\n\t"
    "v_mul_f32_e32 v6, v18, v6\n\t"
    "v_accvgpr_write_b32 a53, v6\n\t"
    "v_accvgpr_read_b32 v5, a54\n\t"
    "v_mul_f32_e32 v5, v18, v5\n\t"
    "v_accvgpr_write_b32 a54, v5\n\t"
    "v_accvgpr_read_b32 v6, a55\n\t"
    "v_mul_f32_e32 v6, v18, v6\n\t"
    "v_accvgpr_write_b32 a55, v6\n\t"
    "v_accvgpr_read_b32 v5, a56\n\t"
    "v_mul_f32_e32 v5, v18, v5\n\t"
    "v_accvgpr_write_b32 a56, v5\n\t"
    "v_accvgpr_read_b32 v6, a57\n\t"
    "v_mul_f32_e32 v6, v18, v6\n\t"
    "v_accvgpr_write_b32 a57, v6\n\t"
    "v_accvgpr_read_b32 v5, a58\n\t"
    "v_mul_f32_e32 v5, v18, v5\n\t"
    "v_accvgpr_write_b32 a58, v5\n\t"
    "v_accvgpr_read_b32 v6, a59\n\t"
    "v_mul_f32_e32 v6, v18, v6\n\t"
    "v_accvgpr_write_b32 a59, v6\n\t"
    "v_accvgpr_read_b32 v5, a60\n\t"
    "v_mul_f32_e32 v5, v18, v5\n\t"
    "v_accvgpr_write_b32 a60, v5\n\t"
    "v_accvgpr_read_b32 v6, a61\n\t"
    "v_mul_f32_e32 v6, v18, v6\n\t"
    "v_accvgpr_write_b32 a61, v6\n\t"
    "v_accvgpr_read_b32 v5, a62\n\t"
    "v_mul_f32_e32 v5, v18, v5\n\t"
    "v_accvgpr_write_b32 a62, v5\n\t"
    "v_accvgpr_read_b32 v6, a63\n\t"
    "v_mul_f32_e32 v6, v18, v6\n\t"
    "v_accvgpr_write_b32 a63, v6\n\t"
    "v_accvgpr_read_b32 v5, a64\n\t"
    "v_mul_f32_e32 v5, v17, v5\n\t"
    "v_accvgpr_write_b32 a64, v5\n\t"
    "v_accvgpr_read_b32 v6, a65\n\t"
    "v_mul_f32_e32 v6, v17, v6\n\t"
    "v_accvgpr_write_b32 a65, v6\n\t"
    "v_accvgpr_read_b32 v5, a66\n\t"
    "v_mul_f32_e32 v5, v17, v5\n\t"
    "v_accvgpr_write_b32 a66, v5\n\t"
    "v_accvgpr_read_b32 v6, a67\n\t"
    "v_mul_f32_e32 v6, v17, v6\n\t"
    "v_accvgpr_write_b32 a67, v6\n\t"
    "v_accvgpr_read_b32 v5, a68\n\t"
    "v_mul_f32_e32 v5, v17, v5\n\t"
    "v_accvgpr_write_b32 a68, v5\n\t"
    "v_accvgpr_read_b32 v6, a69\n\t"
    "v_mul_f32_e32 v6, v17, v6\n\t"
    "v_accvgpr_write_b32 a69, v6\n\t"
    "v_accvgpr_read_b32 v5, a70\n\t"
    "v_mul_f32_e32 v5, v17, v5\n\t"
    "v_accvgpr_write_b32 a70, v5\n\t"
    "v_accvgpr_read_b32 v6, a71\n\t"
    "v_mul_f32_e32 v6, v17, v6\n\t"
    "v_accvgpr_write_b32 a71, v6\n\t"
    "v_accvgpr_read_b32 v5, a72\n\t"
    "v_mul_f32_e32 v5, v17, v5\n\t"
    "v_accvgpr_write_b32 a72, v5\n\t"
    "v_accvgpr_read_b32 v6, a73\n\t"
    "v_mul_f32_e32 v6, v17, v6\n\t"
    "v_accvgpr_write_b32 a73, v6\n\t"
    "v_accvgpr_read_b32 v5, a74\n\t"
    "v_mul_f32_e32 v5, v17, v5\n\t"
    "v_accvgpr_write_b32 a74, v5\n\t"
    "v_accvgpr_read_b32 v6, a75\n\t"
    "v_mul_f32_e32 v6, v17, v6\n\t"
    "v_accvgpr_write_b32 a75, v6\n\t"
    "v_accvgpr_read_b32 v5, a76\n\t"
    "v_mul_f32_e32 v5, v17, v5\n\t"
    "v_accvgpr_write_b32 a76, v5\n\t"
    "v_accvgpr_read_b32 v6, a77\n\t"
    "v_mul_f32_e32 v6, v17, v6\n\t"
    "v_accvgpr_write_b32 a77, v6\n\t"
    "v_accvgpr_read_b32 v5, a78\n\t"
    "v_mul_f32_e32 v5, v17, v5\n\t"
    "v_accvgpr_write_b32 a78, v5\n\t"
    "v_accvgpr_read_b32 v6, a79\n\t"
    "v_mul_f32_e32 v6, v17, v6\n\t"
    "v_accvgpr_write_b32 a79, v6\n\t"
    "v_accvgpr_read_b32 v5, a80\n\t"
    "v_mul_f32_e32 v5, v18, v5\n\t"
    "v_accvgpr_write_b32 a80, v5\n\t"
    "v_accvgpr_read_b32 v6, a81\n\t"
    "v_mul_f32_e32 v6, v18, v6\n\t"
    "v_accvgpr_write_b32 a81, v6\n\t"
    "v_accvgpr_read_b32 v5, a82\n\t"
    "v_mul_f32_e32 v5, v18, v5\n\t"
    "v_accvgpr_write_b32 a82, v5\n\t"
    "v_accvgpr_read_b32 v6, a83\n\t"
    "v_mul_f32_e32 v6, v18, v6\n\t"
    "v_accvgpr_write_b32 a83, v6\n\t"
    "v_accvgpr_read_b32 v5, a84\n\t"
    "v_mul_f32_e32 v5, v18, v5\n\t"
    "v_accvgpr_write_b32 a84, v5\n\t"
    "v_accvgpr_read_b32 v6, a85\n\t"
    "v_mul_f32_e32 v6, v18, v6\n\t"
    "v_accvgpr_write_b32 a85, v6\n\t"
    "v_accvgpr_read_b32 v5, a86\n\t"
    "v_mul_f32_e32 v5, v18, v5\n\t"
    "v_accvgpr_write_b32 a86, v5\n\t"
    "v_accvgpr_read_b32 v6, a87\n\t"
    "v_mul_f32_e32 v6, v18, v6\n\t"
    "v_accvgpr_write_b32 a87, v6\n\t"
    "v_accvgpr_read_b32 v5, a88\n\t"
    "v_mul_f32_e32 v5, v18, v5\n\t"
    "v_accvgpr_write_b32 a88, v5\n\t"
    "v_accvgpr_read_b32 v6, a89\n\t"
    "v_mul_f32_e32 v6, v18, v6\n\t"
    "v_accvgpr_write_b32 a89, v6\n\t"
    "v_accvgpr_read_b32 v5, a90\n\t"
    "v_mul_f32_e32 v5, v18, v5\n\t"
    "v_accvgpr_write_b32 a90, v5\n\t"
    "v_accvgpr_read_b32 v6, a91\n\t"
    "v_mul_f32_e32 v6, v18, v6\n\t"
    "v_accvgpr_write_b32 a91, v6\n\t"
    "v_accvgpr_read_b32 v5, a92\n\t"
    "v_mul_f32_e32 v5, v18, v5\n\t"
    "v_accvgpr_write_b32 a92, v5\n\t"
    "v_accvgpr_read_b32 v6, a93\n\t"
    "v_mul_f32_e32 v6, v18, v6\n\t"
    "v_accvgpr_write_b32 a93, v6\n\t"
    "v_accvgpr_read_b32 v5, a94\n\t"
    "v_mul_f32_e32 v5, v18, v5\n\t"
    "v_accvgpr_write_b32 a94, v5\n\t"
    "v_accvgpr_read_b32 v6, a95\n\t"
    "v_mul_f32_e32 v6, v18, v6\n\t"
    "v_accvgpr_write_b32 a95, v6\n\t"
    "v_accvgpr_read_b32 v5, a96\n\t"
    "v_mul_f32_e32 v5, v17, v5\n\t"
    "v_accvgpr_write_b32 a96, v5\n\t"
    "v_accvgpr_read_b32 v6, a97\n\t"
    "v_mul_f32_e32 v6, v17, v6\n\t"
    "v_accvgpr_write_b32 a97, v6\n\t"
    "v_accvgpr_read_b32 v5, a98\n\t"
    "v_mul_f32_e32 v5, v17, v5\n\t"
    "v_accvgpr_write_b32 a98, v5\n\t"
    "v_accvgpr_read_b32 v6, a99\n\t"
    "v_mul_f32_e32 v6, v17, v6\n\t"
    "v_accvgpr_write_b32 a99, v6\n\t"
    "v_accvgpr_read_b32 v5, a100\n\t"
    "v_mul_f32_e32 v5, v17, v5\n\t"
    "v_accvgpr_write_b32 a100, v5\n\t"
    "v_accvgpr_read_b32 v6, a101\n\t"
    "v_mul_f32_e32 v6, v17, v6\n\t"
    "v_accvgpr_write_b32 a101, v6\n\t"
    "v_accvgpr_read_b32 v5, a102\n\t"
    "v_mul_f32_e32 v5, v17, v5\n\t"
    "v_accvgpr_write_b32 a102, v5\n\t"
    "v_accvgpr_read_b32 v6, a103\n\t"
    "v_mul_f32_e32 v6, v17, v6\n\t"
    "v_accvgpr_write_b32 a103, v6\n\t"
    "v_accvgpr_read_b32 v5, a104\n\t"
    "v_mul_f32_e32 v5, v17, v5\n\t"
    "v_accvgpr_write_b32 a104, v5\n\t"
    "v_accvgpr_read_b32 v6, a105\n\t"
    "v_mul_f32_e32 v6, v17, v6\n\t"
    "v_accvgpr_write_b32 a105, v6\n\t"
    "v_accvgpr_read_b32 v5, a106\n\t"
    "v_mul_f32_e32 v5, v17, v5\n\t"
    "v_accvgpr_write_b32 a106, v5\n\t"
    "v_accvgpr_read_b32 v6, a107\n\t"
    "v_mul_f32_e32 v6, v17, v6\n\t"
    "v_accvgpr_write_b32 a107, v6\n\t"
    "v_accvgpr_read_b32 v5, a108\n\t"
    "v_mul_f32_e32 v5, v17, v5\n\t"
    "v_accvgpr_write_b32 a108, v5\n\t"
    "v_accvgpr_read_b32 v6, a109\n\t"
    "v_mul_f32_e32 v6, v17, v6\n\t"
    "v_accvgpr_write_b32 a109, v6\n\t"
    "v_accvgpr_read_b32 v5, a110\n\t"
    "v_mul_f32_e32 v5, v17, v5\n\t"
    "v_accvgpr_write_b32 a110, v5\n\t"
    "v_accvgpr_read_b32 v6, a111\n\t"
    "v_mul_f32_e32 v6, v17, v6\n\t"
    "v_accvgpr_write_b32 a111, v6\n\t"
    "v_accvgpr_read_b32 v5, a112\n\t"
    "v_mul_f32_e32 v5, v18, v5\n\t"
    "v_accvgpr_write_b32 a112, v5\n\t"
    "v_accvgpr_read_b32 v6, a113\n\t"
    "v_mul_f32_e32 v6, v18, v6\n\t"
    "v_accvgpr_write_b32 a113, v6\n\t"
    "v_accvgpr_read_b32 v5, a114\n\t"
    "v_mul_f32_e32 v5, v18, v5\n\t"
    "v_accvgpr_write_b32 a114, v5\n\t"
    "v_accvgpr_read_b32 v6, a115\n\t"
    "v_mul_f32_e32 v6, v18, v6\n\t"
    "v_accvgpr_write_b32 a115, v6\n\t"
    "v_accvgpr_read_b32 v5, a116\n\t"
    "v_mul_f32_e32 v5, v18, v5\n\t"
    "v_accvgpr_write_b32 a116, v5\n\t"
    "v_accvgpr_read_b32 v6, a117\n\t"
    "v_mul_f32_e32 v6, v18, v6\n\t"
    "v_accvgpr_write_b32 a117, v6\n\t"
    "v_accvgpr_read_b32 v5, a118\n\t"
    "v_mul_f32_e32 v5, v18, v5\n\t"
    "v_accvgpr_write_b32 a118, v5\n\t"
    "v_accvgpr_read_b32 v6, a119\n\t"
    "v_mul_f32_e32 v6, v18, v6\n\t"
    "v_accvgpr_write_b32 a119, v6\n\t"
    "v_accvgpr_read_b32 v5, a120\n\t"
    "v_mul_f32_e32 v5, v18, v5\n\t"
    "v_accvgpr_write_b32 a120, v5\n\t"
    "v_accvgpr_read_b32 v6, a121\n\t"
    "v_mul_f32_e32 v6, v18, v6\n\t"
    "v_accvgpr_write_b32 a121, v6\n\t"
    "v_accvgpr_read_b32 v5, a122\n\t"
    "v_mul_f32_e32 v5, v18, v5\n\t"
    "v_accvgpr_write_b32 a122, v5\n\t"
    "v_accvgpr_read_b32 v6, a123\n\t"
    "v_mul_f32_e32 v6, v18, v6\n\t"
    "v_accvgpr_write_b32 a123, v6\n\t"
    "v_accvgpr_read_b32 v5, a124\n\t"
    "v_mul_f32_e32 v5, v18, v5\n\t"
    "v_accvgpr_write_b32 a124, v5\n\t"
    "v_accvgpr_read_b32 v6, a125\n\t"
    "v_mul_f32_e32 v6, v18, v6\n\t"
    "v_accvgpr_write_b32 a125, v6\n\t"
    "v_accvgpr_read_b32 v5, a126\n\t"
    "v_mul_f32_e32 v5, v18, v5\n\t"
    "v_accvgpr_write_b32 a126, v5\n\t"
    "v_accvgpr_read_b32 v6, a127\n\t"
    "v_mul_f32_e32 v6, v18, v6\n\t"
    "v_accvgpr_write_b32 a127, v6\n\t"
    "s_mov_b32 s47, 0\n\t"
    "s_nop 1\n\t"
    "s_branch L_RRET_p1\n\t"
    "L_RARE_p2:\n\t"
    "s_nop 1\n\t"
    "v_max_f32_e32 v17, 0, v30\n\t"
    "v_add_f32_e32 v24, v24, v17\n\t"
    "v_sub_f32_e32 v32, v32, v17\n\t"
    "v_sub_f32_e32 v33, v33, v17\n\t"
    "v_sub_f32_e32 v34, v34, v17\n\t"
    "v_sub_f32_e32 v35, v35, v17\n\t"
    "v_sub_f32_e32 v36, v36, v17\n\t"
    "v_sub_f32_e32 v37, v37, v17\n\t"
    "v_sub_f32_e32 v38, v38, v17\n\t"
    "v_sub_f32_e32 v39, v39, v17\n\t"
    "v_sub_f32_e32 v40, v40, v17\n\t"
    "v_sub_f32_e32 v41, v41, v17\n\t"
    "v_sub_f32_e32 v42, v42, v17\n\t"
    "v_sub_f32_e32 v43, v43, v17\n\t"
    "v_sub_f32_e32 v44, v44, v17\n\t"
    "v_sub_f32_e32 v45, v45, v17\n\t"
    "v_sub_f32_e32 v46, v46, v17\n\t"
    "v_sub_f32_e32 v47, v47, v17\n\t"
    "v_sub_f32_e32 v64, v64, v17\n\t"
    "v_sub_f32_e32 v65, v65, v17\n\t"
    "v_sub_f32_e32 v66, v66, v17\n\t"
    "v_sub_f32_e32 v67, v67, v17\n\t"
    "v_sub_f32_e32 v68, v68, v17\n\t"
    "v_sub_f32_e32 v69, v69, v17\n\t"
    "v_sub_f32_e32 v70, v70, v17\n\t"
    "v_sub_f32_e32 v71, v71, v17\n\t"
    "v_sub_f32_e32 v72, v72, v17\n\t"
    "v_sub_f32_e32 v73, v73, v17\n\t"
    "v_sub_f32_e32 v74, v74, v17\n\t"
    "v_sub_f32_e32 v75, v75, v17\n\t"
    "v_sub_f32_e32 v76, v76, v17\n\t"
    "v_sub_f32_e32 v77, v77, v17\n\t"
    "v_sub_f32_e32 v78, v78, v17\n\t"
    "v_sub_f32_e32 v79, v79, v17\n\t"
    "v_sub_f32_e32 v160, 0, v24\n\t"
    "v_sub_f32_e32 v161, 0, v24\n\t"
    "v_sub_f32_e32 v162, 0, v24\n\t"
    "v_sub_f32_e32 v163, 0, v24\n\t"
    "v_sub_f32_e32 v164, 0, v24\n\t"
    "v_sub_f32_e32 v165, 0, v24\n\t"
    "v_sub_f32_e32 v166, 0, v24\n\t"
    "v_sub_f32_e32 v167, 0, v24\n\t"
    "v_sub_f32_e32 v168, 0, v24\n\t"
    "v_sub_f32_e32 v169, 0, v24\n\t"
    "v_sub_f32_e32 v170, 0, v24\n\t"
    "v_sub_f32_e32 v171, 0, v24\n\t"
    "v_sub_f32_e32 v172, 0, v24\n\t"
    "v_sub_f32_e32 v173, 0, v24\n\t"
    "v_sub_f32_e32 v174, 0, v24\n\t"
    "v_sub_f32_e32 v175, 0, v24\n\t"
    "v_sub_f32_e32 v17, 0, v17\n\t"
    "v_exp_f32_e32 v17, v17\n\t"
    "s_nop 0\n\t"
    "v_mul_f32_e32 v20, v17, v20\n\t"
    "v_mul_f32_e32 v22, v17, v22\n\t"
    "v_max_f32_e32 v18, 0, v31\n\t"
    "v_add_f32_e32 v25, v25, v18\n\t"
    "v_sub_f32_e32 v48, v48, v18\n\t"
    "v_sub_f32_e32 v49, v49, v18\n\t"
    "v_sub_f32_e32 v50, v50, v18\n\t"
    "v_sub_f32_e32 v51, v51, v18\n\t"
    "v_sub_f32_e32 v52, v52, v18\n\t"
    "v_sub_f32_e32 v53, v53, v18\n\t"
    "v_sub_f32_e32 v54, v54, v18\n\t"
    "v_sub_f32_e32 v55, v55, v18\n\t"
    "v_sub_f32_e32 v56, v56, v18\n\t"
    "v_sub_f32_e32 v57, v57, v18\n\t"
    "v_sub_f32_e32 v58, v58, v18\n\t"
    "v_sub_f32_e32 v59, v59, v18\n\t"
    "v_sub_f32_e32 v60, v60, v18\n\t"
    "v_sub_f32_e32 v61, v61, v18\n\t"
    "v_sub_f32_e32 v62, v62, v18\n\t"
    "v_sub_f32_e32 v63, v63, v18\n\t"
    "v_sub_f32_e32 v80, v80, v18\n\t"
    "v_sub_f32_e32 v81, v81, v18\n\t"
    "v_sub_f32_e32 v82, v82, v18\n\t"
    "v_sub_f32_e32 v83, v83, v18\n\t"
    "v_sub_f32_e32 v84, v84, v18\n\t"
    "v_sub_f32_e32 v85, v85, v18\n\t"
    "v_sub_f32_e32 v86, v86, v18\n\t"
    "v_sub_f32_e32 v87, v87, v18\n\t"
    "v_sub_f32_e32 v88, v88, v18\n\t"
    "v_sub_f32_e32 v89, v89, v18\n\t"
    "v_sub_f32_e32 v90, v90, v18\n\t"
    "v_sub_f32_e32 v91, v91, v18\n\t"
    "v_sub_f32_e32 v92, v92, v18\n\t"
    "v_sub_f32_e32 v93, v93, v18\n\t"
    "v_sub_f32_e32 v94, v94, v18\n\t"
    "v_sub_f32_e32 v95, v95, v18\n\t"
    "v_sub_f32_e32 v176, 0, v25\n\t"
    "v_sub_f32_e32 v177, 0, v25\n\t"
    "v_sub_f32_e32 v178, 0, v25\n\t"
    "v_sub_f32_e32 v179, 0, v25\n\t"
    "v_sub_f32_e32 v180, 0, v25\n\t"
    "v_sub_f32_e32 v181, 0, v25\n\t"
    "v_sub_f32_e32 v182, 0, v25\n\t"
    "v_sub_f32_e32 v183, 0, v25\n\t"
    "v_sub_f32_e32 v184, 0, v25\n\t"
    "v_sub_f32_e32 v185, 0, v25\n\t"
    "v_sub_f32_e32 v186, 0, v25\n\t"
    "v_sub_f32_e32 v187, 0, v25\n\t"
    "v_sub_f32_e32 v188, 0, v25\n\t"
    "v_sub_f32_e32 v189, 0, v25\n\t"
    "v_sub_f32_e32 v190, 0, v25\n\t"
    "v_sub_f32_e32 v191, 0, v25\n\t"
    "v_sub_f32_e32 v18, 0, v18\n\t"
    "v_exp_f32_e32 v18, v18\n\t"
    "s_nop 0\n\t"
    "v_mul_f32_e32 v21, v18, v21\n\t"
    "v_mul_f32_e32 v23, v18, v23\n\t"
    "s_mov_b32 s47, 1\n\t"
    "s_nop 1\n\t"
    "s_branch L_RET_p2\n\t"
    "L_RESC_p2:\n\t"
    "s_nop 7\n\t"
    "s_nop 7\n\t"
    "v_accvgpr_read_b32 v5, a0\n\t"
    "v_mul_f32_e32 v5, v17, v5\n\t"
    "v_accvgpr_write_b32 a0, v5\n\t"
    "v_accvgpr_read_b32 v6, a1\n\t"
    "v_mul_f32_e32 v6, v17, v6\n\t"
    "v_accvgpr_write_b32 a1, v6\n\t"
    "v_accvgpr_read_b32 v5, a2\n\t"
    "v_mul_f32_e32 v5, v17, v5\n\t"
    "v_accvgpr_write_b32 a2, v5\n\t"
    "v_accvgpr_read_b32 v6, a3\n\t"
    "v_mul_f32_e32 v6, v17, v6\n\t"
    "v_accvgpr_write_b32 a3, v6\n\t"
    "v_accvgpr_read_b32 v5, a4\n\t"
    "v_mul_f32_e32 v5, v17, v5\n\t"
    "v_accvgpr_write_b32 a4, v5\n\t"
    "v_accvgpr_read_b32 v6, a5\n\t"
    "v_mul_f32_e32 v6, v17, v6\n\t"
    "v_accvgpr_write_b32 a5, v6\n\t"
    "v_accvgpr_read_b32 v5, a6\n\t"
    "v_mul_f32_e32 v5, v17, v5\n\t"
    "v_accvgpr_write_b32 a6, v5\n\t"
    "v_accvgpr_read_b32 v6, a7\n\t"
    "v_mul_f32_e32 v6, v17, v6\n\t"
    "v_accvgpr_write_b32 a7, v6\n\t"
    "v_accvgpr_read_b32 v5, a8\n\t"
    "v_mul_f32_e32 v5, v17, v5\n\t"
    "v_accvgpr_write_b32 a8, v5\n\t"
    "v_accvgpr_read_b32 v6, a9\n\t"
    "v_mul_f32_e32 v6, v17, v6\n\t"
    "v_accvgpr_write_b32 a9, v6\n\t"
    "v_accvgpr_read_b32 v5, a10\n\t"
    "v_mul_f32_e32 v5, v17, v5\n\t"
    "v_accvgpr_write_b32 a10, v5\n\t"
    "v_accvgpr_read_b32 v6, a11\n\t"
    "v_mul_f32_e32 v6, v17, v6\n\t"
    "v_accvgpr_write_b32 a11, v6\n\t"
    "v_accvgpr_read_b32 v5, a12\n\t"
    "v_mul_f32_e32 v5, v17, v5\n\t"
    "v_accvgpr_write_b32 a12, v5\n\t"
    "v_accvgpr_read_b32 v6, a13\n\t"
    "v_mul_f32_e32 v6, v17, v6\n\t"
    "v_accvgpr_write_b32 a13, v6\n\t"
    "v_accvgpr_read_b32 v5, a14\n\t"
    "v_mul_f32_e32 v5, v17, v5\n\t"
    "v_accvgpr_write_b32 a14, v5\n\t"
    "v_accvgpr_read_b32 v6, a15\n\t"
    "v_mul_f32_e32 v6, v17, v6\n\t"
    "v_accvgpr_write_b32 a15, v6\n\t"
    "v_accvgpr_read_b32 v5, a16\n\t"
    "v_mul_f32_e32 v5, v18, v5\n\t"
    "v_accvgpr_write_b32 a16, v5\n\t"
    "v_accvgpr_read_b32 v6, a17\n\t"
    "v_mul_f32_e32 v6, v18, v6\n\t"
    "v_accvgpr_write_b32 a17, v6\n\t"
    "v_accvgpr_read_b32 v5, a18\n\t"
    "v_mul_f32_e32 v5, v18, v5\n\t"
    "v_accvgpr_write_b32 a18, v5\n\t"
    "v_accvgpr_read_b32 v6, a19\n\t"
    "v_mul_f32_e32 v6, v18, v6\n\t"
    "v_accvgpr_write_b32 a19, v6\n\t"
    "v_accvgpr_read_b32 v5, a20\n\t"
    "v_mul_f32_e32 v5, v18, v5\n\t"
    "v_accvgpr_write_b32 a20, v5\n\t"
    "v_accvgpr_read_b32 v6, a21\n\t"
    "v_mul_f32_e32 v6, v18, v6\n\t"
    "v_accvgpr_write_b32 a21, v6\n\t"
    "v_accvgpr_read_b32 v5, a22\n\t"
    "v_mul_f32_e32 v5, v18, v5\n\t"
    "v_accvgpr_write_b32 a22, v5\n\t"
    "v_accvgpr_read_b32 v6, a23\n\t"
    "v_mul_f32_e32 v6, v18, v6\n\t"
    "v_accvgpr_write_b32 a23, v6\n\t"
    "v_accvgpr_read_b32 v5, a24\n\t"
    "v_mul_f32_e32 v5, v18, v5\n\t"
    "v_accvgpr_write_b32 a24, v5\n\t"
    "v_accvgpr_read_b32 v6, a25\n\t"
    "v_mul_f32_e32 v6, v18, v6\n\t"
    "v_accvgpr_write_b32 a25, v6\n\t"
    "v_accvgpr_read_b32 v5, a26\n\t"
    "v_mul_f32_e32 v5, v18, v5\n\t"
    "v_accvgpr_write_b32 a26, v5\n\t"
    "v_accvgpr_read_b32 v6, a27\n\t"
    "v_mul_f32_e32 v6, v18, v6\n\t"
    "v_accvgpr_write_b32 a27, v6\n\t"
    "v_accvgpr_read_b32 v5, a28\n\t"
    "v_mul_f32_e32 v5, v18, v5\n\t"
    "v_accvgpr_write_b32 a28, v5\n\t"
    "v_accvgpr_read_b32 v6, a29\n\t"
    "v_mul_f32_e32 v6, v18, v6\n\t"
    "v_accvgpr_write_b32 a29, v6\n\t"
    "v_accvgpr_read_b32 v5, a30\n\t"
    "v_mul_f32_e32 v5, v18, v5\n\t"
    "v_accvgpr_write_b32 a30, v5\n\t"
    "v_accvgpr_read_b32 v6, a31\n\t"
    "v_mul_f32_e32 v6, v18, v6\n\t"
    "v_accvgpr_write_b32 a31, v6\n\t"
    "v_accvgpr_read_b32 v5, a32\n\t"
    "v_mul_f32_e32 v5, v17, v5\n\t"
    "v_accvgpr_write_b32 a32, v5\n\t"
    "v_accvgpr_read_b32 v6, a33\n\t"
    "v_mul_f32_e32 v6, v17, v6\n\t"
    "v_accvgpr_write_b32 a33, v6\n\t"
    "v_accvgpr_read_b32 v5, a34\n\t"
    "v_mul_f32_e32 v5, v17, v5\n\t"
    "v_accvgpr_write_b32 a34, v5\n\t"
    "v_accvgpr_read_b32 v6, a35\n\t"
    "v_mul_f32_e32 v6, v17, v6\n\t"
    "v_accvgpr_write_b32 a35, v6\n\t"
    "v_accvgpr_read_b32 v5, a36\n\t"
    "v_mul_f32_e32 v5, v17, v5\n\t"
    "v_accvgpr_write_b32 a36, v5\n\t"
    "v_accvgpr_read_b32 v6, a37\n\t"
    "v_mul_f32_e32 v6, v17, v6\n\t"
    "v_accvgpr_write_b32 a37, v6\n\t"
    "v_accvgpr_read_b32 v5, a38\n\t"
    "v_mul_f32_e32 v5, v17, v5\n\t"
    "v_accvgpr_write_b32 a38, v5\n\t"
    "v_accvgpr_read_b32 v6, a39\n\t"
    "v_mul_f32_e32 v6, v17, v6\n\t"
    "v_accvgpr_write_b32 a39, v6\n\t"
    "v_accvgpr_read_b32 v5, a40\n\t"
    "v_mul_f32_e32 v5, v17, v5\n\t"
    "v_accvgpr_write_b32 a40, v5\n\t"
    "v_accvgpr_read_b32 v6, a41\n\t"
    "v_mul_f32_e32 v6, v17, v6\n\t"
    "v_accvgpr_write_b32 a41, v6\n\t"
    "v_accvgpr_read_b32 v5, a42\n\t"
    "v_mul_f32_e32 v5, v17, v5\n\t"
    "v_accvgpr_write_b32 a42, v5\n\t"
    "v_accvgpr_read_b32 v6, a43\n\t"
    "v_mul_f32_e32 v6, v17, v6\n\t"
    "v_accvgpr_write_b32 a43, v6\n\t"
    "v_accvgpr_read_b32 v5, a44\n\t"
    "v_mul_f32_e32 v5, v17, v5\n\t"
    "v_accvgpr_write_b32 a44, v5\n\t"
    "v_accvgpr_read_b32 v6, a45\n\t"
    "v_mul_f32_e32 v6, v17, v6\n\t"
    "v_accvgpr_write_b32 a45, v6\n\t"
    "v_accvgpr_read_b32 v5, a46\n\t"
    "v_mul_f32_e32 v5, v17, v5\n\t"
    "v_accvgpr_write_b32 a46, v5\n\t"
    "v_accvgpr_read_b32 v6, a47\n\t"
    "v_mul_f32_e32 v6, v17, v6\n\t"
    "v_accvgpr_write_b32 a47, v6\n\t"
    "v_accvgpr_read_b32 v5, a48\n\t"
    "v_mul_f32_e32 v5, v18, v5\n\t"
    "v_accvgpr_write_b32 a48, v5\n\t"
    "v_accvgpr_read_b32 v6, a49\n\t"
    "v_mul_f32_e32 v6, v18, v6\n\t"
    "v_accvgpr_write_b32 a49, v6\n\t"
    "v_accvgpr_read_b32 v5, a50\n\t"
    "v_mul_f32_e32 v5, v18, v5\n\t"
    "v_accvgpr_write_b32 a50, v5\n\t"
    "v_accvgpr_read_b32 v6, a51\n\t"
    "v_mul_f32_e32 v6, v18, v6\n\t"
    "v_accvgpr_write_b32 a51, v6\n\t"
    "v_accvgpr_read_b32 v5, a52\n\t"
    "v_mul_f32_e32 v5, v18, v5\n\t"
    "v_accvgpr_write_b32 a52, v5\n\t"
    "v_accvgpr_read_b32 v6, a53\n\t"
    "v_mul_f32_e32 v6, v18, v6\n\t"
    "v_accvgpr_write_b32 a53, v6\n\t"
    "v_accvgpr_read_b32 v5, a54\n\t"
    "v_mul_f32_e32 v5, v18, v5\n\t"
    "v_accvgpr_write_b32 a54, v5\n\t"
    "v_accvgpr_read_b32 v6, a55\n\t"
    "v_mul_f32_e32 v6, v18, v6\n\t"
    "v_accvgpr_write_b32 a55, v6\n\t"
    "v_accvgpr_read_b32 v5, a56\n\t"
    "v_mul_f32_e32 v5, v18, v5\n\t"
    "v_accvgpr_write_b32 a56, v5\n\t"
    "v_accvgpr_read_b32 v6, a57\n\t"
    "v_mul_f32_e32 v6, v18, v6\n\t"
    "v_accvgpr_write_b32 a57, v6\n\t"
    "v_accvgpr_read_b32 v5, a58\n\t"
    "v_mul_f32_e32 v5, v18, v5\n\t"
    "v_accvgpr_write_b32 a58, v5\n\t"
    "v_accvgpr_read_b32 v6, a59\n\t"
    "v_mul_f32_e32 v6, v18, v6\n\t"
    "v_accvgpr_write_b32 a59, v6\n\t"
    "v_accvgpr_read_b32 v5, a60\n\t"
    "v_mul_f32_e32 v5, v18, v5\n\t"
    "v_accvgpr_write_b32 a60, v5\n\t"
    "v_accvgpr_read_b32 v6, a61\n\t"
    "v_mul_f32_e32 v6, v18, v6\n\t"
    "v_accvgpr_write_b32 a61, v6\n\t"
    "v_accvgpr_read_b32 v5, a62\n\t"
    "v_mul_f32_e32 v5, v18, v5\n\t"
    "v_accvgpr_write_b32 a62, v5\n\t"
    "v_accvgpr_read_b32 v6, a63\n\t"
    "v_mul_f32_e32 v6, v18, v6\n\t"
    "v_accvgpr_write_b32 a63, v6\n\t"
    "v_accvgpr_read_b32 v5, a64\n\t"
    "v_mul_f32_e32 v5, v17, v5\n\t"
    "v_accvgpr_write_b32 a64, v5\n\t"
    "v_accvgpr_read_b32 v6, a65\n\t"
    "v_mul_f32_e32 v6, v17, v6\n\t"
    "v_accvgpr_write_b32 a65, v6\n\t"
    "v_accvgpr_read_b32 v5, a66\n\t"
    "v_mul_f32_e32 v5, v17, v5\n\t"
    "v_accvgpr_write_b32 a66, v5\n\t"
    "v_accvgpr_read_b32 v6, a67\n\t"
    "v_mul_f32_e32 v6, v17, v6\n\t"
    "v_accvgpr_write_b32 a67, v6\n\t"
    "v_accvgpr_read_b32 v5, a68\n\t"
    "v_mul_f32_e32 v5, v17, v5\n\t"
    "v_accvgpr_write_b32 a68, v5\n\t"
    "v_accvgpr_read_b32 v6, a69\n\t"
    "v_mul_f32_e32 v6, v17, v6\n\t"
    "v_accvgpr_write_b32 a69, v6\n\t"
    "v_accvgpr_read_b32 v5, a70\n\t"
    "v_mul_f32_e32 v5, v17, v5\n\t"
    "v_accvgpr_write_b32 a70, v5\n\t"
    "v_accvgpr_read_b32 v6, a71\n\t"
    "v_mul_f32_e32 v6, v17, v6\n\t"
    "v_accvgpr_write_b32 a71, v6\n\t"
    "v_accvgpr_read_b32 v5, a72\n\t"
    "v_mul_f32_e32 v5, v17, v5\n\t"
    "v_accvgpr_write_b32 a72, v5\n\t"
    "v_accvgpr_read_b32 v6, a73\n\t"
    "v_mul_f32_e32 v6, v17, v6\n\t"
    "v_accvgpr_write_b32 a73, v6\n\t"
    "v_accvgpr_read_b32 v5, a74\n\t"
    "v_mul_f32_e32 v5, v17, v5\n\t"
    "v_accvgpr_write_b32 a74, v5\n\t"
    "v_accvgpr_read_b32 v6, a75\n\t"
    "v_mul_f32_e32 v6, v17, v6\n\t"
    "v_accvgpr_write_b32 a75, v6\n\t"
    "v_accvgpr_read_b32 v5, a76\n\t"
    "v_mul_f32_e32 v5, v17, v5\n\t"
    "v_accvgpr_write_b32 a76, v5\n\t"
    "v_accvgpr_read_b32 v6, a77\n\t"
    "v_mul_f32_e32 v6, v17, v6\n\t"
    "v_accvgpr_write_b32 a77, v6\n\t"
    "v_accvgpr_read_b32 v5, a78\n\t"
    "v_mul_f32_e32 v5, v17, v5\n\t"
    "v_accvgpr_write_b32 a78, v5\n\t"
    "v_accvgpr_read_b32 v6, a79\n\t"
    "v_mul_f32_e32 v6, v17, v6\n\t"
    "v_accvgpr_write_b32 a79, v6\n\t"
    "v_accvgpr_read_b32 v5, a80\n\t"
    "v_mul_f32_e32 v5, v18, v5\n\t"
    "v_accvgpr_write_b32 a80, v5\n\t"
    "v_accvgpr_read_b32 v6, a81\n\t"
    "v_mul_f32_e32 v6, v18, v6\n\t"
    "v_accvgpr_write_b32 a81, v6\n\t"
    "v_accvgpr_read_b32 v5, a82\n\t"
    "v_mul_f32_e32 v5, v18, v5\n\t"
    "v_accvgpr_write_b32 a82, v5\n\t"
    "v_accvgpr_read_b32 v6, a83\n\t"
    "v_mul_f32_e32 v6, v18, v6\n\t"
    "v_accvgpr_write_b32 a83, v6\n\t"
    "v_accvgpr_read_b32 v5, a84\n\t"
    "v_mul_f32_e32 v5, v18, v5\n\t"
    "v_accvgpr_write_b32 a84, v5\n\t"
    "v_accvgpr_read_b32 v6, a85\n\t"
    "v_mul_f32_e32 v6, v18, v6\n\t"
    "v_accvgpr_write_b32 a85, v6\n\t"
    "v_accvgpr_read_b32 v5, a86\n\t"
    "v_mul_f32_e32 v5, v18, v5\n\t"
    "v_accvgpr_write_b32 a86, v5\n\t"
    "v_accvgpr_read_b32 v6, a87\n\t"
    "v_mul_f32_e32 v6, v18, v6\n\t"
    "v_accvgpr_write_b32 a87, v6\n\t"
    "v_accvgpr_read_b32 v5, a88\n\t"
    "v_mul_f32_e32 v5, v18, v5\n\t"
    "v_accvgpr_write_b32 a88, v5\n\t"
    "v_accvgpr_read_b32 v6, a89\n\t"
    "v_mul_f32_e32 v6, v18, v6\n\t"
    "v_accvgpr_write_b32 a89, v6\n\t"
    "v_accvgpr_read_b32 v5, a90\n\t"
    "v_mul_f32_e32 v5, v18, v5\n\t"
    "v_accvgpr_write_b32 a90, v5\n\t"
    "v_accvgpr_read_b32 v6, a91\n\t"
    "v_mul_f32_e32 v6, v18, v6\n\t"
    "v_accvgpr_write_b32 a91, v6\n\t"
    "v_accvgpr_read_b32 v5, a92\n\t"
    "v_mul_f32_e32 v5, v18, v5\n\t"
    "v_accvgpr_write_b32 a92, v5\n\t"
    "v_accvgpr_read_b32 v6, a93\n\t"
    "v_mul_f32_e32 v6, v18, v6\n\t"
    "v_accvgpr_write_b32 a93, v6\n\t"
    "v_accvgpr_read_b32 v5, a94\n\t"
    "v_mul_f32_e32 v5, v18, v5\n\t"
    "v_accvgpr_write_b32 a94, v5\n\t"
    "v_accvgpr_read_b32 v6, a95\n\t"
    "v_mul_f32_e32 v6, v18, v6\n\t"
    "v_accvgpr_write_b32 a95, v6\n\t"
    "v_accvgpr_read_b32 v5, a96\n\t"
    "v_mul_f32_e32 v5, v17, v5\n\t"
    "v_accvgpr_write_b32 a96, v5\n\t"
    "v_accvgpr_read_b32 v6, a97\n\t"
    "v_mul_f32_e32 v6, v17, v6\n\t"
    "v_accvgpr_write_b32 a97, v6\n\t"
    "v_accvgpr_read_b32 v5, a98\n\t"
    "v_mul_f32_e32 v5, v17, v5\n\t"
    "v_accvgpr_write_b32 a98, v5\n\t"
    "v_accvgpr_read_b32 v6, a99\n\t"
    "v_mul_f32_e32 v6, v17, v6\n\t"
    "v_accvgpr_write_b32 a99, v6\n\t"
    "v_accvgpr_read_b32 v5, a100\n\t"
    "v_mul_f32_e32 v5, v17, v5\n\t"
    "v_accvgpr_write_b32 a100, v5\n\t"
    "v_accvgpr_read_b32 v6, a101\n\t"
    "v_mul_f32_e32 v6, v17, v6\n\t"
    "v_accvgpr_write_b32 a101, v6\n\t"
    "v_accvgpr_read_b32 v5, a102\n\t"
    "v_mul_f32_e32 v5, v17, v5\n\t"
    "v_accvgpr_write_b32 a102, v5\n\t"
    "v_accvgpr_read_b32 v6, a103\n\t"
    "v_mul_f32_e32 v6, v17, v6\n\t"
    "v_accvgpr_write_b32 a103, v6\n\t"
    "v_accvgpr_read_b32 v5, a104\n\t"
    "v_mul_f32_e32 v5, v17, v5\n\t"
    "v_accvgpr_write_b32 a104, v5\n\t"
    "v_accvgpr_read_b32 v6, a105\n\t"
    "v_mul_f32_e32 v6, v17, v6\n\t"
    "v_accvgpr_write_b32 a105, v6\n\t"
    "v_accvgpr_read_b32 v5, a106\n\t"
    "v_mul_f32_e32 v5, v17, v5\n\t"
    "v_accvgpr_write_b32 a106, v5\n\t"
    "v_accvgpr_read_b32 v6, a107\n\t"
    "v_mul_f32_e32 v6, v17, v6\n\t"
    "v_accvgpr_write_b32 a107, v6\n\t"
    "v_accvgpr_read_b32 v5, a108\n\t"
    "v_mul_f32_e32 v5, v17, v5\n\t"
    "v_accvgpr_write_b32 a108, v5\n\t"
    "v_accvgpr_read_b32 v6, a109\n\t"
    "v_mul_f32_e32 v6, v17, v6\n\t"
    "v_accvgpr_write_b32 a109, v6\n\t"
    "v_accvgpr_read_b32 v5, a110\n\t"
    "v_mul_f32_e32 v5, v17, v5\n\t"
    "v_accvgpr_write_b32 a110, v5\n\t"
    "v_accvgpr_read_b32 v6, a111\n\t"
    "v_mul_f32_e32 v6, v17, v6\n\t"
    "v_accvgpr_write_b32 a111, v6\n\t"
    "v_accvgpr_read_b32 v5, a112\n\t"
    "v_mul_f32_e32 v5, v18, v5\n\t"
    "v_accvgpr_write_b32 a112, v5\n\t"
    "v_accvgpr_read_b32 v6, a113\n\t"
    "v_mul_f32_e32 v6, v18, v6\n\t"
    "v_accvgpr_write_b32 a113, v6\n\t"
    "v_accvgpr_read_b32 v5, a114\n\t"
    "v_mul_f32_e32 v5, v18, v5\n\t"
    "v_accvgpr_write_b32 a114, v5\n\t"
    "v_accvgpr_read_b32 v6, a115\n\t"
    "v_mul_f32_e32 v6, v18, v6\n\t"
    "v_accvgpr_write_b32 a115, v6\n\t"
    "v_accvgpr_read_b32 v5, a116\n\t"
    "v_mul_f32_e32 v5, v18, v5\n\t"
    "v_accvgpr_write_b32 a116, v5\n\t"
    "v_accvgpr_read_b32 v6, a117\n\t"
    "v_mul_f32_e32 v6, v18, v6\n\t"
    "v_accvgpr_write_b32 a117, v6\n\t"
    "v_accvgpr_read_b32 v5, a118\n\t"
    "v_mul_f32_e32 v5, v18, v5\n\t"
    "v_accvgpr_write_b32 a118, v5\n\t"
    "v_accvgpr_read_b32 v6, a119\n\t"
    "v_mul_f32_e32 v6, v18, v6\n\t"
    "v_accvgpr_write_b32 a119, v6\n\t"
    "v_accvgpr_read_b32 v5, a120\n\t"
    "v_mul_f32_e32 v5, v18, v5\n\t"
    "v_accvgpr_write_b32 a120, v5\n\t"
    "v_accvgpr_read_b32 v6, a121\n\t"
    "v_mul_f32_e32 v6, v18, v6\n\t"
    "v_accvgpr_write_b32 a121, v6\n\t"
    "v_accvgpr_read_b32 v5, a122\n\t"
    "v_mul_f32_e32 v5, v18, v5\n\t"
    "v_accvgpr_write_b32 a122, v5\n\t"
    "v_accvgpr_read_b32 v6, a123\n\t"
    "v_mul_f32_e32 v6, v18, v6\n\t"
    "v_accvgpr_write_b32 a123, v6\n\t"
    "v_accvgpr_read_b32 v5, a124\n\t"
    "v_mul_f32_e32 v5, v18, v5\n\t"
    "v_accvgpr_write_b32 a124, v5\n\t"
    "v_accvgpr_read_b32 v6, a125\n\t"
    "v_mul_f32_e32 v6, v18, v6\n\t"
    "v_accvgpr_write_b32 a125, v6\n\t"
    "v_accvgpr_read_b32 v5, a126\n\t"
    "v_mul_f32_e32 v5, v18, v5\n\t"
    "v_accvgpr_write_b32 a126, v5\n\t"
    "v_accvgpr_read_b32 v6, a127\n\t"
    "v_mul_f32_e32 v6, v18, v6\n\t"
    "v_accvgpr_write_b32 a127, v6\n\t"
    "s_mov_b32 s47, 0\n\t"
    "s_nop 1\n\t"
    "s_branch L_RRET_p2\n\t"
    "L_RARE_p3:\n\t"
    "s_nop 1\n\t"
    "v_max_f32_e32 v17, 0, v30\n\t"
    "v_add_f32_e32 v24, v24, v17\n\t"
    "v_sub_f32_e32 v96, v96, v17\n\t"
    "v_sub_f32_e32 v97, v97, v17\n\t"
    "v_sub_f32_e32 v98, v98, v17\n\t"
    "v_sub_f32_e32 v99, v99, v17\n\t"
    "v_sub_f32_e32 v100, v100, v17\n\t"
    "v_sub_f32_e32 v101, v101, v17\n\t"
    "v_sub_f32_e32 v102, v102, v17\n\t"
    "v_sub_f32_e32 v103, v103, v17\n\t"
    "v_sub_f32_e32 v104, v104, v17\n\t"
    "v_sub_f32_e32 v105, v105, v17\n\t"
    "v_sub_f32_e32 v106, v106, v17\n\t"
    "v_sub_f32_e32 v107, v107, v17\n\t"
    "v_sub_f32_e32 v108, v108, v17\n\t"
    "v_sub_f32_e32 v109, v109, v17\n\t"
    "v_sub_f32_e32 v110, v110, v17\n\t"
    "v_sub_f32_e32 v111, v111, v17\n\t"
    "v_sub_f32_e32 v128, v128, v17\n\t"
    "v_sub_f32_e32 v129, v129, v17\n\t"
    "v_sub_f32_e32 v130, v130, v17\n\t"
    "v_sub_f32_e32 v131, v131, v17\n\t"
    "v_sub_f32_e32 v132, v132, v17\n\t"
    "v_sub_f32_e32 v133, v133, v17\n\t"
    "v_sub_f32_e32 v134, v134, v17\n\t"
    "v_sub_f32_e32 v135, v135, v17\n\t"
    "v_sub_f32_e32 v136, v136, v17\n\t"
    "v_sub_f32_e32 v137, v137, v17\n\t"
    "v_sub_f32_e32 v138, v138, v17\n\t"
    "v_sub_f32_e32 v139, v139, v17\n\t"
    "v_sub_f32_e32 v140, v140, v17\n\t"
    "v_sub_f32_e32 v141, v141, v17\n\t"
    "v_sub_f32_e32 v142, v142, v17\n\t"
    "v_sub_f32_e32 v143, v143, v17\n\t"
    "v_sub_f32_e32 v160, 0, v24\n\t"
    "v_sub_f32_e32 v161, 0, v24\n\t"
    "v_sub_f32_e32 v162, 0, v24\n\t"
    "v_sub_f32_e32 v163, 0, v24\n\t"
    "v_sub_f32_e32 v164, 0, v24\n\t"
    "v_sub_f32_e32 v165, 0, v24\n\t"
    "v_sub_f32_e32 v166, 0, v24\n\t"
    "v_sub_f32_e32 v167, 0, v24\n\t"
    "v_sub_f32_e32 v168, 0, v24\n\t"
    "v_sub_f32_e32 v169, 0, v24\n\t"
    "v_sub_f32_e32 v170, 0, v24\n\t"
    "v_sub_f32_e32 v171, 0, v24\n\t"
    "v_sub_f32_e32 v172, 0, v24\n\t"
    "v_sub_f32_e32 v173, 0, v24\n\t"
    "v_sub_f32_e32 v174, 0, v24\n\t"
    "v_sub_f32_e32 v175, 0, v24\n\t"
    "v_sub_f32_e32 v17, 0, v17\n\t"
    "v_exp_f32_e32 v17, v17\n\t"
    "s_nop 0\n\t"
    "v_mul_f32_e32 v20, v17, v20\n\t"
    "v_mul_f32_e32 v22, v17, v22\n\t"
    "v_max_f32_e32 v18, 0, v31\n\t"
    "v_add_f32_e32 v25, v25, v18\n\t"
    "v_sub_f32_e32 v112, v112, v18\n\t"
    "v_sub_f32_e32 v113, v113, v18\n\t"
    "v_sub_f32_e32 v114, v114, v18\n\t"
    "v_sub_f32_e32 v115, v115, v18\n\t"
    "v_sub_f32_e32 v116, v116, v18\n\t"
    "v_sub_f32_e32 v117, v117, v18\n\t"
    "v_sub_f32_e32 v118, v118, v18\n\t"
    "v_sub_f32_e32 v119, v119, v18\n\t"
    "v_sub_f32_e32 v120, v120, v18\n\t"
    "v_sub_f32_e32 v121, v121, v18\n\t"
    "v_sub_f32_e32 v122, v122, v18\n\t"
    "v_sub_f32_e32 v123, v123, v18\n\t"
    "v_sub_f32_e32 v124, v124, v18\n\t"
    "v_sub_f32_e32 v125, v125, v18\n\t"
    "v_sub_f32_e32 v126, v126, v18\n\t"
    "v_sub_f32_e32 v127, v127, v18\n\t"
    "v_sub_f32_e32 v144, v144, v18\n\t"
    "v_sub_f32_e32 v145, v145, v18\n\t"
    "v_sub_f32_e32 v146, v146, v18\n\t"
    "v_sub_f32_e32 v147, v147, v18\n\t"
    "v_sub_f32_e32 v148, v148, v18\n\t"
    "v_sub_f32_e32 v149, v149, v18\n\t"
    "v_sub_f32_e32 v150, v150, v18\n\t"
    "v_sub_f32_e32 v151, v151, v18\n\t"
    "v_sub_f32_e32 v152, v152, v18\n\t"
    "v_sub_f32_e32 v153, v153, v18\n\t"
    "v_sub_f32_e32 v154, v154, v18\n\t"
    "v_sub_f32_e32 v155, v155, v18\n\t"
    "v_sub_f32_e32 v156, v156, v18\n\t"
    "v_sub_f32_e32 v157, v157, v18\n\t"
    "v_sub_f32_e32 v158, v158, v18\n\t"
    "v_sub_f32_e32 v159, v159, v18\n\t"
    "v_sub_f32_e32 v176, 0, v25\n\t"
    "v_sub_f32_e32 v177, 0, v25\n\t"
    "v_sub_f32_e32 v178, 0, v25\n\t"
    "v_sub_f32_e32 v179, 0, v25\n\t"
    "v_sub_f32_e32 v180, 0, v25\n\t"
    "v_sub_f32_e32 v181, 0, v25\n\t"
    "v_sub_f32_e32 v182, 0, v25\n\t"
    "v_sub_f32_e32 v183, 0, v25\n\t"
    "v_sub_f32_e32 v184, 0, v25\n\t"
    "v_sub_f32_e32 v185, 0, v25\n\t"
    "v_sub_f32_e32 v186, 0, v25\n\t"
    "v_sub_f32_e32 v187, 0, v25\n\t"
    "v_sub_f32_e32 v188, 0, v25\n\t"
    "v_sub_f32_e32 v189, 0, v25\n\t"
    "v_sub_f32_e32 v190, 0, v25\n\t"
    "v_sub_f32_e32 v191, 0, v25\n\t"
    "v_sub_f32_e32 v18, 0, v18\n\t"
    "v_exp_f32_e32 v18, v18\n\t"
    "s_nop 0\n\t"
    "v_mul_f32_e32 v21, v18, v21\n\t"
    "v_mul_f32_e32 v23, v18, v23\n\t"
    "s_mov_b32 s47, 1\n\t"
    "s_nop 1\n\t"
    "s_branch L_RET_p3\n\t"
    "L_RESC_p3:\n\t"
    "s_nop 7\n\t"
    "s_nop 7\n\t"
    "v_accvgpr_read_b32 v5, a0\n\t"
    "v_mul_f32_e32 v5, v17, v5\n\t"
    "v_accvgpr_write_b32 a0, v5\n\t"
    "v_accvgpr_read_b32 v6, a1\n\t"
    "v_mul_f32_e32 v6, v17, v6\n\t"
    "v_accvgpr_write_b32 a1, v6\n\t"
    "v_accvgpr_read_b32 v5, a2\n\t"
    "v_mul_f32_e32 v5, v17, v5\n\t"
    "v_accvgpr_write_b32 a2, v5\n\t"
    "v_accvgpr_read_b32 v6, a3\n\t"
    "v_mul_f32_e32 v6, v17, v6\n\t"
    "v_accvgpr_write_b32 a3, v6\n\t"
    "v_accvgpr_read_b32 v5, a4\n\t"
    "v_mul_f32_e32 v5, v17, v5\n\t"
    "v_accvgpr_write_b32 a4, v5\n\t"
    "v_accvgpr_read_b32 v6, a5\n\t"
    "v_mul_f32_e32 v6, v17, v6\n\t"
    "v_accvgpr_write_b32 a5, v6\n\t"
    "v_accvgpr_read_b32 v5, a6\n\t"
    "v_mul_f32_e32 v5, v17, v5\n\t"
    "v_accvgpr_write_b32 a6, v5\n\t"
    "v_accvgpr_read_b32 v6, a7\n\t"
    "v_mul_f32_e32 v6, v17, v6\n\t"
    "v_accvgpr_write_b32 a7, v6\n\t"
    "v_accvgpr_read_b32 v5, a8\n\t"
    "v_mul_f32_e32 v5, v17, v5\n\t"
    "v_accvgpr_write_b32 a8, v5\n\t"
    "v_accvgpr_read_b32 v6, a9\n\t"
    "v_mul_f32_e32 v6, v17, v6\n\t"
    "v_accvgpr_write_b32 a9, v6\n\t"
    "v_accvgpr_read_b32 v5, a10\n\t"
    "v_mul_f32_e32 v5, v17, v5\n\t"
    "v_accvgpr_write_b32 a10, v5\n\t"
    "v_accvgpr_read_b32 v6, a11\n\t"
    "v_mul_f32_e32 v6, v17, v6\n\t"
    "v_accvgpr_write_b32 a11, v6\n\t"
    "v_accvgpr_read_b32 v5, a12\n\t"
    "v_mul_f32_e32 v5, v17, v5\n\t"
    "v_accvgpr_write_b32 a12, v5\n\t"
    "v_accvgpr_read_b32 v6, a13\n\t"
    "v_mul_f32_e32 v6, v17, v6\n\t"
    "v_accvgpr_write_b32 a13, v6\n\t"
    "v_accvgpr_read_b32 v5, a14\n\t"
    "v_mul_f32_e32 v5, v17, v5\n\t"
    "v_accvgpr_write_b32 a14, v5\n\t"
    "v_accvgpr_read_b32 v6, a15\n\t"
    "v_mul_f32_e32 v6, v17, v6\n\t"
    "v_accvgpr_write_b32 a15, v6\n\t"
    "v_accvgpr_read_b32 v5, a16\n\t"
    "v_mul_f32_e32 v5, v18, v5\n\t"
    "v_accvgpr_write_b32 a16, v5\n\t"
    "v_accvgpr_read_b32 v6, a17\n\t"
    "v_mul_f32_e32 v6, v18, v6\n\t"
    "v_accvgpr_write_b32 a17, v6\n\t"
    "v_accvgpr_read_b32 v5, a18\n\t"
    "v_mul_f32_e32 v5, v18, v5\n\t"
    "v_accvgpr_write_b32 a18, v5\n\t"
    "v_accvgpr_read_b32 v6, a19\n\t"
    "v_mul_f32_e32 v6, v18, v6\n\t"
    "v_accvgpr_write_b32 a19, v6\n\t"
    "v_accvgpr_read_b32 v5, a20\n\t"
    "v_mul_f32_e32 v5, v18, v5\n\t"
    "v_accvgpr_write_b32 a20, v5\n\t"
    "v_accvgpr_read_b32 v6, a21\n\t"
    "v_mul_f32_e32 v6, v18, v6\n\t"
    "v_accvgpr_write_b32 a21, v6\n\t"
    "v_accvgpr_read_b32 v5, a22\n\t"
    "v_mul_f32_e32 v5, v18, v5\n\t"
    "v_accvgpr_write_b32 a22, v5\n\t"
    "v_accvgpr_read_b32 v6, a23\n\t"
    "v_mul_f32_e32 v6, v18, v6\n\t"
    "v_accvgpr_write_b32 a23, v6\n\t"
    "v_accvgpr_read_b32 v5, a24\n\t"
    "v_mul_f32_e32 v5, v18, v5\n\t"
    "v_accvgpr_write_b32 a24, v5\n\t"
    "v_accvgpr_read_b32 v6, a25\n\t"
    "v_mul_f32_e32 v6, v18, v6\n\t"
    "v_accvgpr_write_b32 a25, v6\n\t"
    "v_accvgpr_read_b32 v5, a26\n\t"
    "v_mul_f32_e32 v5, v18, v5\n\t"
    "v_accvgpr_write_b32 a26, v5\n\t"
    "v_accvgpr_read_b32 v6, a27\n\t"
    "v_mul_f32_e32 v6, v18, v6\n\t"
    "v_accvgpr_write_b32 a27, v6\n\t"
    "v_accvgpr_read_b32 v5, a28\n\t"
    "v_mul_f32_e32 v5, v18, v5\n\t"
    "v_accvgpr_write_b32 a28, v5\n\t"
    "v_accvgpr_read_b32 v6, a29\n\t"
    "v_mul_f32_e32 v6, v18, v6\n\t"
    "v_accvgpr_write_b32 a29, v6\n\t"
    "v_accvgpr_read_b32 v5, a30\n\t"
    "v_mul_f32_e32 v5, v18, v5\n\t"
    "v_accvgpr_write_b32 a30, v5\n\t"
    "v_accvgpr_read_b32 v6, a31\n\t"
    "v_mul_f32_e32 v6, v18, v6\n\t"
    "v_accvgpr_write_b32 a31, v6\n\t"
    "v_accvgpr_read_b32 v5, a32\n\t"
    "v_mul_f32_e32 v5, v17, v5\n\t"
    "v_accvgpr_write_b32 a32, v5\n\t"
    "v_accvgpr_read_b32 v6, a33\n\t"
    "v_mul_f32_e32 v6, v17, v6\n\t"
    "v_accvgpr_write_b32 a33, v6\n\t"
    "v_accvgpr_read_b32 v5, a34\n\t"
    "v_mul_f32_e32 v5, v17, v5\n\t"
    "v_accvgpr_write_b32 a34, v5\n\t"
    "v_accvgpr_read_b32 v6, a35\n\t"
    "v_mul_f32_e32 v6, v17, v6\n\t"
    "v_accvgpr_write_b32 a35, v6\n\t"
    "v_accvgpr_read_b32 v5, a36\n\t"
    "v_mul_f32_e32 v5, v17, v5\n\t"
    "v_accvgpr_write_b32 a36, v5\n\t"
    "v_accvgpr_read_b32 v6, a37\n\t"
    "v_mul_f32_e32 v6, v17, v6\n\t"
    "v_accvgpr_write_b32 a37, v6\n\t"
    "v_accvgpr_read_b32 v5, a38\n\t"
    "v_mul_f32_e32 v5, v17, v5\n\t"
    "v_accvgpr_write_b32 a38, v5\n\t"
    "v_accvgpr_read_b32 v6, a39\n\t"
    "v_mul_f32_e32 v6, v17, v6\n\t"
    "v_accvgpr_write_b32 a39, v6\n\t"
    "v_accvgpr_read_b32 v5, a40\n\t"
    "v_mul_f32_e32 v5, v17, v5\n\t"
    "v_accvgpr_write_b32 a40, v5\n\t"
    "v_accvgpr_read_b32 v6, a41\n\t"
    "v_mul_f32_e32 v6, v17, v6\n\t"
    "v_accvgpr_write_b32 a41, v6\n\t"
    "v_accvgpr_read_b32 v5, a42\n\t"
    "v_mul_f32_e32 v5, v17, v5\n\t"
    "v_accvgpr_write_b32 a42, v5\n\t"
    "v_accvgpr_read_b32 v6, a43\n\t"
    "v_mul_f32_e32 v6, v17, v6\n\t"
    "v_accvgpr_write_b32 a43, v6\n\t"
    "v_accvgpr_read_b32 v5, a44\n\t"
    "v_mul_f32_e32 v5, v17, v5\n\t"
    "v_accvgpr_write_b32 a44, v5\n\t"
    "v_accvgpr_read_b32 v6, a45\n\t"
    "v_mul_f32_e32 v6, v17, v6\n\t"
    "v_accvgpr_write_b32 a45, v6\n\t"
    "v_accvgpr_read_b32 v5, a46\n\t"
    "v_mul_f32_e32 v5, v17, v5\n\t"
    "v_accvgpr_write_b32 a46, v5\n\t"
    "v_accvgpr_read_b32 v6, a47\n\t"
    "v_mul_f32_e32 v6, v17, v6\n\t"
    "v_accvgpr_write_b32 a47, v6\n\t"
    "v_accvgpr_read_b32 v5, a48\n\t"
    "v_mul_f32_e32 v5, v18, v5\n\t"
    "v_accvgpr_write_b32 a48, v5\n\t"
    "v_accvgpr_read_b32 v6, a49\n\t"
    "v_mul_f32_e32 v6, v18, v6\n\t"
    "v_accvgpr_write_b32 a49, v6\n\t"
    "v_accvgpr_read_b32 v5, a50\n\t"
    "v_mul_f32_e32 v5, v18, v5\n\t"
    "v_accvgpr_write_b32 a50, v5\n\t"
    "v_accvgpr_read_b32 v6, a51\n\t"
    "v_mul_f32_e32 v6, v18, v6\n\t"
    "v_accvgpr_write_b32 a51, v6\n\t"
    "v_accvgpr_read_b32 v5, a52\n\t"
    "v_mul_f32_e32 v5, v18, v5\n\t"
    "v_accvgpr_write_b32 a52, v5\n\t"
    "v_accvgpr_read_b32 v6, a53\n\t"
    "v_mul_f32_e32 v6, v18, v6\n\t"
    "v_accvgpr_write_b32 a53, v6\n\t"
    "v_accvgpr_read_b32 v5, a54\n\t"
    "v_mul_f32_e32 v5, v18, v5\n\t"
    "v_accvgpr_write_b32 a54, v5\n\t"
    "v_accvgpr_read_b32 v6, a55\n\t"
    "v_mul_f32_e32 v6, v18, v6\n\t"
    "v_accvgpr_write_b32 a55, v6\n\t"
    "v_accvgpr_read_b32 v5, a56\n\t"
    "v_mul_f32_e32 v5, v18, v5\n\t"
    "v_accvgpr_write_b32 a56, v5\n\t"
    "v_accvgpr_read_b32 v6, a57\n\t"
    "v_mul_f32_e32 v6, v18, v6\n\t"
    "v_accvgpr_write_b32 a57, v6\n\t"
    "v_accvgpr_read_b32 v5, a58\n\t"
    "v_mul_f32_e32 v5, v18, v5\n\t"
    "v_accvgpr_write_b32 a58, v5\n\t"
    "v_accvgpr_read_b32 v6, a59\n\t"
    "v_mul_f32_e32 v6, v18, v6\n\t"
    "v_accvgpr_write_b32 a59, v6\n\t"
    "v_accvgpr_read_b32 v5, a60\n\t"
    "v_mul_f32_e32 v5, v18, v5\n\t"
    "v_accvgpr_write_b32 a60, v5\n\t"
    "v_accvgpr_read_b32 v6, a61\n\t"
    "v_mul_f32_e32 v6, v18, v6\n\t"
    "v_accvgpr_write_b32 a61, v6\n\t"
    "v_accvgpr_read_b32 v5, a62\n\t"
    "v_mul_f32_e32 v5, v18, v5\n\t"
    "v_accvgpr_write_b32 a62, v5\n\t"
    "v_accvgpr_read_b32 v6, a63\n\t"
    "v_mul_f32_e32 v6, v18, v6\n\t"
    "v_accvgpr_write_b32 a63, v6\n\t"
    "v_accvgpr_read_b32 v5, a64\n\t"
    "v_mul_f32_e32 v5, v17, v5\n\t"
    "v_accvgpr_write_b32 a64, v5\n\t"
    "v_accvgpr_read_b32 v6, a65\n\t"
    "v_mul_f32_e32 v6, v17, v6\n\t"
    "v_accvgpr_write_b32 a65, v6\n\t"
    "v_accvgpr_read_b32 v5, a66\n\t"
    "v_mul_f32_e32 v5, v17, v5\n\t"
    "v_accvgpr_write_b32 a66, v5\n\t"
    "v_accvgpr_read_b32 v6, a67\n\t"
    "v_mul_f32_e32 v6, v17, v6\n\t"
    "v_accvgpr_write_b32 a67, v6\n\t"
    "v_accvgpr_read_b32 v5, a68\n\t"
    "v_mul_f32_e32 v5, v17, v5\n\t"
    "v_accvgpr_write_b32 a68, v5\n\t"
    "v_accvgpr_read_b32 v6, a69\n\t"
    "v_mul_f32_e32 v6, v17, v6\n\t"
    "v_accvgpr_write_b32 a69, v6\n\t"
    "v_accvgpr_read_b32 v5, a70\n\t"
    "v_mul_f32_e32 v5, v17, v5\n\t"
    "v_accvgpr_write_b32 a70, v5\n\t"
    "v_accvgpr_read_b32 v6, a71\n\t"
    "v_mul_f32_e32 v6, v17, v6\n\t"
    "v_accvgpr_write_b32 a71, v6\n\t"
    "v_accvgpr_read_b32 v5, a72\n\t"
    "v_mul_f32_e32 v5, v17, v5\n\t"
    "v_accvgpr_write_b32 a72, v5\n\t"
    "v_accvgpr_read_b32 v6, a73\n\t"
    "v_mul_f32_e32 v6, v17, v6\n\t"
    "v_accvgpr_write_b32 a73, v6\n\t"
    "v_accvgpr_read_b32 v5, a74\n\t"
    "v_mul_f32_e32 v5, v17, v5\n\t"
    "v_accvgpr_write_b32 a74, v5\n\t"
    "v_accvgpr_read_b32 v6, a75\n\t"
    "v_mul_f32_e32 v6, v17, v6\n\t"
    "v_accvgpr_write_b32 a75, v6\n\t"
    "v_accvgpr_read_b32 v5, a76\n\t"
    "v_mul_f32_e32 v5, v17, v5\n\t"
    "v_accvgpr_write_b32 a76, v5\n\t"
    "v_accvgpr_read_b32 v6, a77\n\t"
    "v_mul_f32_e32 v6, v17, v6\n\t"
    "v_accvgpr_write_b32 a77, v6\n\t"
    "v_accvgpr_read_b32 v5, a78\n\t"
    "v_mul_f32_e32 v5, v17, v5\n\t"
    "v_accvgpr_write_b32 a78, v5\n\t"
    "v_accvgpr_read_b32 v6, a79\n\t"
    "v_mul_f32_e32 v6, v17, v6\n\t"
    "v_accvgpr_write_b32 a79, v6\n\t"
    "v_accvgpr_read_b32 v5, a80\n\t"
    "v_mul_f32_e32 v5, v18, v5\n\t"
    "v_accvgpr_write_b32 a80, v5\n\t"
    "v_accvgpr_read_b32 v6, a81\n\t"
    "v_mul_f32_e32 v6, v18, v6\n\t"
    "v_accvgpr_write_b32 a81, v6\n\t"
    "v_accvgpr_read_b32 v5, a82\n\t"
    "v_mul_f32_e32 v5, v18, v5\n\t"
    "v_accvgpr_write_b32 a82, v5\n\t"
    "v_accvgpr_read_b32 v6, a83\n\t"
    "v_mul_f32_e32 v6, v18, v6\n\t"
    "v_accvgpr_write_b32 a83, v6\n\t"
    "v_accvgpr_read_b32 v5, a84\n\t"
    "v_mul_f32_e32 v5, v18, v5\n\t"
    "v_accvgpr_write_b32 a84, v5\n\t"
    "v_accvgpr_read_b32 v6, a85\n\t"
    "v_mul_f32_e32 v6, v18, v6\n\t"
    "v_accvgpr_write_b32 a85, v6\n\t"
    "v_accvgpr_read_b32 v5, a86\n\t"
    "v_mul_f32_e32 v5, v18, v5\n\t"
    "v_accvgpr_write_b32 a86, v5\n\t"
    "v_accvgpr_read_b32 v6, a87\n\t"
    "v_mul_f32_e32 v6, v18, v6\n\t"
    "v_accvgpr_write_b32 a87, v6\n\t"
    "v_accvgpr_read_b32 v5, a88\n\t"
    "v_mul_f32_e32 v5, v18, v5\n\t"
    "v_accvgpr_write_b32 a88, v5\n\t"
    "v_accvgpr_read_b32 v6, a89\n\t"
    "v_mul_f32_e32 v6, v18, v6\n\t"
    "v_accvgpr_write_b32 a89, v6\n\t"
    "v_accvgpr_read_b32 v5, a90\n\t"
    "v_mul_f32_e32 v5, v18, v5\n\t"
    "v_accvgpr_write_b32 a90, v5\n\t"
    "v_accvgpr_read_b32 v6, a91\n\t"
    "v_mul_f32_e32 v6, v18, v6\n\t"
    "v_accvgpr_write_b32 a91, v6\n\t"
    "v_accvgpr_read_b32 v5, a92\n\t"
    "v_mul_f32_e32 v5, v18, v5\n\t"
    "v_accvgpr_write_b32 a92, v5\n\t"
    "v_accvgpr_read_b32 v6, a93\n\t"
    "v_mul_f32_e32 v6, v18, v6\n\t"
    "v_accvgpr_write_b32 a93, v6\n\t"
    "v_accvgpr_read_b32 v5, a94\n\t"
    "v_mul_f32_e32 v5, v18, v5\n\t"
    "v_accvgpr_write_b32 a94, v5\n\t"
    "v_accvgpr_read_b32 v6, a95\n\t"
    "v_mul_f32_e32 v6, v18, v6\n\t"
    "v_accvgpr_write_b32 a95, v6\n\t"
    "v_accvgpr_read_b32 v5, a96\n\t"
    "v_mul_f32_e32 v5, v17, v5\n\t"
    "v_accvgpr_write_b32 a96, v5\n\t"
    "v_accvgpr_read_b32 v6, a97\n\t"
    "v_mul_f32_e32 v6, v17, v6\n\t"
    "v_accvgpr_write_b32 a97, v6\n\t"
    "v_accvgpr_read_b32 v5, a98\n\t"
    "v_mul_f32_e32 v5, v17, v5\n\t"
    "v_accvgpr_write_b32 a98, v5\n\t"
    "v_accvgpr_read_b32 v6, a99\n\t"
    "v_mul_f32_e32 v6, v17, v6\n\t"
    "v_accvgpr_write_b32 a99, v6\n\t"
    "v_accvgpr_read_b32 v5, a100\n\t"
    "v_mul_f32_e32 v5, v17, v5\n\t"
    "v_accvgpr_write_b32 a100, v5\n\t"
    "v_accvgpr_read_b32 v6, a101\n\t"
    "v_mul_f32_e32 v6, v17, v6\n\t"
    "v_accvgpr_write_b32 a101, v6\n\t"
    "v_accvgpr_read_b32 v5, a102\n\t"
    "v_mul_f32_e32 v5, v17, v5\n\t"
    "v_accvgpr_write_b32 a102, v5\n\t"
    "v_accvgpr_read_b32 v6, a103\n\t"
    "v_mul_f32_e32 v6, v17, v6\n\t"
    "v_accvgpr_write_b32 a103, v6\n\t"
    "v_accvgpr_read_b32 v5, a104\n\t"
    "v_mul_f32_e32 v5, v17, v5\n\t"
    "v_accvgpr_write_b32 a104, v5\n\t"
    "v_accvgpr_read_b32 v6, a105\n\t"
    "v_mul_f32_e32 v6, v17, v6\n\t"
    "v_accvgpr_write_b32 a105, v6\n\t"
    "v_accvgpr_read_b32 v5, a106\n\t"
    "v_mul_f32_e32 v5, v17, v5\n\t"
    "v_accvgpr_write_b32 a106, v5\n\t"
    "v_accvgpr_read_b32 v6, a107\n\t"
    "v_mul_f32_e32 v6, v17, v6\n\t"
    "v_accvgpr_write_b32 a107, v6\n\t"
    "v_accvgpr_read_b32 v5, a108\n\t"
    "v_mul_f32_e32 v5, v17, v5\n\t"
    "v_accvgpr_write_b32 a108, v5\n\t"
    "v_accvgpr_read_b32 v6, a109\n\t"
    "v_mul_f32_e32 v6, v17, v6\n\t"
    "v_accvgpr_write_b32 a109, v6\n\t"
    "v_accvgpr_read_b32 v5, a110\n\t"
    "v_mul_f32_e32 v5, v17, v5\n\t"
    "v_accvgpr_write_b32 a110, v5\n\t"
    "v_accvgpr_read_b32 v6, a111\n\t"
    "v_mul_f32_e32 v6, v17, v6\n\t"
    "v_accvgpr_write_b32 a111, v6\n\t"
    "v_accvgpr_read_b32 v5, a112\n\t"
    "v_mul_f32_e32 v5, v18, v5\n\t"
    "v_accvgpr_write_b32 a112, v5\n\t"
    "v_accvgpr_read_b32 v6, a113\n\t"
    "v_mul_f32_e32 v6, v18, v6\n\t"
    "v_accvgpr_write_b32 a113, v6\n\t"
    "v_accvgpr_read_b32 v5, a114\n\t"
    "v_mul_f32_e32 v5, v18, v5\n\t"
    "v_accvgpr_write_b32 a114, v5\n\t"
    "v_accvgpr_read_b32 v6, a115\n\t"
    "v_mul_f32_e32 v6, v18, v6\n\t"
    "v_accvgpr_write_b32 a115, v6\n\t"
    "v_accvgpr_read_b32 v5, a116\n\t"
    "v_mul_f32_e32 v5, v18, v5\n\t"
    "v_accvgpr_write_b32 a116, v5\n\t"
    "v_accvgpr_read_b32 v6, a117\n\t"
    "v_mul_f32_e32 v6, v18, v6\n\t"
    "v_accvgpr_write_b32 a117, v6\n\t"
    "v_accvgpr_read_b32 v5, a118\n\t"
    "v_mul_f32_e32 v5, v18, v5\n\t"
    "v_accvgpr_write_b32 a118, v5\n\t"
    "v_accvgpr_read_b32 v6, a119\n\t"
    "v_mul_f32_e32 v6, v18, v6\n\t"
    "v_accvgpr_write_b32 a119, v6\n\t"
    "v_accvgpr_read_b32 v5, a120\n\t"
    "v_mul_f32_e32 v5, v18, v5\n\t"
    "v_accvgpr_write_b32 a120, v5\n\t"
    "v_accvgpr_read_b32 v6, a121\n\t"
    "v_mul_f32_e32 v6, v18, v6\n\t"
    "v_accvgpr_write_b32 a121, v6\n\t"
    "v_accvgpr_read_b32 v5, a122\n\t"
    "v_mul_f32_e32 v5, v18, v5\n\t"
    "v_accvgpr_write_b32 a122, v5\n\t"
    "v_accvgpr_read_b32 v6, a123\n\t"
    "v_mul_f32_e32 v6, v18, v6\n\t"
    "v_accvgpr_write_b32 a123, v6\n\t"
    "v_accvgpr_read_b32 v5, a124\n\t"
    "v_mul_f32_e32 v5, v18, v5\n\t"
    "v_accvgpr_write_b32 a124, v5\n\t"
    "v_accvgpr_read_b32 v6, a125\n\t"
    "v_mul_f32_e32 v6, v18, v6\n\t"
    "v_accvgpr_write_b32 a125, v6\n\t"
    "v_accvgpr_read_b32 v5, a126\n\t"
    "v_mul_f32_e32 v5, v18, v5\n\t"
    "v_accvgpr_write_b32 a126, v5\n\t"
    "v_accvgpr_read_b32 v6, a127\n\t"
    "v_mul_f32_e32 v6, v18, v6\n\t"
    "v_accvgpr_write_b32 a127, v6\n\t"
    "s_mov_b32 s47, 0\n\t"
    "s_nop 1\n\t"
    "s_branch L_RRET_p3\n\t"
    "L_RARE_l4:\n\t"
    "s_nop 1\n\t"
    "v_max_f32_e32 v17, 0, v30\n\t"
    "v_add_f32_e32 v24, v24, v17\n\t"
    "v_sub_f32_e32 v32, v32, v17\n\t"
    "v_sub_f32_e32 v33, v33, v17\n\t"
    "v_sub_f32_e32 v34, v34, v17\n\t"
    "v_sub_f32_e32 v35, v35, v17\n\t"
    "v_sub_f32_e32 v36, v36, v17\n\t"
    "v_sub_f32_e32 v37, v37, v17\n\t"
    "v_sub_f32_e32 v38, v38, v17\n\t"
    "v_sub_f32_e32 v39, v39, v17\n\t"
    "v_sub_f32_e32 v40, v40, v17\n\t"
    "v_sub_f32_e32 v41, v41, v17\n\t"
    "v_sub_f32_e32 v42, v42, v17\n\t"
    "v_sub_f32_e32 v43, v43, v17\n\t"
    "v_sub_f32_e32 v44, v44, v17\n\t"
    "v_sub_f32_e32 v45, v45, v17\n\t"
    "v_sub_f32_e32 v46, v46, v17\n\t"
    "v_sub_f32_e32 v47, v47, v17\n\t"
    "v_sub_f32_e32 v64, v64, v17\n\t"
    "v_sub_f32_e32 v65, v65, v17\n\t"
    "v_sub_f32_e32 v66, v66, v17\n\t"
    "v_sub_f32_e32 v67, v67, v17\n\t"
    "v_sub_f32_e32 v68, v68, v17\n\t"
    "v_sub_f32_e32 v69, v69, v17\n\t"
    "v_sub_f32_e32 v70, v70, v17\n\t"
    "v_sub_f32_e32 v71, v71, v17\n\t"
    "v_sub_f32_e32 v72, v72, v17\n\t"
    "v_sub_f32_e32 v73, v73, v17\n\t"
    "v_sub_f32_e32 v74, v74, v17\n\t"
    "v_sub_f32_e32 v75, v75, v17\n\t"
    "v_sub_f32_e32 v76, v76, v17\n\t"
    "v_sub_f32_e32 v77, v77, v17\n\t"
    "v_sub_f32_e32 v78, v78, v17\n\t"
    "v_sub_f32_e32 v79, v79, v17\n\t"
    "v_sub_f32_e32 v160, 0, v24\n\t"
    "v_sub_f32_e32 v161, 0, v24\n\t"
    "v_sub_f32_e32 v162, 0, v24\n\t"
    "v_sub_f32_e32 v163, 0, v24\n\t"
    "v_sub_f32_e32 v164, 0, v24\n\t"
    "v_sub_f32_e32 v165, 0, v24\n\t"
    "v_sub_f32_e32 v166, 0, v24\n\t"
    "v_sub_f32_e32 v167, 0, v24\n\t"
    "v_sub_f32_e32 v168, 0, v24\n\t"
    "v_sub_f32_e32 v169, 0, v24\n\t"
    "v_sub_f32_e32 v170, 0, v24\n\t"
    "v_sub_f32_e32 v171, 0, v24\n\t"
    "v_sub_f32_e32 v172, 0, v24\n\t"
    "v_sub_f32_e32 v173, 0, v24\n\t"
    "v_sub_f32_e32 v174, 0, v24\n\t"
    "v_sub_f32_e32 v175, 0, v24\n\t"
    "v_sub_f32_e32 v17, 0, v17\n\t"
    "v_exp_f32_e32 v17, v17\n\t"
    "s_nop 0\n\t"
    "v_mul_f32_e32 v20, v17, v20\n\t"
    "v_mul_f32_e32 v22, v17, v22\n\t"
    "v_max_f32_e32 v18, 0, v31\n\t"
    "v_add_f32_e32 v25, v25, v18\n\t"
    "v_sub_f32_e32 v48, v48, v18\n\t"
    "v_sub_f32_e32 v49, v49, v18\n\t"
    "v_sub_f32_e32 v50, v50, v18\n\t"
    "v_sub_f32_e32 v51, v51, v18\n\t"
    "v_sub_f32_e32 v52, v52, v18\n\t"
    "v_sub_f32_e32 v53, v53, v18\n\t"
    "v_sub_f32_e32 v54, v54, v18\n\t"
    "v_sub_f32_e32 v55, v55, v18\n\t"
    "v_sub_f32_e32 v56, v56, v18\n\t"
    "v_sub_f32_e32 v57, v57, v18\n\t"
    "v_sub_f32_e32 v58, v58, v18\n\t"
    "v_sub_f32_e32 v59, v59, v18\n\t"
    "v_sub_f32_e32 v60, v60, v18\n\t"
    "v_sub_f32_e32 v61, v61, v18\n\t"
    "v_sub_f32_e32 v62, v62, v18\n\t"
    "v_sub_f32_e32 v63, v63, v18\n\t"
    "v_sub_f32_e32 v80, v80, v18\n\t"
    "v_sub_f32_e32 v81, v81, v18\n\t"
    "v_sub_f32_e32 v82, v82, v18\n\t"
    "v_sub_f32_e32 v83, v83, v18\n\t"
    "v_sub_f32_e32 v84, v84, v18\n\t"
    "v_sub_f32_e32 v85, v85, v18\n\t"
    "v_sub_f32_e32 v86, v86, v18\n\t"
    "v_sub_f32_e32 v87, v87, v18\n\t"
    "v_sub_f32_e32 v88, v88, v18\n\t"
    "v_sub_f32_e32 v89, v89, v18\n\t"
    "v_sub_f32_e32 v90, v90, v18\n\t"
    "v_sub_f32_e32 v91, v91, v18\n\t"
    "v_sub_f32_e32 v92, v92, v18\n\t"
    "v_sub_f32_e32 v93, v93, v18\n\t"
    "v_sub_f32_e32 v94, v94, v18\n\t"
    "v_sub_f32_e32 v95, v95, v18\n\t"
    "v_sub_f32_e32 v176, 0, v25\n\t"
    "v_sub_f32_e32 v177, 0, v25\n\t"
    "v_sub_f32_e32 v178, 0, v25\n\t"
    "v_sub_f32_e32 v179, 0, v25\n\t"
    "v_sub_f32_e32 v180, 0, v25\n\t"
    "v_sub_f32_e32 v181, 0, v25\n\t"
    "v_sub_f32_e32 v182, 0, v25\n\t"
    "v_sub_f32_e32 v183, 0, v25\n\t"
    "v_sub_f32_e32 v184, 0, v25\n\t"
    "v_sub_f32_e32 v185, 0, v25\n\t"
    "v_sub_f32_e32 v186, 0, v25\n\t"
    "v_sub_f32_e32 v187, 0, v25\n\t"
    "v_sub_f32_e32 v188, 0, v25\n\t"
    "v_sub_f32_e32 v189, 0, v25\n\t"
    "v_sub_f32_e32 v190, 0, v25\n\t"
    "v_sub_f32_e32 v191, 0, v25\n\t"
    "v_sub_f32_e32 v18, 0, v18\n\t"
    "v_exp_f32_e32 v18, v18\n\t"
    "s_nop 0\n\t"
    "v_mul_f32_e32 v21, v18, v21\n\t"
    "v_mul_f32_e32 v23, v18, v23\n\t"
    "s_mov_b32 s47, 1\n\t"
    "s_nop 1\n\t"
    "s_branch L_RET_l4\n\t"
    "L_RESC_l4:\n\t"
    "s_nop 7\n\t"
    "s_nop 7\n\t"
    "v_accvgpr_read_b32 v5, a0\n\t"
    "v_mul_f32_e32 v5, v17, v5\n\t"
    "v_accvgpr_write_b32 a0, v5\n\t"
    "v_accvgpr_read_b32 v6, a1\n\t"
    "v_mul_f32_e32 v6, v17, v6\n\t"
    "v_accvgpr_write_b32 a1, v6\n\t"
    "v_accvgpr_read_b32 v5, a2\n\t"
    "v_mul_f32_e32 v5, v17, v5\n\t"
    "v_accvgpr_write_b32 a2, v5\n\t"
    "v_accvgpr_read_b32 v6, a3\n\t"
    "v_mul_f32_e32 v6, v17, v6\n\t"
    "v_accvgpr_write_b32 a3, v6\n\t"
    "v_accvgpr_read_b32 v5, a4\n\t"
    "v_mul_f32_e32 v5, v17, v5\n\t"
    "v_accvgpr_write_b32 a4, v5\n\t"
    "v_accvgpr_read_b32 v6, a5\n\t"
    "v_mul_f32_e32 v6, v17, v6\n\t"
    "v_accvgpr_write_b32 a5, v6\n\t"
    "v_accvgpr_read_b32 v5, a6\n\t"
    "v_mul_f32_e32 v5, v17, v5\n\t"
    "v_accvgpr_write_b32 a6, v5\n\t"
    "v_accvgpr_read_b32 v6, a7\n\t"
    "v_mul_f32_e32 v6, v17, v6\n\t"
    "v_accvgpr_write_b32 a7, v6\n\t"
    "v_accvgpr_read_b32 v5, a8\n\t"
    "v_mul_f32_e32 v5, v17, v5\n\t"
    "v_accvgpr_write_b32 a8, v5\n\t"
    "v_accvgpr_read_b32 v6, a9\n\t"
    "v_mul_f32_e32 v6, v17, v6\n\t"
    "v_accvgpr_write_b32 a9, v6\n\t"
    "v_accvgpr_read_b32 v5, a10\n\t"
    "v_mul_f32_e32 v5, v17, v5\n\t"
    "v_accvgpr_write_b32 a10, v5\n\t"
    "v_accvgpr_read_b32 v6, a11\n\t"
    "v_mul_f32_e32 v6, v17, v6\n\t"
    "v_accvgpr_write_b32 a11, v6\n\t"
    "v_accvgpr_read_b32 v5, a12\n\t"
    "v_mul_f32_e32 v5, v17, v5\n\t"
    "v_accvgpr_write_b32 a12, v5\n\t"
    "v_accvgpr_read_b32 v6, a13\n\t"
    "v_mul_f32_e32 v6, v17, v6\n\t"
    "v_accvgpr_write_b32 a13, v6\n\t"
    "v_accvgpr_read_b32 v5, a14\n\t"
    "v_mul_f32_e32 v5, v17, v5\n\t"
    "v_accvgpr_write_b32 a14, v5\n\t"
    "v_accvgpr_read_b32 v6, a15\n\t"
    "v_mul_f32_e32 v6, v17, v6\n\t"
    "v_accvgpr_write_b32 a15, v6\n\t"
    "v_accvgpr_read_b32 v5, a16\n\t"
    "v_mul_f32_e32 v5, v18, v5\n\t"
    "v_accvgpr_write_b32 a16, v5\n\t"
    "v_accvgpr_read_b32 v6, a17\n\t"
    "v_mul_f32_e32 v6, v18, v6\n\t"
    "v_accvgpr_write_b32 a17, v6\n\t"
    "v_accvgpr_read_b32 v5, a18\n\t"
    "v_mul_f32_e32 v5, v18, v5\n\t"
    "v_accvgpr_write_b32 a18, v5\n\t"
    "v_accvgpr_read_b32 v6, a19\n\t"
    "v_mul_f32_e32 v6, v18, v6\n\t"
    "v_accvgpr_write_b32 a19, v6\n\t"
    "v_accvgpr_read_b32 v5, a20\n\t"
    "v_mul_f32_e32 v5, v18, v5\n\t"
    "v_accvgpr_write_b32 a20, v5\n\t"
    "v_accvgpr_read_b32 v6, a21\n\t"
    "v_mul_f32_e32 v6, v18, v6\n\t"
    "v_accvgpr_write_b32 a21, v6\n\t"
    "v_accvgpr_read_b32 v5, a22\n\t"
    "v_mul_f32_e32 v5, v18, v5\n\t"
    "v_accvgpr_write_b32 a22, v5\n\t"
    "v_accvgpr_read_b32 v6, a23\n\t"
    "v_mul_f32_e32 v6, v18, v6\n\t"
    "v_accvgpr_write_b32 a23, v6\n\t"
    "v_accvgpr_read_b32 v5, a24\n\t"
    "v_mul_f32_e32 v5, v18, v5\n\t"
    "v_accvgpr_write_b32 a24, v5\n\t"
    "v_accvgpr_read_b32 v6, a25\n\t"
    "v_mul_f32_e32 v6, v18, v6\n\t"
    "v_accvgpr_write_b32 a25, v6\n\t"
    "v_accvgpr_read_b32 v5, a26\n\t"
    "v_mul_f32_e32 v5, v18, v5\n\t"
    "v_accvgpr_write_b32 a26, v5\n\t"
    "v_accvgpr_read_b32 v6, a27\n\t"
    "v_mul_f32_e32 v6, v18, v6\n\t"
    "v_accvgpr_write_b32 a27, v6\n\t"
    "v_accvgpr_read_b32 v5, a28\n\t"
    "v_mul_f32_e32 v5, v18, v5\n\t"
    "v_accvgpr_write_b32 a28, v5\n\t"
    "v_accvgpr_read_b32 v6, a29\n\t"
    "v_mul_f32_e32 v6, v18, v6\n\t"
    "v_accvgpr_write_b32 a29, v6\n\t"
    "v_accvgpr_read_b32 v5, a30\n\t"
    "v_mul_f32_e32 v5, v18, v5\n\t"
    "v_accvgpr_write_b32 a30, v5\n\t"
    "v_accvgpr_read_b32 v6, a31\n\t"
    "v_mul_f32_e32 v6, v18, v6\n\t"
    "v_accvgpr_write_b32 a31, v6\n\t"
    "v_accvgpr_read_b32 v5, a32\n\t"
    "v_mul_f32_e32 v5, v17, v5\n\t"
    "v_accvgpr_write_b32 a32, v5\n\t"
    "v_accvgpr_read_b32 v6, a33\n\t"
    "v_mul_f32_e32 v6, v17, v6\n\t"
    "v_accvgpr_write_b32 a33, v6\n\t"
    "v_accvgpr_read_b32 v5, a34\n\t"
    "v_mul_f32_e32 v5, v17, v5\n\t"
    "v_accvgpr_write_b32 a34, v5\n\t"
    "v_accvgpr_read_b32 v6, a35\n\t"
    "v_mul_f32_e32 v6, v17, v6\n\t"
    "v_accvgpr_write_b32 a35, v6\n\t"
    "v_accvgpr_read_b32 v5, a36\n\t"
    "v_mul_f32_e32 v5, v17, v5\n\t"
    "v_accvgpr_write_b32 a36, v5\n\t"
    "v_accvgpr_read_b32 v6, a37\n\t"
    "v_mul_f32_e32 v6, v17, v6\n\t"
    "v_accvgpr_write_b32 a37, v6\n\t"
    "v_accvgpr_read_b32 v5, a38\n\t"
    "v_mul_f32_e32 v5, v17, v5\n\t"
    "v_accvgpr_write_b32 a38, v5\n\t"
    "v_accvgpr_read_b32 v6, a39\n\t"
    "v_mul_f32_e32 v6, v17, v6\n\t"
    "v_accvgpr_write_b32 a39, v6\n\t"
    "v_accvgpr_read_b32 v5, a40\n\t"
    "v_mul_f32_e32 v5, v17, v5\n\t"
    "v_accvgpr_write_b32 a40, v5\n\t"
    "v_accvgpr_read_b32 v6, a41\n\t"
    "v_mul_f32_e32 v6, v17, v6\n\t"
    "v_accvgpr_write_b32 a41, v6\n\t"
    "v_accvgpr_read_b32 v5, a42\n\t"
    "v_mul_f32_e32 v5, v17, v5\n\t"
    "v_accvgpr_write_b32 a42, v5\n\t"
    "v_accvgpr_read_b32 v6, a43\n\t"
    "v_mul_f32_e32 v6, v17, v6\n\t"
    "v_accvgpr_write_b32 a43, v6\n\t"
    "v_accvgpr_read_b32 v5, a44\n\t"
    "v_mul_f32_e32 v5, v17, v5\n\t"
    "v_accvgpr_write_b32 a44, v5\n\t"
    "v_accvgpr_read_b32 v6, a45\n\t"
    "v_mul_f32_e32 v6, v17, v6\n\t"
    "v_accvgpr_write_b32 a45, v6\n\t"
    "v_accvgpr_read_b32 v5, a46\n\t"
    "v_mul_f32_e32 v5, v17, v5\n\t"
    "v_accvgpr_write_b32 a46, v5\n\t"
    "v_accvgpr_read_b32 v6, a47\n\t"
    "v_mul_f32_e32 v6, v17, v6\n\t"
    "v_accvgpr_write_b32 a47, v6\n\t"
    "v_accvgpr_read_b32 v5, a48\n\t"
    "v_mul_f32_e32 v5, v18, v5\n\t"
    "v_accvgpr_write_b32 a48, v5\n\t"
    "v_accvgpr_read_b32 v6, a49\n\t"
    "v_mul_f32_e32 v6, v18, v6\n\t"
    "v_accvgpr_write_b32 a49, v6\n\t"
    "v_accvgpr_read_b32 v5, a50\n\t"
    "v_mul_f32_e32 v5, v18, v5\n\t"
    "v_accvgpr_write_b32 a50, v5\n\t"
    "v_accvgpr_read_b32 v6, a51\n\t"
    "v_mul_f32_e32 v6, v18, v6\n\t"
    "v_accvgpr_write_b32 a51, v6\n\t"
    "v_accvgpr_read_b32 v5, a52\n\t"
    "v_mul_f32_e32 v5, v18, v5\n\t"
    "v_accvgpr_write_b32 a52, v5\n\t"
    "v_accvgpr_read_b32 v6, a53\n\t"
    "v_mul_f32_e32 v6, v18, v6\n\t"
    "v_accvgpr_write_b32 a53, v6\n\t"
    "v_accvgpr_read_b32 v5, a54\n\t"
    "v_mul_f32_e32 v5, v18, v5\n\t"
    "v_accvgpr_write_b32 a54, v5\n\t"
    "v_accvgpr_read_b32 v6, a55\n\t"
    "v_mul_f32_e32 v6, v18, v6\n\t"
    "v_accvgpr_write_b32 a55, v6\n\t"
    "v_accvgpr_read_b32 v5, a56\n\t"
    "v_mul_f32_e32 v5, v18, v5\n\t"
    "v_accvgpr_write_b32 a56, v5\n\t"
    "v_accvgpr_read_b32 v6, a57\n\t"
    "v_mul_f32_e32 v6, v18, v6\n\t"
    "v_accvgpr_write_b32 a57, v6\n\t"
    "v_accvgpr_read_b32 v5, a58\n\t"
    "v_mul_f32_e32 v5, v18, v5\n\t"
    "v_accvgpr_write_b32 a58, v5\n\t"
    "v_accvgpr_read_b32 v6, a59\n\t"
    "v_mul_f32_e32 v6, v18, v6\n\t"
    "v_accvgpr_write_b32 a59, v6\n\t"
    "v_accvgpr_read_b32 v5, a60\n\t"
    "v_mul_f32_e32 v5, v18, v5\n\t"
    "v_accvgpr_write_b32 a60, v5\n\t"
    "v_accvgpr_read_b32 v6, a61\n\t"
    "v_mul_f32_e32 v6, v18, v6\n\t"
    "v_accvgpr_write_b32 a61, v6\n\t"
    "v_accvgpr_read_b32 v5, a62\n\t"
    "v_mul_f32_e32 v5, v18, v5\n\t"
    "v_accvgpr_write_b32 a62, v5\n\t"
    "v_accvgpr_read_b32 v6, a63\n\t"
    "v_mul_f32_e32 v6, v18, v6\n\t"
    "v_accvgpr_write_b32 a63, v6\n\t"
    "v_accvgpr_read_b32 v5, a64\n\t"
    "v_mul_f32_e32 v5, v17, v5\n\t"
    "v_accvgpr_write_b32 a64, v5\n\t"
    "v_accvgpr_read_b32 v6, a65\n\t"
    "v_mul_f32_e32 v6, v17, v6\n\t"
    "v_accvgpr_write_b32 a65, v6\n\t"
    "v_accvgpr_read_b32 v5, a66\n\t"
    "v_mul_f32_e32 v5, v17, v5\n\t"
    "v_accvgpr_write_b32 a66, v5\n\t"
    "v_accvgpr_read_b32 v6, a67\n\t"
    "v_mul_f32_e32 v6, v17, v6\n\t"
    "v_accvgpr_write_b32 a67, v6\n\t"
    "v_accvgpr_read_b32 v5, a68\n\t"
    "v_mul_f32_e32 v5, v17, v5\n\t"
    "v_accvgpr_write_b32 a68, v5\n\t"
    "v_accvgpr_read_b32 v6, a69\n\t"
    "v_mul_f32_e32 v6, v17, v6\n\t"
    "v_accvgpr_write_b32 a69, v6\n\t"
    "v_accvgpr_read_b32 v5, a70\n\t"
    "v_mul_f32_e32 v5, v17, v5\n\t"
    "v_accvgpr_write_b32 a70, v5\n\t"
    "v_accvgpr_read_b32 v6, a71\n\t"
    "v_mul_f32_e32 v6, v17, v6\n\t"
    "v_accvgpr_write_b32 a71, v6\n\t"
    "v_accvgpr_read_b32 v5, a72\n\t"
    "v_mul_f32_e32 v5, v17, v5\n\t"
    "v_accvgpr_write_b32 a72, v5\n\t"
    "v_accvgpr_read_b32 v6, a73\n\t"
    "v_mul_f32_e32 v6, v17, v6\n\t"
    "v_accvgpr_write_b32 a73, v6\n\t"
    "v_accvgpr_read_b32 v5, a74\n\t"
    "v_mul_f32_e32 v5, v17, v5\n\t"
    "v_accvgpr_write_b32 a74, v5\n\t"
    "v_accvgpr_read_b32 v6, a75\n\t"
    "v_mul_f32_e32 v6, v17, v6\n\t"
    "v_accvgpr_write_b32 a75, v6\n\t"
    "v_accvgpr_read_b32 v5, a76\n\t"
    "v_mul_f32_e32 v5, v17, v5\n\t"
    "v_accvgpr_write_b32 a76, v5\n\t"
    "v_accvgpr_read_b32 v6, a77\n\t"
    "v_mul_f32_e32 v6, v17, v6\n\t"
    "v_accvgpr_write_b32 a77, v6\n\t"
    "v_accvgpr_read_b32 v5, a78\n\t"
    "v_mul_f32_e32 v5, v17, v5\n\t"
    "v_accvgpr_write_b32 a78, v5\n\t"
    "v_accvgpr_read_b32 v6, a79\n\t"
    "v_mul_f32_e32 v6, v17, v6\n\t"
    "v_accvgpr_write_b32 a79, v6\n\t"
    "v_accvgpr_read_b32 v5, a80\n\t"
    "v_mul_f32_e32 v5, v18, v5\n\t"
    "v_accvgpr_write_b32 a80, v5\n\t"
    "v_accvgpr_read_b32 v6, a81\n\t"
    "v_mul_f32_e32 v6, v18, v6\n\t"
    "v_accvgpr_write_b32 a81, v6\n\t"
    "v_accvgpr_read_b32 v5, a82\n\t"
    "v_mul_f32_e32 v5, v18, v5\n\t"
    "v_accvgpr_write_b32 a82, v5\n\t"
    "v_accvgpr_read_b32 v6, a83\n\t"
    "v_mul_f32_e32 v6, v18, v6\n\t"
    "v_accvgpr_write_b32 a83, v6\n\t"
    "v_accvgpr_read_b32 v5, a84\n\t"
    "v_mul_f32_e32 v5, v18, v5\n\t"
    "v_accvgpr_write_b32 a84, v5\n\t"
    "v_accvgpr_read_b32 v6, a85\n\t"
    "v_mul_f32_e32 v6, v18, v6\n\t"
    "v_accvgpr_write_b32 a85, v6\n\t"
    "v_accvgpr_read_b32 v5, a86\n\t"
    "v_mul_f32_e32 v5, v18, v5\n\t"
    "v_accvgpr_write_b32 a86, v5\n\t"
    "v_accvgpr_read_b32 v6, a87\n\t"
    "v_mul_f32_e32 v6, v18, v6\n\t"
    "v_accvgpr_write_b32 a87, v6\n\t"
    "v_accvgpr_read_b32 v5, a88\n\t"
    "v_mul_f32_e32 v5, v18, v5\n\t"
    "v_accvgpr_write_b32 a88, v5\n\t"
    "v_accvgpr_read_b32 v6, a89\n\t"
    "v_mul_f32_e32 v6, v18, v6\n\t"
    "v_accvgpr_write_b32 a89, v6\n\t"
    "v_accvgpr_read_b32 v5, a90\n\t"
    "v_mul_f32_e32 v5, v18, v5\n\t"
    "v_accvgpr_write_b32 a90, v5\n\t"
    "v_accvgpr_read_b32 v6, a91\n\t"
    "v_mul_f32_e32 v6, v18, v6\n\t"
    "v_accvgpr_write_b32 a91, v6\n\t"
    "v_accvgpr_read_b32 v5, a92\n\t"
    "v_mul_f32_e32 v5, v18, v5\n\t"
    "v_accvgpr_write_b32 a92, v5\n\t"
    "v_accvgpr_read_b32 v6, a93\n\t"
    "v_mul_f32_e32 v6, v18, v6\n\t"
    "v_accvgpr_write_b32 a93, v6\n\t"
    "v_accvgpr_read_b32 v5, a94\n\t"
    "v_mul_f32_e32 v5, v18, v5\n\t"
    "v_accvgpr_write_b32 a94, v5\n\t"
    "v_accvgpr_read_b32 v6, a95\n\t"
    "v_mul_f32_e32 v6, v18, v6\n\t"
    "v_accvgpr_write_b32 a95, v6\n\t"
    "v_accvgpr_read_b32 v5, a96\n\t"
    "v_mul_f32_e32 v5, v17, v5\n\t"
    "v_accvgpr_write_b32 a96, v5\n\t"
    "v_accvgpr_read_b32 v6, a97\n\t"
    "v_mul_f32_e32 v6, v17, v6\n\t"
    "v_accvgpr_write_b32 a97, v6\n\t"
    "v_accvgpr_read_b32 v5, a98\n\t"
    "v_mul_f32_e32 v5, v17, v5\n\t"
    "v_accvgpr_write_b32 a98, v5\n\t"
    "v_accvgpr_read_b32 v6, a99\n\t"
    "v_mul_f32_e32 v6, v17, v6\n\t"
    "v_accvgpr_write_b32 a99, v6\n\t"
    "v_accvgpr_read_b32 v5, a100\n\t"
    "v_mul_f32_e32 v5, v17, v5\n\t"
    "v_accvgpr_write_b32 a100, v5\n\t"
    "v_accvgpr_read_b32 v6, a101\n\t"
    "v_mul_f32_e32 v6, v17, v6\n\t"
    "v_accvgpr_write_b32 a101, v6\n\t"
    "v_accvgpr_read_b32 v5, a102\n\t"
    "v_mul_f32_e32 v5, v17, v5\n\t"
    "v_accvgpr_write_b32 a102, v5\n\t"
    "v_accvgpr_read_b32 v6, a103\n\t"
    "v_mul_f32_e32 v6, v17, v6\n\t"
    "v_accvgpr_write_b32 a103, v6\n\t"
    "v_accvgpr_read_b32 v5, a104\n\t"
    "v_mul_f32_e32 v5, v17, v5\n\t"
    "v_accvgpr_write_b32 a104, v5\n\t"
    "v_accvgpr_read_b32 v6, a105\n\t"
    "v_mul_f32_e32 v6, v17, v6\n\t"
    "v_accvgpr_write_b32 a105, v6\n\t"
    "v_accvgpr_read_b32 v5, a106\n\t"
    "v_mul_f32_e32 v5, v17, v5\n\t"
    "v_accvgpr_write_b32 a106, v5\n\t"
    "v_accvgpr_read_b32 v6, a107\n\t"
    "v_mul_f32_e32 v6, v17, v6\n\t"
    "v_accvgpr_write_b32 a107, v6\n\t"
    "v_accvgpr_read_b32 v5, a108\n\t"
    "v_mul_f32_e32 v5, v17, v5\n\t"
    "v_accvgpr_write_b32 a108, v5\n\t"
    "v_accvgpr_read_b32 v6, a109\n\t"
    "v_mul_f32_e32 v6, v17, v6\n\t"
    "v_accvgpr_write_b32 a109, v6\n\t"
    "v_accvgpr_read_b32 v5, a110\n\t"
    "v_mul_f32_e32 v5, v17, v5\n\t"
    "v_accvgpr_write_b32 a110, v5\n\t"
    "v_accvgpr_read_b32 v6, a111\n\t"
    "v_mul_f32_e32 v6, v17, v6\n\t"
    "v_accvgpr_write_b32 a111, v6\n\t"
    "v_accvgpr_read_b32 v5, a112\n\t"
    "v_mul_f32_e32 v5, v18, v5\n\t"
    "v_accvgpr_write_b32 a112, v5\n\t"
    "v_accvgpr_read_b32 v6, a113\n\t"
    "v_mul_f32_e32 v6, v18, v6\n\t"
    "v_accvgpr_write_b32 a113, v6\n\t"
    "v_accvgpr_read_b32 v5, a114\n\t"
    "v_mul_f32_e32 v5, v18, v5\n\t"
    "v_accvgpr_write_b32 a114, v5\n\t"
    "v_accvgpr_read_b32 v6, a115\n\t"
    "v_mul_f32_e32 v6, v18, v6\n\t"
    "v_accvgpr_write_b32 a115, v6\n\t"
    "v_accvgpr_read_b32 v5, a116\n\t"
    "v_mul_f32_e32 v5, v18, v5\n\t"
    "v_accvgpr_write_b32 a116, v5\n\t"
    "v_accvgpr_read_b32 v6, a117\n\t"
    "v_mul_f32_e32 v6, v18, v6\n\t"
    "v_accvgpr_write_b32 a117, v6\n\t"
    "v_accvgpr_read_b32 v5, a118\n\t"
    "v_mul_f32_e32 v5, v18, v5\n\t"
    "v_accvgpr_write_b32 a118, v5\n\t"
    "v_accvgpr_read_b32 v6, a119\n\t"
    "v_mul_f32_e32 v6, v18, v6\n\t"
    "v_accvgpr_write_b32 a119, v6\n\t"
    "v_accvgpr_read_b32 v5, a120\n\t"
    "v_mul_f32_e32 v5, v18, v5\n\t"
    "v_accvgpr_write_b32 a120, v5\n\t"
    "v_accvgpr_read_b32 v6, a121\n\t"
    "v_mul_f32_e32 v6, v18, v6\n\t"
    "v_accvgpr_write_b32 a121, v6\n\t"
    "v_accvgpr_read_b32 v5, a122\n\t"
    "v_mul_f32_e32 v5, v18, v5\n\t"
    "v_accvgpr_write_b32 a122, v5\n\t"
    "v_accvgpr_read_b32 v6, a123\n\t"
    "v_mul_f32_e32 v6, v18, v6\n\t"
    "v_accvgpr_write_b32 a123, v6\n\t"
    "v_accvgpr_read_b32 v5, a124\n\t"
    "v_mul_f32_e32 v5, v18, v5\n\t"
    "v_accvgpr_write_b32 a124, v5\n\t"
    "v_accvgpr_read_b32 v6, a125\n\t"
    "v_mul_f32_e32 v6, v18, v6\n\t"
    "v_accvgpr_write_b32 a125, v6\n\t"
    "v_accvgpr_read_b32 v5, a126\n\t"
    "v_mul_f32_e32 v5, v18, v5\n\t"
    "v_accvgpr_write_b32 a126, v5\n\t"
    "v_accvgpr_read_b32 v6, a127\n\t"
    "v_mul_f32_e32 v6, v18, v6\n\t"
    "v_accvgpr_write_b32 a127, v6\n\t"
    "s_mov_b32 s47, 0\n\t"
    "s_nop 1\n\t"
    "s_branch L_RRET_l4\n\t"
    "L_RARE_l5:\n\t"
    "s_nop 1\n\t"
    "v_max_f32_e32 v17, 0, v30\n\t"
    "v_add_f32_e32 v24, v24, v17\n\t"
    "v_sub_f32_e32 v96, v96, v17\n\t"
    "v_sub_f32_e32 v97, v97, v17\n\t"
    "v_sub_f32_e32 v98, v98, v17\n\t"
    "v_sub_f32_e32 v99, v99, v17\n\t"
    "v_sub_f32_e32 v100, v100, v17\n\t"
    "v_sub_f32_e32 v101, v101, v17\n\t"
    "v_sub_f32_e32 v102, v102, v17\n\t"
    "v_sub_f32_e32 v103, v103, v17\n\t"
    "v_sub_f32_e32 v104, v104, v17\n\t"
    "v_sub_f32_e32 v105, v105, v17\n\t"
    "v_sub_f32_e32 v106, v106, v17\n\t"
    "v_sub_f32_e32 v107, v107, v17\n\t"
    "v_sub_f32_e32 v108, v108, v17\n\t"
    "v_sub_f32_e32 v109, v109, v17\n\t"
    "v_sub_f32_e32 v110, v110, v17\n\t"
    "v_sub_f32_e32 v111, v111, v17\n\t"
    "v_sub_f32_e32 v128, v128, v17\n\t"
    "v_sub_f32_e32 v129, v129, v17\n\t"
    "v_sub_f32_e32 v130, v130, v17\n\t"
    "v_sub_f32_e32 v131, v131, v17\n\t"
    "v_sub_f32_e32 v132, v132, v17\n\t"
    "v_sub_f32_e32 v133, v133, v17\n\t"
    "v_sub_f32_e32 v134, v134, v17\n\t"
    "v_sub_f32_e32 v135, v135, v17\n\t"
    "v_sub_f32_e32 v136, v136, v17\n\t"
    "v_sub_f32_e32 v137, v137, v17\n\t"
    "v_sub_f32_e32 v138, v138, v17\n\t"
    "v_sub_f32_e32 v139, v139, v17\n\t"
    "v_sub_f32_e32 v140, v140, v17\n\t"
    "v_sub_f32_e32 v141, v141, v17\n\t"
    "v_sub_f32_e32 v142, v142, v17\n\t"
    "v_sub_f32_e32 v143, v143, v17\n\t"
    "v_sub_f32_e32 v160, 0, v24\n\t"
    "v_sub_f32_e32 v161, 0, v24\n\t"
    "v_sub_f32_e32 v162, 0, v24\n\t"
    "v_sub_f32_e32 v163, 0, v24\n\t"
    "v_sub_f32_e32 v164, 0, v24\n\t"
    "v_sub_f32_e32 v165, 0, v24\n\t"
    "v_sub_f32_e32 v166, 0, v24\n\t"
    "v_sub_f32_e32 v167, 0, v24\n\t"
    "v_sub_f32_e32 v168, 0, v24\n\t"
    "v_sub_f32_e32 v169, 0, v24\n\t"
    "v_sub_f32_e32 v170, 0, v24\n\t"
    "v_sub_f32_e32 v171, 0, v24\n\t"
    "v_sub_f32_e32 v172, 0, v24\n\t"
    "v_sub_f32_e32 v173, 0, v24\n\t"
    "v_sub_f32_e32 v174, 0, v24\n\t"
    "v_sub_f32_e32 v175, 0, v24\n\t"
    "v_sub_f32_e32 v17, 0, v17\n\t"
    "v_exp_f32_e32 v17, v17\n\t"
    "s_nop 0\n\t"
    "v_mul_f32_e32 v20, v17, v20\n\t"
    "v_mul_f32_e32 v22, v17, v22\n\t"
    "v_max_f32_e32 v18, 0, v31\n\t"
    "v_add_f32_e32 v25, v25, v18\n\t"
    "v_sub_f32_e32 v112, v112, v18\n\t"
    "v_sub_f32_e32 v113, v113, v18\n\t"
    "v_sub_f32_e32 v114, v114, v18\n\t"
    "v_sub_f32_e32 v115, v115, v18\n\t"
    "v_sub_f32_e32 v116, v116, v18\n\t"
    "v_sub_f32_e32 v117, v117, v18\n\t"
    "v_sub_f32_e32 v118, v118, v18\n\t"
    "v_sub_f32_e32 v119, v119, v18\n\t"
    "v_sub_f32_e32 v120, v120, v18\n\t"
    "v_sub_f32_e32 v121, v121, v18\n\t"
    "v_sub_f32_e32 v122, v122, v18\n\t"
    "v_sub_f32_e32 v123, v123, v18\n\t"
    "v_sub_f32_e32 v124, v124, v18\n\t"
    "v_sub_f32_e32 v125, v125, v18\n\t"
    "v_sub_f32_e32 v126, v126, v18\n\t"
    "v_sub_f32_e32 v127, v127, v18\n\t"
    "v_sub_f32_e32 v144, v144, v18\n\t"
    "v_sub_f32_e32 v145, v145, v18\n\t"
    "v_sub_f32_e32 v146, v146, v18\n\t"
    "v_sub_f32_e32 v147, v147, v18\n\t"
    "v_sub_f32_e32 v148, v148, v18\n\t"
    "v_sub_f32_e32 v149, v149, v18\n\t"
    "v_sub_f32_e32 v150, v150, v18\n\t"
    "v_sub_f32_e32 v151, v151, v18\n\t"
    "v_sub_f32_e32 v152, v152, v18\n\t"
    "v_sub_f32_e32 v153, v153, v18\n\t"
    "v_sub_f32_e32 v154, v154, v18\n\t"
    "v_sub_f32_e32 v155, v155, v18\n\t"
    "v_sub_f32_e32 v156, v156, v18\n\t"
    "v_sub_f32_e32 v157, v157, v18\n\t"
    "v_sub_f32_e32 v158, v158, v18\n\t"
    "v_sub_f32_e32 v159, v159, v18\n\t"
    "v_sub_f32_e32 v176, 0, v25\n\t"
    "v_sub_f32_e32 v177, 0, v25\n\t"
    "v_sub_f32_e32 v178, 0, v25\n\t"
    "v_sub_f32_e32 v179, 0, v25\n\t"
    "v_sub_f32_e32 v180, 0, v25\n\t"
    "v_sub_f32_e32 v181, 0, v25\n\t"
    "v_sub_f32_e32 v182, 0, v25\n\t"
    "v_sub_f32_e32 v183, 0, v25\n\t"
    "v_sub_f32_e32 v184, 0, v25\n\t"
    "v_sub_f32_e32 v185, 0, v25\n\t"
    "v_sub_f32_e32 v186, 0, v25\n\t"
    "v_sub_f32_e32 v187, 0, v25\n\t"
    "v_sub_f32_e32 v188, 0, v25\n\t"
    "v_sub_f32_e32 v189, 0, v25\n\t"
    "v_sub_f32_e32 v190, 0, v25\n\t"
    "v_sub_f32_e32 v191, 0, v25\n\t"
    "v_sub_f32_e32 v18, 0, v18\n\t"
    "v_exp_f32_e32 v18, v18\n\t"
    "s_nop 0\n\t"
    "v_mul_f32_e32 v21, v18, v21\n\t"
    "v_mul_f32_e32 v23, v18, v23\n\t"
    "s_mov_b32 s47, 1\n\t"
    "s_nop 1\n\t"
    "s_branch L_RET_l5\n\t"
    "L_RESC_l5:\n\t"
    "s_nop 7\n\t"
    "s_nop 7\n\t"
    "v_accvgpr_read_b32 v5, a0\n\t"
    "v_mul_f32_e32 v5, v17, v5\n\t"
    "v_accvgpr_write_b32 a0, v5\n\t"
    "v_accvgpr_read_b32 v6, a1\n\t"
    "v_mul_f32_e32 v6, v17, v6\n\t"
    "v_accvgpr_write_b32 a1, v6\n\t"
    "v_accvgpr_read_b32 v5, a2\n\t"
    "v_mul_f32_e32 v5, v17, v5\n\t"
    "v_accvgpr_write_b32 a2, v5\n\t"
    "v_accvgpr_read_b32 v6, a3\n\t"
    "v_mul_f32_e32 v6, v17, v6\n\t"
    "v_accvgpr_write_b32 a3, v6\n\t"
    "v_accvgpr_read_b32 v5, a4\n\t"
    "v_mul_f32_e32 v5, v17, v5\n\t"
    "v_accvgpr_write_b32 a4, v5\n\t"
    "v_accvgpr_read_b32 v6, a5\n\t"
    "v_mul_f32_e32 v6, v17, v6\n\t"
    "v_accvgpr_write_b32 a5, v6\n\t"
    "v_accvgpr_read_b32 v5, a6\n\t"
    "v_mul_f32_e32 v5, v17, v5\n\t"
    "v_accvgpr_write_b32 a6, v5\n\t"
    "v_accvgpr_read_b32 v6, a7\n\t"
    "v_mul_f32_e32 v6, v17, v6\n\t"
    "v_accvgpr_write_b32 a7, v6\n\t"
    "v_accvgpr_read_b32 v5, a8\n\t"
    "v_mul_f32_e32 v5, v17, v5\n\t"
    "v_accvgpr_write_b32 a8, v5\n\t"
    "v_accvgpr_read_b32 v6, a9\n\t"
    "v_mul_f32_e32 v6, v17, v6\n\t"
    "v_accvgpr_write_b32 a9, v6\n\t"
    "v_accvgpr_read_b32 v5, a10\n\t"
    "v_mul_f32_e32 v5, v17, v5\n\t"
    "v_accvgpr_write_b32 a10, v5\n\t"
    "v_accvgpr_read_b32 v6, a11\n\t"
    "v_mul_f32_e32 v6, v17, v6\n\t"
    "v_accvgpr_write_b32 a11, v6\n\t"
    "v_accvgpr_read_b32 v5, a12\n\t"
    "v_mul_f32_e32 v5, v17, v5\n\t"
    "v_accvgpr_write_b32 a12, v5\n\t"
    "v_accvgpr_read_b32 v6, a13\n\t"
    "v_mul_f32_e32 v6, v17, v6\n\t"
    "v_accvgpr_write_b32 a13, v6\n\t"
    "v_accvgpr_read_b32 v5, a14\n\t"
    "v_mul_f32_e32 v5, v17, v5\n\t"
    "v_accvgpr_write_b32 a14, v5\n\t"
    "v_accvgpr_read_b32 v6, a15\n\t"
    "v_mul_f32_e32 v6, v17, v6\n\t"
    "v_accvgpr_write_b32 a15, v6\n\t"
    "v_accvgpr_read_b32 v5, a16\n\t"
    "v_mul_f32_e32 v5, v18, v5\n\t"
    "v_accvgpr_write_b32 a16, v5\n\t"
    "v_accvgpr_read_b32 v6, a17\n\t"
    "v_mul_f32_e32 v6, v18, v6\n\t"
    "v_accvgpr_write_b32 a17, v6\n\t"
    "v_accvgpr_read_b32 v5, a18\n\t"
    "v_mul_f32_e32 v5, v18, v5\n\t"
    "v_accvgpr_write_b32 a18, v5\n\t"
    "v_accvgpr_read_b32 v6, a19\n\t"
    "v_mul_f32_e32 v6, v18, v6\n\t"
    "v_accvgpr_write_b32 a19, v6\n\t"
    "v_accvgpr_read_b32 v5, a20\n\t"
    "v_mul_f32_e32 v5, v18, v5\n\t"
    "v_accvgpr_write_b32 a20, v5\n\t"
    "v_accvgpr_read_b32 v6, a21\n\t"
    "v_mul_f32_e32 v6, v18, v6\n\t"
    "v_accvgpr_write_b32 a21, v6\n\t"
    "v_accvgpr_read_b32 v5, a22\n\t"
    "v_mul_f32_e32 v5, v18, v5\n\t"
    "v_accvgpr_write_b32 a22, v5\n\t"
    "v_accvgpr_read_b32 v6, a23\n\t"
    "v_mul_f32_e32 v6, v18, v6\n\t"
    "v_accvgpr_write_b32 a23, v6\n\t"
    "v_accvgpr_read_b32 v5, a24\n\t"
    "v_mul_f32_e32 v5, v18, v5\n\t"
    "v_accvgpr_write_b32 a24, v5\n\t"
    "v_accvgpr_read_b32 v6, a25\n\t"
    "v_mul_f32_e32 v6, v18, v6\n\t"
    "v_accvgpr_write_b32 a25, v6\n\t"
    "v_accvgpr_read_b32 v5, a26\n\t"
    "v_mul_f32_e32 v5, v18, v5\n\t"
    "v_accvgpr_write_b32 a26, v5\n\t"
    "v_accvgpr_read_b32 v6, a27\n\t"
    "v_mul_f32_e32 v6, v18, v6\n\t"
    "v_accvgpr_write_b32 a27, v6\n\t"
    "v_accvgpr_read_b32 v5, a28\n\t"
    "v_mul_f32_e32 v5, v18, v5\n\t"
    "v_accvgpr_write_b32 a28, v5\n\t"
    "v_accvgpr_read_b32 v6, a29\n\t"
    "v_mul_f32_e32 v6, v18, v6\n\t"
    "v_accvgpr_write_b32 a29, v6\n\t"
    "v_accvgpr_read_b32 v5, a30\n\t"
    "v_mul_f32_e32 v5, v18, v5\n\t"
    "v_accvgpr_write_b32 a30, v5\n\t"
    "v_accvgpr_read_b32 v6, a31\n\t"
    "v_mul_f32_e32 v6, v18, v6\n\t"
    "v_accvgpr_write_b32 a31, v6\n\t"
    "v_accvgpr_read_b32 v5, a32\n\t"
    "v_mul_f32_e32 v5, v17, v5\n\t"
    "v_accvgpr_write_b32 a32, v5\n\t"
    "v_accvgpr_read_b32 v6, a33\n\t"
    "v_mul_f32_e32 v6, v17, v6\n\t"
    "v_accvgpr_write_b32 a33, v6\n\t"
    "v_accvgpr_read_b32 v5, a34\n\t"
    "v_mul_f32_e32 v5, v17, v5\n\t"
    "v_accvgpr_write_b32 a34, v5\n\t"
    "v_accvgpr_read_b32 v6, a35\n\t"
    "v_mul_f32_e32 v6, v17, v6\n\t"
    "v_accvgpr_write_b32 a35, v6\n\t"
    "v_accvgpr_read_b32 v5, a36\n\t"
    "v_mul_f32_e32 v5, v17, v5\n\t"
    "v_accvgpr_write_b32 a36, v5\n\t"
    "v_accvgpr_read_b32 v6, a37\n\t"
    "v_mul_f32_e32 v6, v17, v6\n\t"
    "v_accvgpr_write_b32 a37, v6\n\t"
    "v_accvgpr_read_b32 v5, a38\n\t"
    "v_mul_f32_e32 v5, v17, v5\n\t"
    "v_accvgpr_write_b32 a38, v5\n\t"
    "v_accvgpr_read_b32 v6, a39\n\t"
    "v_mul_f32_e32 v6, v17, v6\n\t"
    "v_accvgpr_write_b32 a39, v6\n\t"
    "v_accvgpr_read_b32 v5, a40\n\t"
    "v_mul_f32_e32 v5, v17, v5\n\t"
    "v_accvgpr_write_b32 a40, v5\n\t"
    "v_accvgpr_read_b32 v6, a41\n\t"
    "v_mul_f32_e32 v6, v17, v6\n\t"
    "v_accvgpr_write_b32 a41, v6\n\t"
    "v_accvgpr_read_b32 v5, a42\n\t"
    "v_mul_f32_e32 v5, v17, v5\n\t"
    "v_accvgpr_write_b32 a42, v5\n\t"
    "v_accvgpr_read_b32 v6, a43\n\t"
    "v_mul_f32_e32 v6, v17, v6\n\t"
    "v_accvgpr_write_b32 a43, v6\n\t"
    "v_accvgpr_read_b32 v5, a44\n\t"
    "v_mul_f32_e32 v5, v17, v5\n\t"
    "v_accvgpr_write_b32 a44, v5\n\t"
    "v_accvgpr_read_b32 v6, a45\n\t"
    "v_mul_f32_e32 v6, v17, v6\n\t"
    "v_accvgpr_write_b32 a45, v6\n\t"
    "v_accvgpr_read_b32 v5, a46\n\t"
    "v_mul_f32_e32 v5, v17, v5\n\t"
    "v_accvgpr_write_b32 a46, v5\n\t"
    "v_accvgpr_read_b32 v6, a47\n\t"
    "v_mul_f32_e32 v6, v17, v6\n\t"
    "v_accvgpr_write_b32 a47, v6\n\t"
    "v_accvgpr_read_b32 v5, a48\n\t"
    "v_mul_f32_e32 v5, v18, v5\n\t"
    "v_accvgpr_write_b32 a48, v5\n\t"
    "v_accvgpr_read_b32 v6, a49\n\t"
    "v_mul_f32_e32 v6, v18, v6\n\t"
    "v_accvgpr_write_b32 a49, v6\n\t"
    "v_accvgpr_read_b32 v5, a50\n\t"
    "v_mul_f32_e32 v5, v18, v5\n\t"
    "v_accvgpr_write_b32 a50, v5\n\t"
    "v_accvgpr_read_b32 v6, a51\n\t"
    "v_mul_f32_e32 v6, v18, v6\n\t"
    "v_accvgpr_write_b32 a51, v6\n\t"
    "v_accvgpr_read_b32 v5, a52\n\t"
    "v_mul_f32_e32 v5, v18, v5\n\t"
    "v_accvgpr_write_b32 a52, v5\n\t"
    "v_accvgpr_read_b32 v6, a53\n\t"
    "v_mul_f32_e32 v6, v18, v6\n\t"
    "v_accvgpr_write_b32 a53, v6\n\t"
    "v_accvgpr_read_b32 v5, a54\n\t"
    "v_mul_f32_e32 v5, v18, v5\n\t"
    "v_accvgpr_write_b32 a54, v5\n\t"
    "v_accvgpr_read_b32 v6, a55\n\t"
    "v_mul_f32_e32 v6, v18, v6\n\t"
    "v_accvgpr_write_b32 a55, v6\n\t"
    "v_accvgpr_read_b32 v5, a56\n\t"
    "v_mul_f32_e32 v5, v18, v5\n\t"
    "v_accvgpr_write_b32 a56, v5\n\t"
    "v_accvgpr_read_b32 v6, a57\n\t"
    "v_mul_f32_e32 v6, v18, v6\n\t"
    "v_accvgpr_write_b32 a57, v6\n\t"
    "v_accvgpr_read_b32 v5, a58\n\t"
    "v_mul_f32_e32 v5, v18, v5\n\t"
    "v_accvgpr_write_b32 a58, v5\n\t"
    "v_accvgpr_read_b32 v6, a59\n\t"
    "v_mul_f32_e32 v6, v18, v6\n\t"
    "v_accvgpr_write_b32 a59, v6\n\t"
    "v_accvgpr_read_b32 v5, a60\n\t"
    "v_mul_f32_e32 v5, v18, v5\n\t"
    "v_accvgpr_write_b32 a60, v5\n\t"
    "v_accvgpr_read_b32 v6, a61\n\t"
    "v_mul_f32_e32 v6, v18, v6\n\t"
    "v_accvgpr_write_b32 a61, v6\n\t"
    "v_accvgpr_read_b32 v5, a62\n\t"
    "v_mul_f32_e32 v5, v18, v5\n\t"
    "v_accvgpr_write_b32 a62, v5\n\t"
    "v_accvgpr_read_b32 v6, a63\n\t"
    "v_mul_f32_e32 v6, v18, v6\n\t"
    "v_accvgpr_write_b32 a63, v6\n\t"
    "v_accvgpr_read_b32 v5, a64\n\t"
    "v_mul_f32_e32 v5, v17, v5\n\t"
    "v_accvgpr_write_b32 a64, v5\n\t"
    "v_accvgpr_read_b32 v6, a65\n\t"
    "v_mul_f32_e32 v6, v17, v6\n\t"
    "v_accvgpr_write_b32 a65, v6\n\t"
    "v_accvgpr_read_b32 v5, a66\n\t"
    "v_mul_f32_e32 v5, v17, v5\n\t"
    "v_accvgpr_write_b32 a66, v5\n\t"
    "v_accvgpr_read_b32 v6, a67\n\t"
    "v_mul_f32_e32 v6, v17, v6\n\t"
    "v_accvgpr_write_b32 a67, v6\n\t"
    "v_accvgpr_read_b32 v5, a68\n\t"
    "v_mul_f32_e32 v5, v17, v5\n\t"
    "v_accvgpr_write_b32 a68, v5\n\t"
    "v_accvgpr_read_b32 v6, a69\n\t"
    "v_mul_f32_e32 v6, v17, v6\n\t"
    "v_accvgpr_write_b32 a69, v6\n\t"
    "v_accvgpr_read_b32 v5, a70\n\t"
    "v_mul_f32_e32 v5, v17, v5\n\t"
    "v_accvgpr_write_b32 a70, v5\n\t"
    "v_accvgpr_read_b32 v6, a71\n\t"
    "v_mul_f32_e32 v6, v17, v6\n\t"
    "v_accvgpr_write_b32 a71, v6\n\t"
    "v_accvgpr_read_b32 v5, a72\n\t"
    "v_mul_f32_e32 v5, v17, v5\n\t"
    "v_accvgpr_write_b32 a72, v5\n\t"
    "v_accvgpr_read_b32 v6, a73\n\t"
    "v_mul_f32_e32 v6, v17, v6\n\t"
    "v_accvgpr_write_b32 a73, v6\n\t"
    "v_accvgpr_read_b32 v5, a74\n\t"
    "v_mul_f32_e32 v5, v17, v5\n\t"
    "v_accvgpr_write_b32 a74, v5\n\t"
    "v_accvgpr_read_b32 v6, a75\n\t"
    "v_mul_f32_e32 v6, v17, v6\n\t"
    "v_accvgpr_write_b32 a75, v6\n\t"
    "v_accvgpr_read_b32 v5, a76\n\t"
    "v_mul_f32_e32 v5, v17, v5\n\t"
    "v_accvgpr_write_b32 a76, v5\n\t"
    "v_accvgpr_read_b32 v6, a77\n\t"
    "v_mul_f32_e32 v6, v17, v6\n\t"
    "v_accvgpr_write_b32 a77, v6\n\t"
    "v_accvgpr_read_b32 v5, a78\n\t"
    "v_mul_f32_e32 v5, v17, v5\n\t"
    "v_accvgpr_write_b32 a78, v5\n\t"
    "v_accvgpr_read_b32 v6, a79\n\t"
    "v_mul_f32_e32 v6, v17, v6\n\t"
    "v_accvgpr_write_b32 a79, v6\n\t"
    "v_accvgpr_read_b32 v5, a80\n\t"
    "v_mul_f32_e32 v5, v18, v5\n\t"
    "v_accvgpr_write_b32 a80, v5\n\t"
    "v_accvgpr_read_b32 v6, a81\n\t"
    "v_mul_f32_e32 v6, v18, v6\n\t"
    "v_accvgpr_write_b32 a81, v6\n\t"
    "v_accvgpr_read_b32 v5, a82\n\t"
    "v_mul_f32_e32 v5, v18, v5\n\t"
    "v_accvgpr_write_b32 a82, v5\n\t"
    "v_accvgpr_read_b32 v6, a83\n\t"
    "v_mul_f32_e32 v6, v18, v6\n\t"
    "v_accvgpr_write_b32 a83, v6\n\t"
    "v_accvgpr_read_b32 v5, a84\n\t"
    "v_mul_f32_e32 v5, v18, v5\n\t"
    "v_accvgpr_write_b32 a84, v5\n\t"
    "v_accvgpr_read_b32 v6, a85\n\t"
    "v_mul_f32_e32 v6, v18, v6\n\t"
    "v_accvgpr_write_b32 a85, v6\n\t"
    "v_accvgpr_read_b32 v5, a86\n\t"
    "v_mul_f32_e32 v5, v18, v5\n\t"
    "v_accvgpr_write_b32 a86, v5\n\t"
    "v_accvgpr_read_b32 v6, a87\n\t"
    "v_mul_f32_e32 v6, v18, v6\n\t"
    "v_accvgpr_write_b32 a87, v6\n\t"
    "v_accvgpr_read_b32 v5, a88\n\t"
    "v_mul_f32_e32 v5, v18, v5\n\t"
    "v_accvgpr_write_b32 a88, v5\n\t"
    "v_accvgpr_read_b32 v6, a89\n\t"
    "v_mul_f32_e32 v6, v18, v6\n\t"
    "v_accvgpr_write_b32 a89, v6\n\t"
    "v_accvgpr_read_b32 v5, a90\n\t"
    "v_mul_f32_e32 v5, v18, v5\n\t"
    "v_accvgpr_write_b32 a90, v5\n\t"
    "v_accvgpr_read_b32 v6, a91\n\t"
    "v_mul_f32_e32 v6, v18, v6\n\t"
    "v_accvgpr_write_b32 a91, v6\n\t"
    "v_accvgpr_read_b32 v5, a92\n\t"
    "v_mul_f32_e32 v5, v18, v5\n\t"
    "v_accvgpr_write_b32 a92, v5\n\t"
    "v_accvgpr_read_b32 v6, a93\n\t"
    "v_mul_f32_e32 v6, v18, v6\n\t"
    "v_accvgpr_write_b32 a93, v6\n\t"
    "v_accvgpr_read_b32 v5, a94\n\t"
    "v_mul_f32_e32 v5, v18, v5\n\t"
    "v_accvgpr_write_b32 a94, v5\n\t"
    "v_accvgpr_read_b32 v6, a95\n\t"
    "v_mul_f32_e32 v6, v18, v6\n\t"
    "v_accvgpr_write_b32 a95, v6\n\t"
    "v_accvgpr_read_b32 v5, a96\n\t"
    "v_mul_f32_e32 v5, v17, v5\n\t"
    "v_accvgpr_write_b32 a96, v5\n\t"
    "v_accvgpr_read_b32 v6, a97\n\t"
    "v_mul_f32_e32 v6, v17, v6\n\t"
    "v_accvgpr_write_b32 a97, v6\n\t"
    "v_accvgpr_read_b32 v5, a98\n\t"
    "v_mul_f32_e32 v5, v17, v5\n\t"
    "v_accvgpr_write_b32 a98, v5\n\t"
    "v_accvgpr_read_b32 v6, a99\n\t"
    "v_mul_f32_e32 v6, v17, v6\n\t"
    "v_accvgpr_write_b32 a99, v6\n\t"
    "v_accvgpr_read_b32 v5, a100\n\t"
    "v_mul_f32_e32 v5, v17, v5\n\t"
    "v_accvgpr_write_b32 a100, v5\n\t"
    "v_accvgpr_read_b32 v6, a101\n\t"
    "v_mul_f32_e32 v6, v17, v6\n\t"
    "v_accvgpr_write_b32 a101, v6\n\t"
    "v_accvgpr_read_b32 v5, a102\n\t"
    "v_mul_f32_e32 v5, v17, v5\n\t"
    "v_accvgpr_write_b32 a102, v5\n\t"
    "v_accvgpr_read_b32 v6, a103\n\t"
    "v_mul_f32_e32 v6, v17, v6\n\t"
    "v_accvgpr_write_b32 a103, v6\n\t"
    "v_accvgpr_read_b32 v5, a104\n\t"
    "v_mul_f32_e32 v5, v17, v5\n\t"
    "v_accvgpr_write_b32 a104, v5\n\t"
    "v_accvgpr_read_b32 v6, a105\n\t"
    "v_mul_f32_e32 v6, v17, v6\n\t"
    "v_accvgpr_write_b32 a105, v6\n\t"
    "v_accvgpr_read_b32 v5, a106\n\t"
    "v_mul_f32_e32 v5, v17, v5\n\t"
    "v_accvgpr_write_b32 a106, v5\n\t"
    "v_accvgpr_read_b32 v6, a107\n\t"
    "v_mul_f32_e32 v6, v17, v6\n\t"
    "v_accvgpr_write_b32 a107, v6\n\t"
    "v_accvgpr_read_b32 v5, a108\n\t"
    "v_mul_f32_e32 v5, v17, v5\n\t"
    "v_accvgpr_write_b32 a108, v5\n\t"
    "v_accvgpr_read_b32 v6, a109\n\t"
    "v_mul_f32_e32 v6, v17, v6\n\t"
    "v_accvgpr_write_b32 a109, v6\n\t"
    "v_accvgpr_read_b32 v5, a110\n\t"
    "v_mul_f32_e32 v5, v17, v5\n\t"
    "v_accvgpr_write_b32 a110, v5\n\t"
    "v_accvgpr_read_b32 v6, a111\n\t"
    "v_mul_f32_e32 v6, v17, v6\n\t"
    "v_accvgpr_write_b32 a111, v6\n\t"
    "v_accvgpr_read_b32 v5, a112\n\t"
    "v_mul_f32_e32 v5, v18, v5\n\t"
    "v_accvgpr_write_b32 a112, v5\n\t"
    "v_accvgpr_read_b32 v6, a113\n\t"
    "v_mul_f32_e32 v6, v18, v6\n\t"
    "v_accvgpr_write_b32 a113, v6\n\t"
    "v_accvgpr_read_b32 v5, a114\n\t"
    "v_mul_f32_e32 v5, v18, v5\n\t"
    "v_accvgpr_write_b32 a114, v5\n\t"
    "v_accvgpr_read_b32 v6, a115\n\t"
    "v_mul_f32_e32 v6, v18, v6\n\t"
    "v_accvgpr_write_b32 a115, v6\n\t"
    "v_accvgpr_read_b32 v5, a116\n\t"
    "v_mul_f32_e32 v5, v18, v5\n\t"
    "v_accvgpr_write_b32 a116, v5\n\t"
    "v_accvgpr_read_b32 v6, a117\n\t"
    "v_mul_f32_e32 v6, v18, v6\n\t"
    "v_accvgpr_write_b32 a117, v6\n\t"
    "v_accvgpr_read_b32 v5, a118\n\t"
    "v_mul_f32_e32 v5, v18, v5\n\t"
    "v_accvgpr_write_b32 a118, v5\n\t"
    "v_accvgpr_read_b32 v6, a119\n\t"
    "v_mul_f32_e32 v6, v18, v6\n\t"
    "v_accvgpr_write_b32 a119, v6\n\t"
    "v_accvgpr_read_b32 v5, a120\n\t"
    "v_mul_f32_e32 v5, v18, v5\n\t"
    "v_accvgpr_write_b32 a120, v5\n\t"
    "v_accvgpr_read_b32 v6, a121\n\t"
    "v_mul_f32_e32 v6, v18, v6\n\t"
    "v_accvgpr_write_b32 a121, v6\n\t"
    "v_accvgpr_read_b32 v5, a122\n\t"
    "v_mul_f32_e32 v5, v18, v5\n\t"
    "v_accvgpr_write_b32 a122, v5\n\t"
    "v_accvgpr_read_b32 v6, a123\n\t"
    "v_mul_f32_e32 v6, v18, v6\n\t"
    "v_accvgpr_write_b32 a123, v6\n\t"
    "v_accvgpr_read_b32 v5, a124\n\t"
    "v_mul_f32_e32 v5, v18, v5\n\t"
    "v_accvgpr_write_b32 a124, v5\n\t"
    "v_accvgpr_read_b32 v6, a125\n\t"
    "v_mul_f32_e32 v6, v18, v6\n\t"
    "v_accvgpr_write_b32 a125, v6\n\t"
    "v_accvgpr_read_b32 v5, a126\n\t"
    "v_mul_f32_e32 v5, v18, v5\n\t"
    "v_accvgpr_write_b32 a126, v5\n\t"
    "v_accvgpr_read_b32 v6, a127\n\t"
    "v_mul_f32_e32 v6, v18, v6\n\t"
    "v_accvgpr_write_b32 a127, v6\n\t"
    "s_mov_b32 s47, 0\n\t"
    "s_nop 1\n\t"
    "s_branch L_RRET_l5\n\t"
    "L_RARE_l6:\n\t"
    "s_nop 1\n\t"
    "v_max_f32_e32 v17, 0, v30\n\t"
    "v_add_f32_e32 v24, v24, v17\n\t"
    "v_sub_f32_e32 v32, v32, v17\n\t"
    "v_sub_f32_e32 v33, v33, v17\n\t"
    "v_sub_f32_e32 v34, v34, v17\n\t"
    "v_sub_f32_e32 v35, v35, v17\n\t"
    "v_sub_f32_e32 v36, v36, v17\n\t"
    "v_sub_f32_e32 v37, v37, v17\n\t"
    "v_sub_f32_e32 v38, v38, v17\n\t"
    "v_sub_f32_e32 v39, v39, v17\n\t"
    "v_sub_f32_e32 v40, v40, v17\n\t"
    "v_sub_f32_e32 v41, v41, v17\n\t"
    "v_sub_f32_e32 v42, v42, v17\n\t"
    "v_sub_f32_e32 v43, v43, v17\n\t"
    "v_sub_f32_e32 v44, v44, v17\n\t"
    "v_sub_f32_e32 v45, v45, v17\n\t"
    "v_sub_f32_e32 v46, v46, v17\n\t"
    "v_sub_f32_e32 v47, v47, v17\n\t"
    "v_sub_f32_e32 v64, v64, v17\n\t"
    "v_sub_f32_e32 v65, v65, v17\n\t"
    "v_sub_f32_e32 v66, v66, v17\n\t"
    "v_sub_f32_e32 v67, v67, v17\n\t"
    "v_sub_f32_e32 v68, v68, v17\n\t"
    "v_sub_f32_e32 v69, v69, v17\n\t"
    "v_sub_f32_e32 v70, v70, v17\n\t"
    "v_sub_f32_e32 v71, v71, v17\n\t"
    "v_sub_f32_e32 v72, v72, v17\n\t"
    "v_sub_f32_e32 v73, v73, v17\n\t"
    "v_sub_f32_e32 v74, v74, v17\n\t"
    "v_sub_f32_e32 v75, v75, v17\n\t"
    "v_sub_f32_e32 v76, v76, v17\n\t"
    "v_sub_f32_e32 v77, v77, v17\n\t"
    "v_sub_f32_e32 v78, v78, v17\n\t"
    "v_sub_f32_e32 v79, v79, v17\n\t"
    "v_sub_f32_e32 v160, 0, v24\n\t"
    "v_sub_f32_e32 v161, 0, v24\n\t"
    "v_sub_f32_e32 v162, 0, v24\n\t"
    "v_sub_f32_e32 v163, 0, v24\n\t"
    "v_sub_f32_e32 v164, 0, v24\n\t"
    "v_sub_f32_e32 v165, 0, v24\n\t"
    "v_sub_f32_e32 v166, 0, v24\n\t"
    "v_sub_f32_e32 v167, 0, v24\n\t"
    "v_sub_f32_e32 v168, 0, v24\n\t"
    "v_sub_f32_e32 v169, 0, v24\n\t"
    "v_sub_f32_e32 v170, 0, v24\n\t"
    "v_sub_f32_e32 v171, 0, v24\n\t"
    "v_sub_f32_e32 v172, 0, v24\n\t"
    "v_sub_f32_e32 v173, 0, v24\n\t"
    "v_sub_f32_e32 v174, 0, v24\n\t"
    "v_sub_f32_e32 v175, 0, v24\n\t"
    "v_sub_f32_e32 v17, 0, v17\n\t"
    "v_exp_f32_e32 v17, v17\n\t"
    "s_nop 0\n\t"
    "v_mul_f32_e32 v20, v17, v20\n\t"
    "v_mul_f32_e32 v22, v17, v22\n\t"
    "v_max_f32_e32 v18, 0, v31\n\t"
    "v_add_f32_e32 v25, v25, v18\n\t"
    "v_sub_f32_e32 v48, v48, v18\n\t"
    "v_sub_f32_e32 v49, v49, v18\n\t"
    "v_sub_f32_e32 v50, v50, v18\n\t"
    "v_sub_f32_e32 v51, v51, v18\n\t"
    "v_sub_f32_e32 v52, v52, v18\n\t"
    "v_sub_f32_e32 v53, v53, v18\n\t"
    "v_sub_f32_e32 v54, v54, v18\n\t"
    "v_sub_f32_e32 v55, v55, v18\n\t"
    "v_sub_f32_e32 v56, v56, v18\n\t"
    "v_sub_f32_e32 v57, v57, v18\n\t"
    "v_sub_f32_e32 v58, v58, v18\n\t"
    "v_sub_f32_e32 v59, v59, v18\n\t"
    "v_sub_f32_e32 v60, v60, v18\n\t"
    "v_sub_f32_e32 v61, v61, v18\n\t"
    "v_sub_f32_e32 v62, v62, v18\n\t"
    "v_sub_f32_e32 v63, v63, v18\n\t"
    "v_sub_f32_e32 v80, v80, v18\n\t"
    "v_sub_f32_e32 v81, v81, v18\n\t"
    "v_sub_f32_e32 v82, v82, v18\n\t"
    "v_sub_f32_e32 v83, v83, v18\n\t"
    "v_sub_f32_e32 v84, v84, v18\n\t"
    "v_sub_f32_e32 v85, v85, v18\n\t"
    "v_sub_f32_e32 v86, v86, v18\n\t"
    "v_sub_f32_e32 v87, v87, v18\n\t"
    "v_sub_f32_e32 v88, v88, v18\n\t"
    "v_sub_f32_e32 v89, v89, v18\n\t"
    "v_sub_f32_e32 v90, v90, v18\n\t"
    "v_sub_f32_e32 v91, v91, v18\n\t"
    "v_sub_f32_e32 v92, v92, v18\n\t"
    "v_sub_f32_e32 v93, v93, v18\n\t"
    "v_sub_f32_e32 v94, v94, v18\n\t"
    "v_sub_f32_e32 v95, v95, v18\n\t"
    "v_sub_f32_e32 v176, 0, v25\n\t"
    "v_sub_f32_e32 v177, 0, v25\n\t"
    "v_sub_f32_e32 v178, 0, v25\n\t"
    "v_sub_f32_e32 v179, 0, v25\n\t"
    "v_sub_f32_e32 v180, 0, v25\n\t"
    "v_sub_f32_e32 v181, 0, v25\n\t"
    "v_sub_f32_e32 v182, 0, v25\n\t"
    "v_sub_f32_e32 v183, 0, v25\n\t"
    "v_sub_f32_e32 v184, 0, v25\n\t"
    "v_sub_f32_e32 v185, 0, v25\n\t"
    "v_sub_f32_e32 v186, 0, v25\n\t"
    "v_sub_f32_e32 v187, 0, v25\n\t"
    "v_sub_f32_e32 v188, 0, v25\n\t"
    "v_sub_f32_e32 v189, 0, v25\n\t"
    "v_sub_f32_e32 v190, 0, v25\n\t"
    "v_sub_f32_e32 v191, 0, v25\n\t"
    "v_sub_f32_e32 v18, 0, v18\n\t"
    "v_exp_f32_e32 v18, v18\n\t"
    "s_nop 0\n\t"
    "v_mul_f32_e32 v21, v18, v21\n\t"
    "v_mul_f32_e32 v23, v18, v23\n\t"
    "s_mov_b32 s47, 1\n\t"
    "s_nop 1\n\t"
    "s_branch L_RET_l6\n\t"
    "L_RESC_l6:\n\t"
    "s_nop 7\n\t"
    "s_nop 7\n\t"
    "v_accvgpr_read_b32 v5, a0\n\t"
    "v_mul_f32_e32 v5, v17, v5\n\t"
    "v_accvgpr_write_b32 a0, v5\n\t"
    "v_accvgpr_read_b32 v6, a1\n\t"
    "v_mul_f32_e32 v6, v17, v6\n\t"
    "v_accvgpr_write_b32 a1, v6\n\t"
    "v_accvgpr_read_b32 v5, a2\n\t"
    "v_mul_f32_e32 v5, v17, v5\n\t"
    "v_accvgpr_write_b32 a2, v5\n\t"
    "v_accvgpr_read_b32 v6, a3\n\t"
    "v_mul_f32_e32 v6, v17, v6\n\t"
    "v_accvgpr_write_b32 a3, v6\n\t"
    "v_accvgpr_read_b32 v5, a4\n\t"
    "v_mul_f32_e32 v5, v17, v5\n\t"
    "v_accvgpr_write_b32 a4, v5\n\t"
    "v_accvgpr_read_b32 v6, a5\n\t"
    "v_mul_f32_e32 v6, v17, v6\n\t"
    "v_accvgpr_write_b32 a5, v6\n\t"
    "v_accvgpr_read_b32 v5, a6\n\t"
    "v_mul_f32_e32 v5, v17, v5\n\t"
    "v_accvgpr_write_b32 a6, v5\n\t"
    "v_accvgpr_read_b32 v6, a7\n\t"
    "v_mul_f32_e32 v6, v17, v6\n\t"
    "v_accvgpr_write_b32 a7, v6\n\t"
    "v_accvgpr_read_b32 v5, a8\n\t"
    "v_mul_f32_e32 v5, v17, v5\n\t"
    "v_accvgpr_write_b32 a8, v5\n\t"
    "v_accvgpr_read_b32 v6, a9\n\t"
    "v_mul_f32_e32 v6, v17, v6\n\t"
    "v_accvgpr_write_b32 a9, v6\n\t"
    "v_accvgpr_read_b32 v5, a10\n\t"
    "v_mul_f32_e32 v5, v17, v5\n\t"
    "v_accvgpr_write_b32 a10, v5\n\t"
    "v_accvgpr_read_b32 v6, a11\n\t"
    "v_mul_f32_e32 v6, v17, v6\n\t"
    "v_accvgpr_write_b32 a11, v6\n\t"
    "v_accvgpr_read_b32 v5, a12\n\t"
    "v_mul_f32_e32 v5, v17, v5\n\t"
    "v_accvgpr_write_b32 a12, v5\n\t"
    "v_accvgpr_read_b32 v6, a13\n\t"
    "v_mul_f32_e32 v6, v17, v6\n\t"
    "v_accvgpr_write_b32 a13, v6\n\t"
    "v_accvgpr_read_b32 v5, a14\n\t"
    "v_mul_f32_e32 v5, v17, v5\n\t"
    "v_accvgpr_write_b32 a14, v5\n\t"
    "v_accvgpr_read_b32 v6, a15\n\t"
    "v_mul_f32_e32 v6, v17, v6\n\t"
    "v_accvgpr_write_b32 a15, v6\n\t"
    "v_accvgpr_read_b32 v5, a16\n\t"
    "v_mul_f32_e32 v5, v18, v5\n\t"
    "v_accvgpr_write_b32 a16, v5\n\t"
    "v_accvgpr_read_b32 v6, a17\n\t"
    "v_mul_f32_e32 v6, v18, v6\n\t"
    "v_accvgpr_write_b32 a17, v6\n\t"
    "v_accvgpr_read_b32 v5, a18\n\t"
    "v_mul_f32_e32 v5, v18, v5\n\t"
    "v_accvgpr_write_b32 a18, v5\n\t"
    "v_accvgpr_read_b32 v6, a19\n\t"
    "v_mul_f32_e32 v6, v18, v6\n\t"
    "v_accvgpr_write_b32 a19, v6\n\t"
    "v_accvgpr_read_b32 v5, a20\n\t"
    "v_mul_f32_e32 v5, v18, v5\n\t"
    "v_accvgpr_write_b32 a20, v5\n\t"
    "v_accvgpr_read_b32 v6, a21\n\t"
    "v_mul_f32_e32 v6, v18, v6\n\t"
    "v_accvgpr_write_b32 a21, v6\n\t"
    "v_accvgpr_read_b32 v5, a22\n\t"
    "v_mul_f32_e32 v5, v18, v5\n\t"
    "v_accvgpr_write_b32 a22, v5\n\t"
    "v_accvgpr_read_b32 v6, a23\n\t"
    "v_mul_f32_e32 v6, v18, v6\n\t"
    "v_accvgpr_write_b32 a23, v6\n\t"
    "v_accvgpr_read_b32 v5, a24\n\t"
    "v_mul_f32_e32 v5, v18, v5\n\t"
    "v_accvgpr_write_b32 a24, v5\n\t"
    "v_accvgpr_read_b32 v6, a25\n\t"
    "v_mul_f32_e32 v6, v18, v6\n\t"
    "v_accvgpr_write_b32 a25, v6\n\t"
    "v_accvgpr_read_b32 v5, a26\n\t"
    "v_mul_f32_e32 v5, v18, v5\n\t"
    "v_accvgpr_write_b32 a26, v5\n\t"
    "v_accvgpr_read_b32 v6, a27\n\t"
    "v_mul_f32_e32 v6, v18, v6\n\t"
    "v_accvgpr_write_b32 a27, v6\n\t"
    "v_accvgpr_read_b32 v5, a28\n\t"
    "v_mul_f32_e32 v5, v18, v5\n\t"
    "v_accvgpr_write_b32 a28, v5\n\t"
    "v_accvgpr_read_b32 v6, a29\n\t"
    "v_mul_f32_e32 v6, v18, v6\n\t"
    "v_accvgpr_write_b32 a29, v6\n\t"
    "v_accvgpr_read_b32 v5, a30\n\t"
    "v_mul_f32_e32 v5, v18, v5\n\t"
    "v_accvgpr_write_b32 a30, v5\n\t"
    "v_accvgpr_read_b32 v6, a31\n\t"
    "v_mul_f32_e32 v6, v18, v6\n\t"
    "v_accvgpr_write_b32 a31, v6\n\t"
    "v_accvgpr_read_b32 v5, a32\n\t"
    "v_mul_f32_e32 v5, v17, v5\n\t"
    "v_accvgpr_write_b32 a32, v5\n\t"
    "v_accvgpr_read_b32 v6, a33\n\t"
    "v_mul_f32_e32 v6, v17, v6\n\t"
    "v_accvgpr_write_b32 a33, v6\n\t"
    "v_accvgpr_read_b32 v5, a34\n\t"
    "v_mul_f32_e32 v5, v17, v5\n\t"
    "v_accvgpr_write_b32 a34, v5\n\t"
    "v_accvgpr_read_b32 v6, a35\n\t"
    "v_mul_f32_e32 v6, v17, v6\n\t"
    "v_accvgpr_write_b32 a35, v6\n\t"
    "v_accvgpr_read_b32 v5, a36\n\t"
    "v_mul_f32_e32 v5, v17, v5\n\t"
    "v_accvgpr_write_b32 a36, v5\n\t"
    "v_accvgpr_read_b32 v6, a37\n\t"
    "v_mul_f32_e32 v6, v17, v6\n\t"
    "v_accvgpr_write_b32 a37, v6\n\t"
    "v_accvgpr_read_b32 v5, a38\n\t"
    "v_mul_f32_e32 v5, v17, v5\n\t"
    "v_accvgpr_write_b32 a38, v5\n\t"
    "v_accvgpr_read_b32 v6, a39\n\t"
    "v_mul_f32_e32 v6, v17, v6\n\t"
    "v_accvgpr_write_b32 a39, v6\n\t"
    "v_accvgpr_read_b32 v5, a40\n\t"
    "v_mul_f32_e32 v5, v17, v5\n\t"
    "v_accvgpr_write_b32 a40, v5\n\t"
    "v_accvgpr_read_b32 v6, a41\n\t"
    "v_mul_f32_e32 v6, v17, v6\n\t"
    "v_accvgpr_write_b32 a41, v6\n\t"
    "v_accvgpr_read_b32 v5, a42\n\t"
    "v_mul_f32_e32 v5, v17, v5\n\t"
    "v_accvgpr_write_b32 a42, v5\n\t"
    "v_accvgpr_read_b32 v6, a43\n\t"
    "v_mul_f32_e32 v6, v17, v6\n\t"
    "v_accvgpr_write_b32 a43, v6\n\t"
    "v_accvgpr_read_b32 v5, a44\n\t"
    "v_mul_f32_e32 v5, v17, v5\n\t"
    "v_accvgpr_write_b32 a44, v5\n\t"
    "v_accvgpr_read_b32 v6, a45\n\t"
    "v_mul_f32_e32 v6, v17, v6\n\t"
    "v_accvgpr_write_b32 a45, v6\n\t"
    "v_accvgpr_read_b32 v5, a46\n\t"
    "v_mul_f32_e32 v5, v17, v5\n\t"
    "v_accvgpr_write_b32 a46, v5\n\t"
    "v_accvgpr_read_b32 v6, a47\n\t"
    "v_mul_f32_e32 v6, v17, v6\n\t"
    "v_accvgpr_write_b32 a47, v6\n\t"
    "v_accvgpr_read_b32 v5, a48\n\t"
    "v_mul_f32_e32 v5, v18, v5\n\t"
    "v_accvgpr_write_b32 a48, v5\n\t"
    "v_accvgpr_read_b32 v6, a49\n\t"
    "v_mul_f32_e32 v6, v18, v6\n\t"
    "v_accvgpr_write_b32 a49, v6\n\t"
    "v_accvgpr_read_b32 v5, a50\n\t"
    "v_mul_f32_e32 v5, v18, v5\n\t"
    "v_accvgpr_write_b32 a50, v5\n\t"
    "v_accvgpr_read_b32 v6, a51\n\t"
    "v_mul_f32_e32 v6, v18, v6\n\t"
    "v_accvgpr_write_b32 a51, v6\n\t"
    "v_accvgpr_read_b32 v5, a52\n\t"
    "v_mul_f32_e32 v5, v18, v5\n\t"
    "v_accvgpr_write_b32 a52, v5\n\t"
    "v_accvgpr_read_b32 v6, a53\n\t"
    "v_mul_f32_e32 v6, v18, v6\n\t"
    "v_accvgpr_write_b32 a53, v6\n\t"
    "v_accvgpr_read_b32 v5, a54\n\t"
    "v_mul_f32_e32 v5, v18, v5\n\t"
    "v_accvgpr_write_b32 a54, v5\n\t"
    "v_accvgpr_read_b32 v6, a55\n\t"
    "v_mul_f32_e32 v6, v18, v6\n\t"
    "v_accvgpr_write_b32 a55, v6\n\t"
    "v_accvgpr_read_b32 v5, a56\n\t"
    "v_mul_f32_e32 v5, v18, v5\n\t"
    "v_accvgpr_write_b32 a56, v5\n\t"
    "v_accvgpr_read_b32 v6, a57\n\t"
    "v_mul_f32_e32 v6, v18, v6\n\t"
    "v_accvgpr_write_b32 a57, v6\n\t"
    "v_accvgpr_read_b32 v5, a58\n\t"
    "v_mul_f32_e32 v5, v18, v5\n\t"
    "v_accvgpr_write_b32 a58, v5\n\t"
    "v_accvgpr_read_b32 v6, a59\n\t"
    "v_mul_f32_e32 v6, v18, v6\n\t"
    "v_accvgpr_write_b32 a59, v6\n\t"
    "v_accvgpr_read_b32 v5, a60\n\t"
    "v_mul_f32_e32 v5, v18, v5\n\t"
    "v_accvgpr_write_b32 a60, v5\n\t"
    "v_accvgpr_read_b32 v6, a61\n\t"
    "v_mul_f32_e32 v6, v18, v6\n\t"
    "v_accvgpr_write_b32 a61, v6\n\t"
    "v_accvgpr_read_b32 v5, a62\n\t"
    "v_mul_f32_e32 v5, v18, v5\n\t"
    "v_accvgpr_write_b32 a62, v5\n\t"
    "v_accvgpr_read_b32 v6, a63\n\t"
    "v_mul_f32_e32 v6, v18, v6\n\t"
    "v_accvgpr_write_b32 a63, v6\n\t"
    "v_accvgpr_read_b32 v5, a64\n\t"
    "v_mul_f32_e32 v5, v17, v5\n\t"
    "v_accvgpr_write_b32 a64, v5\n\t"
    "v_accvgpr_read_b32 v6, a65\n\t"
    "v_mul_f32_e32 v6, v17, v6\n\t"
    "v_accvgpr_write_b32 a65, v6\n\t"
    "v_accvgpr_read_b32 v5, a66\n\t"
    "v_mul_f32_e32 v5, v17, v5\n\t"
    "v_accvgpr_write_b32 a66, v5\n\t"
    "v_accvgpr_read_b32 v6, a67\n\t"
    "v_mul_f32_e32 v6, v17, v6\n\t"
    "v_accvgpr_write_b32 a67, v6\n\t"
    "v_accvgpr_read_b32 v5, a68\n\t"
    "v_mul_f32_e32 v5, v17, v5\n\t"
    "v_accvgpr_write_b32 a68, v5\n\t"
    "v_accvgpr_read_b32 v6, a69\n\t"
    "v_mul_f32_e32 v6, v17, v6\n\t"
    "v_accvgpr_write_b32 a69, v6\n\t"
    "v_accvgpr_read_b32 v5, a70\n\t"
    "v_mul_f32_e32 v5, v17, v5\n\t"
    "v_accvgpr_write_b32 a70, v5\n\t"
    "v_accvgpr_read_b32 v6, a71\n\t"
    "v_mul_f32_e32 v6, v17, v6\n\t"
    "v_accvgpr_write_b32 a71, v6\n\t"
    "v_accvgpr_read_b32 v5, a72\n\t"
    "v_mul_f32_e32 v5, v17, v5\n\t"
    "v_accvgpr_write_b32 a72, v5\n\t"
    "v_accvgpr_read_b32 v6, a73\n\t"
    "v_mul_f32_e32 v6, v17, v6\n\t"
    "v_accvgpr_write_b32 a73, v6\n\t"
    "v_accvgpr_read_b32 v5, a74\n\t"
    "v_mul_f32_e32 v5, v17, v5\n\t"
    "v_accvgpr_write_b32 a74, v5\n\t"
    "v_accvgpr_read_b32 v6, a75\n\t"
    "v_mul_f32_e32 v6, v17, v6\n\t"
    "v_accvgpr_write_b32 a75, v6\n\t"
    "v_accvgpr_read_b32 v5, a76\n\t"
    "v_mul_f32_e32 v5, v17, v5\n\t"
    "v_accvgpr_write_b32 a76, v5\n\t"
    "v_accvgpr_read_b32 v6, a77\n\t"
    "v_mul_f32_e32 v6, v17, v6\n\t"
    "v_accvgpr_write_b32 a77, v6\n\t"
    "v_accvgpr_read_b32 v5, a78\n\t"
    "v_mul_f32_e32 v5, v17, v5\n\t"
    "v_accvgpr_write_b32 a78, v5\n\t"
    "v_accvgpr_read_b32 v6, a79\n\t"
    "v_mul_f32_e32 v6, v17, v6\n\t"
    "v_accvgpr_write_b32 a79, v6\n\t"
    "v_accvgpr_read_b32 v5, a80\n\t"
    "v_mul_f32_e32 v5, v18, v5\n\t"
    "v_accvgpr_write_b32 a80, v5\n\t"
    "v_accvgpr_read_b32 v6, a81\n\t"
    "v_mul_f32_e32 v6, v18, v6\n\t"
    "v_accvgpr_write_b32 a81, v6\n\t"
    "v_accvgpr_read_b32 v5, a82\n\t"
    "v_mul_f32_e32 v5, v18, v5\n\t"
    "v_accvgpr_write_b32 a82, v5\n\t"
    "v_accvgpr_read_b32 v6, a83\n\t"
    "v_mul_f32_e32 v6, v18, v6\n\t"
    "v_accvgpr_write_b32 a83, v6\n\t"
    "v_accvgpr_read_b32 v5, a84\n\t"
    "v_mul_f32_e32 v5, v18, v5\n\t"
    "v_accvgpr_write_b32 a84, v5\n\t"
    "v_accvgpr_read_b32 v6, a85\n\t"
    "v_mul_f32_e32 v6, v18, v6\n\t"
    "v_accvgpr_write_b32 a85, v6\n\t"
    "v_accvgpr_read_b32 v5, a86\n\t"
    "v_mul_f32_e32 v5, v18, v5\n\t"
    "v_accvgpr_write_b32 a86, v5\n\t"
    "v_accvgpr_read_b32 v6, a87\n\t"
    "v_mul_f32_e32 v6, v18, v6\n\t"
    "v_accvgpr_write_b32 a87, v6\n\t"
    "v_accvgpr_read_b32 v5, a88\n\t"
    "v_mul_f32_e32 v5, v18, v5\n\t"
    "v_accvgpr_write_b32 a88, v5\n\t"
    "v_accvgpr_read_b32 v6, a89\n\t"
    "v_mul_f32_e32 v6, v18, v6\n\t"
    "v_accvgpr_write_b32 a89, v6\n\t"
    "v_accvgpr_read_b32 v5, a90\n\t"
    "v_mul_f32_e32 v5, v18, v5\n\t"
    "v_accvgpr_write_b32 a90, v5\n\t"
    "v_accvgpr_read_b32 v6, a91\n\t"
    "v_mul_f32_e32 v6, v18, v6\n\t"
    "v_accvgpr_write_b32 a91, v6\n\t"
    "v_accvgpr_read_b32 v5, a92\n\t"
    "v_mul_f32_e32 v5, v18, v5\n\t"
    "v_accvgpr_write_b32 a92, v5\n\t"
    "v_accvgpr_read_b32 v6, a93\n\t"
    "v_mul_f32_e32 v6, v18, v6\n\t"
    "v_accvgpr_write_b32 a93, v6\n\t"
    "v_accvgpr_read_b32 v5, a94\n\t"
    "v_mul_f32_e32 v5, v18, v5\n\t"
    "v_accvgpr_write_b32 a94, v5\n\t"
    "v_accvgpr_read_b32 v6, a95\n\t"
    "v_mul_f32_e32 v6, v18, v6\n\t"
    "v_accvgpr_write_b32 a95, v6\n\t"
    "v_accvgpr_read_b32 v5, a96\n\t"
    "v_mul_f32_e32 v5, v17, v5\n\t"
    "v_accvgpr_write_b32 a96, v5\n\t"
    "v_accvgpr_read_b32 v6, a97\n\t"
    "v_mul_f32_e32 v6, v17, v6\n\t"
    "v_accvgpr_write_b32 a97, v6\n\t"
    "v_accvgpr_read_b32 v5, a98\n\t"
    "v_mul_f32_e32 v5, v17, v5\n\t"
    "v_accvgpr_write_b32 a98, v5\n\t"
    "v_accvgpr_read_b32 v6, a99\n\t"
    "v_mul_f32_e32 v6, v17, v6\n\t"
    "v_accvgpr_write_b32 a99, v6\n\t"
    "v_accvgpr_read_b32 v5, a100\n\t"
    "v_mul_f32_e32 v5, v17, v5\n\t"
    "v_accvgpr_write_b32 a100, v5\n\t"
    "v_accvgpr_read_b32 v6, a101\n\t"
    "v_mul_f32_e32 v6, v17, v6\n\t"
    "v_accvgpr_write_b32 a101, v6\n\t"
    "v_accvgpr_read_b32 v5, a102\n\t"
    "v_mul_f32_e32 v5, v17, v5\n\t"
    "v_accvgpr_write_b32 a102, v5\n\t"
    "v_accvgpr_read_b32 v6, a103\n\t"
    "v_mul_f32_e32 v6, v17, v6\n\t"
    "v_accvgpr_write_b32 a103, v6\n\t"
    "v_accvgpr_read_b32 v5, a104\n\t"
    "v_mul_f32_e32 v5, v17, v5\n\t"
    "v_accvgpr_write_b32 a104, v5\n\t"
    "v_accvgpr_read_b32 v6, a105\n\t"
    "v_mul_f32_e32 v6, v17, v6\n\t"
    "v_accvgpr_write_b32 a105, v6\n\t"
    "v_accvgpr_read_b32 v5, a106\n\t"
    "v_mul_f32_e32 v5, v17, v5\n\t"
    "v_accvgpr_write_b32 a106, v5\n\t"
    "v_accvgpr_read_b32 v6, a107\n\t"
    "v_mul_f32_e32 v6, v17, v6\n\t"
    "v_accvgpr_write_b32 a107, v6\n\t"
    "v_accvgpr_read_b32 v5, a108\n\t"
    "v_mul_f32_e32 v5, v17, v5\n\t"
    "v_accvgpr_write_b32 a108, v5\n\t"
    "v_accvgpr_read_b32 v6, a109\n\t"
    "v_mul_f32_e32 v6, v17, v6\n\t"
    "v_accvgpr_write_b32 a109, v6\n\t"
    "v_accvgpr_read_b32 v5, a110\n\t"
    "v_mul_f32_e32 v5, v17, v5\n\t"
    "v_accvgpr_write_b32 a110, v5\n\t"
    "v_accvgpr_read_b32 v6, a111\n\t"
    "v_mul_f32_e32 v6, v17, v6\n\t"
    "v_accvgpr_write_b32 a111, v6\n\t"
    "v_accvgpr_read_b32 v5, a112\n\t"
    "v_mul_f32_e32 v5, v18, v5\n\t"
    "v_accvgpr_write_b32 a112, v5\n\t"
    "v_accvgpr_read_b32 v6, a113\n\t"
    "v_mul_f32_e32 v6, v18, v6\n\t"
    "v_accvgpr_write_b32 a113, v6\n\t"
    "v_accvgpr_read_b32 v5, a114\n\t"
    "v_mul_f32_e32 v5, v18, v5\n\t"
    "v_accvgpr_write_b32 a114, v5\n\t"
    "v_accvgpr_read_b32 v6, a115\n\t"
    "v_mul_f32_e32 v6, v18, v6\n\t"
    "v_accvgpr_write_b32 a115, v6\n\t"
    "v_accvgpr_read_b32 v5, a116\n\t"
    "v_mul_f32_e32 v5, v18, v5\n\t"
    "v_accvgpr_write_b32 a116, v5\n\t"
    "v_accvgpr_read_b32 v6, a117\n\t"
    "v_mul_f32_e32 v6, v18, v6\n\t"
    "v_accvgpr_write_b32 a117, v6\n\t"
    "v_accvgpr_read_b32 v5, a118\n\t"
    "v_mul_f32_e32 v5, v18, v5\n\t"
    "v_accvgpr_write_b32 a118, v5\n\t"
    "v_accvgpr_read_b32 v6, a119\n\t"
    "v_mul_f32_e32 v6, v18, v6\n\t"
    "v_accvgpr_write_b32 a119, v6\n\t"
    "v_accvgpr_read_b32 v5, a120\n\t"
    "v_mul_f32_e32 v5, v18, v5\n\t"
    "v_accvgpr_write_b32 a120, v5\n\t"
    "v_accvgpr_read_b32 v6, a121\n\t"
    "v_mul_f32_e32 v6, v18, v6\n\t"
    "v_accvgpr_write_b32 a121, v6\n\t"
    "v_accvgpr_read_b32 v5, a122\n\t"
    "v_mul_f32_e32 v5, v18, v5\n\t"
    "v_accvgpr_write_b32 a122, v5\n\t"
    "v_accvgpr_read_b32 v6, a123\n\t"
    "v_mul_f32_e32 v6, v18, v6\n\t"
    "v_accvgpr_write_b32 a123, v6\n\t"
    "v_accvgpr_read_b32 v5, a124\n\t"
    "v_mul_f32_e32 v5, v18, v5\n\t"
    "v_accvgpr_write_b32 a124, v5\n\t"
    "v_accvgpr_read_b32 v6, a125\n\t"
    "v_mul_f32_e32 v6, v18, v6\n\t"
    "v_accvgpr_write_b32 a125, v6\n\t"
    "v_accvgpr_read_b32 v5, a126\n\t"
    "v_mul_f32_e32 v5, v18, v5\n\t"
    "v_accvgpr_write_b32 a126, v5\n\t"
    "v_accvgpr_read_b32 v6, a127\n\t"
    "v_mul_f32_e32 v6, v18, v6\n\t"
    "v_accvgpr_write_b32 a127, v6\n\t"
    "s_mov_b32 s47, 0\n\t"
    "s_nop 1\n\t"
    "s_branch L_RRET_l6\n\t"
    "L_RARE_l7:\n\t"
    "s_nop 1\n\t"
    "v_max_f32_e32 v17, 0, v30\n\t"
    "v_add_f32_e32 v24, v24, v17\n\t"
    "v_sub_f32_e32 v96, v96, v17\n\t"
    "v_sub_f32_e32 v97, v97, v17\n\t"
    "v_sub_f32_e32 v98, v98, v17\n\t"
    "v_sub_f32_e32 v99, v99, v17\n\t"
    "v_sub_f32_e32 v100, v100, v17\n\t"
    "v_sub_f32_e32 v101, v101, v17\n\t"
    "v_sub_f32_e32 v102, v102, v17\n\t"
    "v_sub_f32_e32 v103, v103, v17\n\t"
    "v_sub_f32_e32 v104, v104, v17\n\t"
    "v_sub_f32_e32 v105, v105, v17\n\t"
    "v_sub_f32_e32 v106, v106, v17\n\t"
    "v_sub_f32_e32 v107, v107, v17\n\t"
    "v_sub_f32_e32 v108, v108, v17\n\t"
    "v_sub_f32_e32 v109, v109, v17\n\t"
    "v_sub_f32_e32 v110, v110, v17\n\t"
    "v_sub_f32_e32 v111, v111, v17\n\t"
    "v_sub_f32_e32 v128, v128, v17\n\t"
    "v_sub_f32_e32 v129, v129, v17\n\t"
    "v_sub_f32_e32 v130, v130, v17\n\t"
    "v_sub_f32_e32 v131, v131, v17\n\t"
    "v_sub_f32_e32 v132, v132, v17\n\t"
    "v_sub_f32_e32 v133, v133, v17\n\t"
    "v_sub_f32_e32 v134, v134, v17\n\t"
    "v_sub_f32_e32 v135, v135, v17\n\t"
    "v_sub_f32_e32 v136, v136, v17\n\t"
    "v_sub_f32_e32 v137, v137, v17\n\t"
    "v_sub_f32_e32 v138, v138, v17\n\t"
    "v_sub_f32_e32 v139, v139, v17\n\t"
    "v_sub_f32_e32 v140, v140, v17\n\t"
    "v_sub_f32_e32 v141, v141, v17\n\t"
    "v_sub_f32_e32 v142, v142, v17\n\t"
    "v_sub_f32_e32 v143, v143, v17\n\t"
    "v_sub_f32_e32 v160, 0, v24\n\t"
    "v_sub_f32_e32 v161, 0, v24\n\t"
    "v_sub_f32_e32 v162, 0, v24\n\t"
    "v_sub_f32_e32 v163, 0, v24\n\t"
    "v_sub_f32_e32 v164, 0, v24\n\t"
    "v_sub_f32_e32 v165, 0, v24\n\t"
    "v_sub_f32_e32 v166, 0, v24\n\t"
    "v_sub_f32_e32 v167, 0, v24\n\t"
    "v_sub_f32_e32 v168, 0, v24\n\t"
    "v_sub_f32_e32 v169, 0, v24\n\t"
    "v_sub_f32_e32 v170, 0, v24\n\t"
    "v_sub_f32_e32 v171, 0, v24\n\t"
    "v_sub_f32_e32 v172, 0, v24\n\t"
    "v_sub_f32_e32 v173, 0, v24\n\t"
    "v_sub_f32_e32 v174, 0, v24\n\t"
    "v_sub_f32_e32 v175, 0, v24\n\t"
    "v_sub_f32_e32 v17, 0, v17\n\t"
    "v_exp_f32_e32 v17, v17\n\t"
    "s_nop 0\n\t"
    "v_mul_f32_e32 v20, v17, v20\n\t"
    "v_mul_f32_e32 v22, v17, v22\n\t"
    "v_max_f32_e32 v18, 0, v31\n\t"
    "v_add_f32_e32 v25, v25, v18\n\t"
    "v_sub_f32_e32 v112, v112, v18\n\t"
    "v_sub_f32_e32 v113, v113, v18\n\t"
    "v_sub_f32_e32 v114, v114, v18\n\t"
    "v_sub_f32_e32 v115, v115, v18\n\t"
    "v_sub_f32_e32 v116, v116, v18\n\t"
    "v_sub_f32_e32 v117, v117, v18\n\t"
    "v_sub_f32_e32 v118, v118, v18\n\t"
    "v_sub_f32_e32 v119, v119, v18\n\t"
    "v_sub_f32_e32 v120, v120, v18\n\t"
    "v_sub_f32_e32 v121, v121, v18\n\t"
    "v_sub_f32_e32 v122, v122, v18\n\t"
    "v_sub_f32_e32 v123, v123, v18\n\t"
    "v_sub_f32_e32 v124, v124, v18\n\t"
    "v_sub_f32_e32 v125, v125, v18\n\t"
    "v_sub_f32_e32 v126, v126, v18\n\t"
    "v_sub_f32_e32 v127, v127, v18\n\t"
    "v_sub_f32_e32 v144, v144, v18\n\t"
    "v_sub_f32_e32 v145, v145, v18\n\t"
    "v_sub_f32_e32 v146, v146, v18\n\t"
    "v_sub_f32_e32 v147, v147, v18\n\t"
    "v_sub_f32_e32 v148, v148, v18\n\t"
    "v_sub_f32_e32 v149, v149, v18\n\t"
    "v_sub_f32_e32 v150, v150, v18\n\t"
    "v_sub_f32_e32 v151, v151, v18\n\t"
    "v_sub_f32_e32 v152, v152, v18\n\t"
    "v_sub_f32_e32 v153, v153, v18\n\t"
    "v_sub_f32_e32 v154, v154, v18\n\t"
    "v_sub_f32_e32 v155, v155, v18\n\t"
    "v_sub_f32_e32 v156, v156, v18\n\t"
    "v_sub_f32_e32 v157, v157, v18\n\t"
    "v_sub_f32_e32 v158, v158, v18\n\t"
    "v_sub_f32_e32 v159, v159, v18\n\t"
    "v_sub_f32_e32 v176, 0, v25\n\t"
    "v_sub_f32_e32 v177, 0, v25\n\t"
    "v_sub_f32_e32 v178, 0, v25\n\t"
    "v_sub_f32_e32 v179, 0, v25\n\t"
    "v_sub_f32_e32 v180, 0, v25\n\t"
    "v_sub_f32_e32 v181, 0, v25\n\t"
    "v_sub_f32_e32 v182, 0, v25\n\t"
    "v_sub_f32_e32 v183, 0, v25\n\t"
    "v_sub_f32_e32 v184, 0, v25\n\t"
    "v_sub_f32_e32 v185, 0, v25\n\t"
    "v_sub_f32_e32 v186, 0, v25\n\t"
    "v_sub_f32_e32 v187, 0, v25\n\t"
    "v_sub_f32_e32 v188, 0, v25\n\t"
    "v_sub_f32_e32 v189, 0, v25\n\t"
    "v_sub_f32_e32 v190, 0, v25\n\t"
    "v_sub_f32_e32 v191, 0, v25\n\t"
    "v_sub_f32_e32 v18, 0, v18\n\t"
    "v_exp_f32_e32 v18, v18\n\t"
    "s_nop 0\n\t"
    "v_mul_f32_e32 v21, v18, v21\n\t"
    "v_mul_f32_e32 v23, v18, v23\n\t"
    "s_mov_b32 s47, 1\n\t"
    "s_nop 1\n\t"
    "s_branch L_RET_l7\n\t"
    "L_RESC_l7:\n\t"
    "s_nop 7\n\t"
    "s_nop 7\n\t"
    "v_accvgpr_read_b32 v5, a0\n\t"
    "v_mul_f32_e32 v5, v17, v5\n\t"
    "v_accvgpr_write_b32 a0, v5\n\t"
    "v_accvgpr_read_b32 v6, a1\n\t"
    "v_mul_f32_e32 v6, v17, v6\n\t"
    "v_accvgpr_write_b32 a1, v6\n\t"
    "v_accvgpr_read_b32 v5, a2\n\t"
    "v_mul_f32_e32 v5, v17, v5\n\t"
    "v_accvgpr_write_b32 a2, v5\n\t"
    "v_accvgpr_read_b32 v6, a3\n\t"
    "v_mul_f32_e32 v6, v17, v6\n\t"
    "v_accvgpr_write_b32 a3, v6\n\t"
    "v_accvgpr_read_b32 v5, a4\n\t"
    "v_mul_f32_e32 v5, v17, v5\n\t"
    "v_accvgpr_write_b32 a4, v5\n\t"
    "v_accvgpr_read_b32 v6, a5\n\t"
    "v_mul_f32_e32 v6, v17, v6\n\t"
    "v_accvgpr_write_b32 a5, v6\n\t"
    "v_accvgpr_read_b32 v5, a6\n\t"
    "v_mul_f32_e32 v5, v17, v5\n\t"
    "v_accvgpr_write_b32 a6, v5\n\t"
    "v_accvgpr_read_b32 v6, a7\n\t"
    "v_mul_f32_e32 v6, v17, v6\n\t"
    "v_accvgpr_write_b32 a7, v6\n\t"
    "v_accvgpr_read_b32 v5, a8\n\t"
    "v_mul_f32_e32 v5, v17, v5\n\t"
    "v_accvgpr_write_b32 a8, v5\n\t"
    "v_accvgpr_read_b32 v6, a9\n\t"
    "v_mul_f32_e32 v6, v17, v6\n\t"
    "v_accvgpr_write_b32 a9, v6\n\t"
    "v_accvgpr_read_b32 v5, a10\n\t"
    "v_mul_f32_e32 v5, v17, v5\n\t"
    "v_accvgpr_write_b32 a10, v5\n\t"
    "v_accvgpr_read_b32 v6, a11\n\t"
    "v_mul_f32_e32 v6, v17, v6\n\t"
    "v_accvgpr_write_b32 a11, v6\n\t"
    "v_accvgpr_read_b32 v5, a12\n\t"
    "v_mul_f32_e32 v5, v17, v5\n\t"
    "v_accvgpr_write_b32 a12, v5\n\t"
    "v_accvgpr_read_b32 v6, a13\n\t"
    "v_mul_f32_e32 v6, v17, v6\n\t"
    "v_accvgpr_write_b32 a13, v6\n\t"
    "v_accvgpr_read_b32 v5, a14\n\t"
    "v_mul_f32_e32 v5, v17, v5\n\t"
    "v_accvgpr_write_b32 a14, v5\n\t"
    "v_accvgpr_read_b32 v6, a15\n\t"
    "v_mul_f32_e32 v6, v17, v6\n\t"
    "v_accvgpr_write_b32 a15, v6\n\t"
    "v_accvgpr_read_b32 v5, a16\n\t"
    "v_mul_f32_e32 v5, v18, v5\n\t"
    "v_accvgpr_write_b32 a16, v5\n\t"
    "v_accvgpr_read_b32 v6, a17\n\t"
    "v_mul_f32_e32 v6, v18, v6\n\t"
    "v_accvgpr_write_b32 a17, v6\n\t"
    "v_accvgpr_read_b32 v5, a18\n\t"
    "v_mul_f32_e32 v5, v18, v5\n\t"
    "v_accvgpr_write_b32 a18, v5\n\t"
    "v_accvgpr_read_b32 v6, a19\n\t"
    "v_mul_f32_e32 v6, v18, v6\n\t"
    "v_accvgpr_write_b32 a19, v6\n\t"
    "v_accvgpr_read_b32 v5, a20\n\t"
    "v_mul_f32_e32 v5, v18, v5\n\t"
    "v_accvgpr_write_b32 a20, v5\n\t"
    "v_accvgpr_read_b32 v6, a21\n\t"
    "v_mul_f32_e32 v6, v18, v6\n\t"
    "v_accvgpr_write_b32 a21, v6\n\t"
    "v_accvgpr_read_b32 v5, a22\n\t"
    "v_mul_f32_e32 v5, v18, v5\n\t"
    "v_accvgpr_write_b32 a22, v5\n\t"
    "v_accvgpr_read_b32 v6, a23\n\t"
    "v_mul_f32_e32 v6, v18, v6\n\t"
    "v_accvgpr_write_b32 a23, v6\n\t"
    "v_accvgpr_read_b32 v5, a24\n\t"
    "v_mul_f32_e32 v5, v18, v5\n\t"
    "v_accvgpr_write_b32 a24, v5\n\t"
    "v_accvgpr_read_b32 v6, a25\n\t"
    "v_mul_f32_e32 v6, v18, v6\n\t"
    "v_accvgpr_write_b32 a25, v6\n\t"
    "v_accvgpr_read_b32 v5, a26\n\t"
    "v_mul_f32_e32 v5, v18, v5\n\t"
    "v_accvgpr_write_b32 a26, v5\n\t"
    "v_accvgpr_read_b32 v6, a27\n\t"
    "v_mul_f32_e32 v6, v18, v6\n\t"
    "v_accvgpr_write_b32 a27, v6\n\t"
    "v_accvgpr_read_b32 v5, a28\n\t"
    "v_mul_f32_e32 v5, v18, v5\n\t"
    "v_accvgpr_write_b32 a28, v5\n\t"
    "v_accvgpr_read_b32 v6, a29\n\t"
    "v_mul_f32_e32 v6, v18, v6\n\t"
    "v_accvgpr_write_b32 a29, v6\n\t"
    "v_accvgpr_read_b32 v5, a30\n\t"
    "v_mul_f32_e32 v5, v18, v5\n\t"
    "v_accvgpr_write_b32 a30, v5\n\t"
    "v_accvgpr_read_b32 v6, a31\n\t"
    "v_mul_f32_e32 v6, v18, v6\n\t"
    "v_accvgpr_write_b32 a31, v6\n\t"
    "v_accvgpr_read_b32 v5, a32\n\t"
    "v_mul_f32_e32 v5, v17, v5\n\t"
    "v_accvgpr_write_b32 a32, v5\n\t"
    "v_accvgpr_read_b32 v6, a33\n\t"
    "v_mul_f32_e32 v6, v17, v6\n\t"
    "v_accvgpr_write_b32 a33, v6\n\t"
    "v_accvgpr_read_b32 v5, a34\n\t"
    "v_mul_f32_e32 v5, v17, v5\n\t"
    "v_accvgpr_write_b32 a34, v5\n\t"
    "v_accvgpr_read_b32 v6, a35\n\t"
    "v_mul_f32_e32 v6, v17, v6\n\t"
    "v_accvgpr_write_b32 a35, v6\n\t"
    "v_accvgpr_read_b32 v5, a36\n\t"
    "v_mul_f32_e32 v5, v17, v5\n\t"
    "v_accvgpr_write_b32 a36, v5\n\t"
    "v_accvgpr_read_b32 v6, a37\n\t"
    "v_mul_f32_e32 v6, v17, v6\n\t"
    "v_accvgpr_write_b32 a37, v6\n\t"
    "v_accvgpr_read_b32 v5, a38\n\t"
    "v_mul_f32_e32 v5, v17, v5\n\t"
    "v_accvgpr_write_b32 a38, v5\n\t"
    "v_accvgpr_read_b32 v6, a39\n\t"
    "v_mul_f32_e32 v6, v17, v6\n\t"
    "v_accvgpr_write_b32 a39, v6\n\t"
    "v_accvgpr_read_b32 v5, a40\n\t"
    "v_mul_f32_e32 v5, v17, v5\n\t"
    "v_accvgpr_write_b32 a40, v5\n\t"
    "v_accvgpr_read_b32 v6, a41\n\t"
    "v_mul_f32_e32 v6, v17, v6\n\t"
    "v_accvgpr_write_b32 a41, v6\n\t"
    "v_accvgpr_read_b32 v5, a42\n\t"
    "v_mul_f32_e32 v5, v17, v5\n\t"
    "v_accvgpr_write_b32 a42, v5\n\t"
    "v_accvgpr_read_b32 v6, a43\n\t"
    "v_mul_f32_e32 v6, v17, v6\n\t"
    "v_accvgpr_write_b32 a43, v6\n\t"
    "v_accvgpr_read_b32 v5, a44\n\t"
    "v_mul_f32_e32 v5, v17, v5\n\t"
    "v_accvgpr_write_b32 a44, v5\n\t"
    "v_accvgpr_read_b32 v6, a45\n\t"
    "v_mul_f32_e32 v6, v17, v6\n\t"
    "v_accvgpr_write_b32 a45, v6\n\t"
    "v_accvgpr_read_b32 v5, a46\n\t"
    "v_mul_f32_e32 v5, v17, v5\n\t"
    "v_accvgpr_write_b32 a46, v5\n\t"
    "v_accvgpr_read_b32 v6, a47\n\t"
    "v_mul_f32_e32 v6, v17, v6\n\t"
    "v_accvgpr_write_b32 a47, v6\n\t"
    "v_accvgpr_read_b32 v5, a48\n\t"
    "v_mul_f32_e32 v5, v18, v5\n\t"
    "v_accvgpr_write_b32 a48, v5\n\t"
    "v_accvgpr_read_b32 v6, a49\n\t"
    "v_mul_f32_e32 v6, v18, v6\n\t"
    "v_accvgpr_write_b32 a49, v6\n\t"
    "v_accvgpr_read_b32 v5, a50\n\t"
    "v_mul_f32_e32 v5, v18, v5\n\t"
    "v_accvgpr_write_b32 a50, v5\n\t"
    "v_accvgpr_read_b32 v6, a51\n\t"
    "v_mul_f32_e32 v6, v18, v6\n\t"
    "v_accvgpr_write_b32 a51, v6\n\t"
    "v_accvgpr_read_b32 v5, a52\n\t"
    "v_mul_f32_e32 v5, v18, v5\n\t"
    "v_accvgpr_write_b32 a52, v5\n\t"
    "v_accvgpr_read_b32 v6, a53\n\t"
    "v_mul_f32_e32 v6, v18, v6\n\t"
    "v_accvgpr_write_b32 a53, v6\n\t"
    "v_accvgpr_read_b32 v5, a54\n\t"
    "v_mul_f32_e32 v5, v18, v5\n\t"
    "v_accvgpr_write_b32 a54, v5\n\t"
    "v_accvgpr_read_b32 v6, a55\n\t"
    "v_mul_f32_e32 v6, v18, v6\n\t"
    "v_accvgpr_write_b32 a55, v6\n\t"
    "v_accvgpr_read_b32 v5, a56\n\t"
    "v_mul_f32_e32 v5, v18, v5\n\t"
    "v_accvgpr_write_b32 a56, v5\n\t"
    "v_accvgpr_read_b32 v6, a57\n\t"
    "v_mul_f32_e32 v6, v18, v6\n\t"
    "v_accvgpr_write_b32 a57, v6\n\t"
    "v_accvgpr_read_b32 v5, a58\n\t"
    "v_mul_f32_e32 v5, v18, v5\n\t"
    "v_accvgpr_write_b32 a58, v5\n\t"
    "v_accvgpr_read_b32 v6, a59\n\t"
    "v_mul_f32_e32 v6, v18, v6\n\t"
    "v_accvgpr_write_b32 a59, v6\n\t"
    "v_accvgpr_read_b32 v5, a60\n\t"
    "v_mul_f32_e32 v5, v18, v5\n\t"
    "v_accvgpr_write_b32 a60, v5\n\t"
    "v_accvgpr_read_b32 v6, a61\n\t"
    "v_mul_f32_e32 v6, v18, v6\n\t"
    "v_accvgpr_write_b32 a61, v6\n\t"
    "v_accvgpr_read_b32 v5, a62\n\t"
    "v_mul_f32_e32 v5, v18, v5\n\t"
    "v_accvgpr_write_b32 a62, v5\n\t"
    "v_accvgpr_read_b32 v6, a63\n\t"
    "v_mul_f32_e32 v6, v18, v6\n\t"
    "v_accvgpr_write_b32 a63, v6\n\t"
    "v_accvgpr_read_b32 v5, a64\n\t"
    "v_mul_f32_e32 v5, v17, v5\n\t"
    "v_accvgpr_write_b32 a64, v5\n\t"
    "v_accvgpr_read_b32 v6, a65\n\t"
    "v_mul_f32_e32 v6, v17, v6\n\t"
    "v_accvgpr_write_b32 a65, v6\n\t"
    "v_accvgpr_read_b32 v5, a66\n\t"
    "v_mul_f32_e32 v5, v17, v5\n\t"
    "v_accvgpr_write_b32 a66, v5\n\t"
    "v_accvgpr_read_b32 v6, a67\n\t"
    "v_mul_f32_e32 v6, v17, v6\n\t"
    "v_accvgpr_write_b32 a67, v6\n\t"
    "v_accvgpr_read_b32 v5, a68\n\t"
    "v_mul_f32_e32 v5, v17, v5\n\t"
    "v_accvgpr_write_b32 a68, v5\n\t"
    "v_accvgpr_read_b32 v6, a69\n\t"
    "v_mul_f32_e32 v6, v17, v6\n\t"
    "v_accvgpr_write_b32 a69, v6\n\t"
    "v_accvgpr_read_b32 v5, a70\n\t"
    "v_mul_f32_e32 v5, v17, v5\n\t"
    "v_accvgpr_write_b32 a70, v5\n\t"
    "v_accvgpr_read_b32 v6, a71\n\t"
    "v_mul_f32_e32 v6, v17, v6\n\t"
    "v_accvgpr_write_b32 a71, v6\n\t"
    "v_accvgpr_read_b32 v5, a72\n\t"
    "v_mul_f32_e32 v5, v17, v5\n\t"
    "v_accvgpr_write_b32 a72, v5\n\t"
    "v_accvgpr_read_b32 v6, a73\n\t"
    "v_mul_f32_e32 v6, v17, v6\n\t"
    "v_accvgpr_write_b32 a73, v6\n\t"
    "v_accvgpr_read_b32 v5, a74\n\t"
    "v_mul_f32_e32 v5, v17, v5\n\t"
    "v_accvgpr_write_b32 a74, v5\n\t"
    "v_accvgpr_read_b32 v6, a75\n\t"
    "v_mul_f32_e32 v6, v17, v6\n\t"
    "v_accvgpr_write_b32 a75, v6\n\t"
    "v_accvgpr_read_b32 v5, a76\n\t"
    "v_mul_f32_e32 v5, v17, v5\n\t"
    "v_accvgpr_write_b32 a76, v5\n\t"
    "v_accvgpr_read_b32 v6, a77\n\t"
    "v_mul_f32_e32 v6, v17, v6\n\t"
    "v_accvgpr_write_b32 a77, v6\n\t"
    "v_accvgpr_read_b32 v5, a78\n\t"
    "v_mul_f32_e32 v5, v17, v5\n\t"
    "v_accvgpr_write_b32 a78, v5\n\t"
    "v_accvgpr_read_b32 v6, a79\n\t"
    "v_mul_f32_e32 v6, v17, v6\n\t"
    "v_accvgpr_write_b32 a79, v6\n\t"
    "v_accvgpr_read_b32 v5, a80\n\t"
    "v_mul_f32_e32 v5, v18, v5\n\t"
    "v_accvgpr_write_b32 a80, v5\n\t"
    "v_accvgpr_read_b32 v6, a81\n\t"
    "v_mul_f32_e32 v6, v18, v6\n\t"
    "v_accvgpr_write_b32 a81, v6\n\t"
    "v_accvgpr_read_b32 v5, a82\n\t"
    "v_mul_f32_e32 v5, v18, v5\n\t"
    "v_accvgpr_write_b32 a82, v5\n\t"
    "v_accvgpr_read_b32 v6, a83\n\t"
    "v_mul_f32_e32 v6, v18, v6\n\t"
    "v_accvgpr_write_b32 a83, v6\n\t"
    "v_accvgpr_read_b32 v5, a84\n\t"
    "v_mul_f32_e32 v5, v18, v5\n\t"
    "v_accvgpr_write_b32 a84, v5\n\t"
    "v_accvgpr_read_b32 v6, a85\n\t"
    "v_mul_f32_e32 v6, v18, v6\n\t"
    "v_accvgpr_write_b32 a85, v6\n\t"
    "v_accvgpr_read_b32 v5, a86\n\t"
    "v_mul_f32_e32 v5, v18, v5\n\t"
    "v_accvgpr_write_b32 a86, v5\n\t"
    "v_accvgpr_read_b32 v6, a87\n\t"
    "v_mul_f32_e32 v6, v18, v6\n\t"
    "v_accvgpr_write_b32 a87, v6\n\t"
    "v_accvgpr_read_b32 v5, a88\n\t"
    "v_mul_f32_e32 v5, v18, v5\n\t"
    "v_accvgpr_write_b32 a88, v5\n\t"
    "v_accvgpr_read_b32 v6, a89\n\t"
    "v_mul_f32_e32 v6, v18, v6\n\t"
    "v_accvgpr_write_b32 a89, v6\n\t"
    "v_accvgpr_read_b32 v5, a90\n\t"
    "v_mul_f32_e32 v5, v18, v5\n\t"
    "v_accvgpr_write_b32 a90, v5\n\t"
    "v_accvgpr_read_b32 v6, a91\n\t"
    "v_mul_f32_e32 v6, v18, v6\n\t"
    "v_accvgpr_write_b32 a91, v6\n\t"
    "v_accvgpr_read_b32 v5, a92\n\t"
    "v_mul_f32_e32 v5, v18, v5\n\t"
    "v_accvgpr_write_b32 a92, v5\n\t"
    "v_accvgpr_read_b32 v6, a93\n\t"
    "v_mul_f32_e32 v6, v18, v6\n\t"
    "v_accvgpr_write_b32 a93, v6\n\t"
    "v_accvgpr_read_b32 v5, a94\n\t"
    "v_mul_f32_e32 v5, v18, v5\n\t"
    "v_accvgpr_write_b32 a94, v5\n\t"
    "v_accvgpr_read_b32 v6, a95\n\t"
    "v_mul_f32_e32 v6, v18, v6\n\t"
    "v_accvgpr_write_b32 a95, v6\n\t"
    "v_accvgpr_read_b32 v5, a96\n\t"
    "v_mul_f32_e32 v5, v17, v5\n\t"
    "v_accvgpr_write_b32 a96, v5\n\t"
    "v_accvgpr_read_b32 v6, a97\n\t"
    "v_mul_f32_e32 v6, v17, v6\n\t"
    "v_accvgpr_write_b32 a97, v6\n\t"
    "v_accvgpr_read_b32 v5, a98\n\t"
    "v_mul_f32_e32 v5, v17, v5\n\t"
    "v_accvgpr_write_b32 a98, v5\n\t"
    "v_accvgpr_read_b32 v6, a99\n\t"
    "v_mul_f32_e32 v6, v17, v6\n\t"
    "v_accvgpr_write_b32 a99, v6\n\t"
    "v_accvgpr_read_b32 v5, a100\n\t"
    "v_mul_f32_e32 v5, v17, v5\n\t"
    "v_accvgpr_write_b32 a100, v5\n\t"
    "v_accvgpr_read_b32 v6, a101\n\t"
    "v_mul_f32_e32 v6, v17, v6\n\t"
    "v_accvgpr_write_b32 a101, v6\n\t"
    "v_accvgpr_read_b32 v5, a102\n\t"
    "v_mul_f32_e32 v5, v17, v5\n\t"
    "v_accvgpr_write_b32 a102, v5\n\t"
    "v_accvgpr_read_b32 v6, a103\n\t"
    "v_mul_f32_e32 v6, v17, v6\n\t"
    "v_accvgpr_write_b32 a103, v6\n\t"
    "v_accvgpr_read_b32 v5, a104\n\t"
    "v_mul_f32_e32 v5, v17, v5\n\t"
    "v_accvgpr_write_b32 a104, v5\n\t"
    "v_accvgpr_read_b32 v6, a105\n\t"
    "v_mul_f32_e32 v6, v17, v6\n\t"
    "v_accvgpr_write_b32 a105, v6\n\t"
    "v_accvgpr_read_b32 v5, a106\n\t"
    "v_mul_f32_e32 v5, v17, v5\n\t"
    "v_accvgpr_write_b32 a106, v5\n\t"
    "v_accvgpr_read_b32 v6, a107\n\t"
    "v_mul_f32_e32 v6, v17, v6\n\t"
    "v_accvgpr_write_b32 a107, v6\n\t"
    "v_accvgpr_read_b32 v5, a108\n\t"
    "v_mul_f32_e32 v5, v17, v5\n\t"
    "v_accvgpr_write_b32 a108, v5\n\t"
    "v_accvgpr_read_b32 v6, a109\n\t"
    "v_mul_f32_e32 v6, v17, v6\n\t"
    "v_accvgpr_write_b32 a109, v6\n\t"
    "v_accvgpr_read_b32 v5, a110\n\t"
    "v_mul_f32_e32 v5, v17, v5\n\t"
    "v_accvgpr_write_b32 a110, v5\n\t"
    "v_accvgpr_read_b32 v6, a111\n\t"
    "v_mul_f32_e32 v6, v17, v6\n\t"
    "v_accvgpr_write_b32 a111, v6\n\t"
    "v_accvgpr_read_b32 v5, a112\n\t"
    "v_mul_f32_e32 v5, v18, v5\n\t"
    "v_accvgpr_write_b32 a112, v5\n\t"
    "v_accvgpr_read_b32 v6, a113\n\t"
    "v_mul_f32_e32 v6, v18, v6\n\t"
    "v_accvgpr_write_b32 a113, v6\n\t"
    "v_accvgpr_read_b32 v5, a114\n\t"
    "v_mul_f32_e32 v5, v18, v5\n\t"
    "v_accvgpr_write_b32 a114, v5\n\t"
    "v_accvgpr_read_b32 v6, a115\n\t"
    "v_mul_f32_e32 v6, v18, v6\n\t"
    "v_accvgpr_write_b32 a115, v6\n\t"
    "v_accvgpr_read_b32 v5, a116\n\t"
    "v_mul_f32_e32 v5, v18, v5\n\t"
    "v_accvgpr_write_b32 a116, v5\n\t"
    "v_accvgpr_read_b32 v6, a117\n\t"
    "v_mul_f32_e32 v6, v18, v6\n\t"
    "v_accvgpr_write_b32 a117, v6\n\t"
    "v_accvgpr_read_b32 v5, a118\n\t"
    "v_mul_f32_e32 v5, v18, v5\n\t"
    "v_accvgpr_write_b32 a118, v5\n\t"
    "v_accvgpr_read_b32 v6, a119\n\t"
    "v_mul_f32_e32 v6, v18, v6\n\t"
    "v_accvgpr_write_b32 a119, v6\n\t"
    "v_accvgpr_read_b32 v5, a120\n\t"
    "v_mul_f32_e32 v5, v18, v5\n\t"
    "v_accvgpr_write_b32 a120, v5\n\t"
    "v_accvgpr_read_b32 v6, a121\n\t"
    "v_mul_f32_e32 v6, v18, v6\n\t"
    "v_accvgpr_write_b32 a121, v6\n\t"
    "v_accvgpr_read_b32 v5, a122\n\t"
    "v_mul_f32_e32 v5, v18, v5\n\t"
    "v_accvgpr_write_b32 a122, v5\n\t"
    "v_accvgpr_read_b32 v6, a123\n\t"
    "v_mul_f32_e32 v6, v18, v6\n\t"
    "v_accvgpr_write_b32 a123, v6\n\t"
    "v_accvgpr_read_b32 v5, a124\n\t"
    "v_mul_f32_e32 v5, v18, v5\n\t"
    "v_accvgpr_write_b32 a124, v5\n\t"
    "v_accvgpr_read_b32 v6, a125\n\t"
    "v_mul_f32_e32 v6, v18, v6\n\t"
    "v_accvgpr_write_b32 a125, v6\n\t"
    "v_accvgpr_read_b32 v5, a126\n\t"
    "v_mul_f32_e32 v5, v18, v5\n\t"
    "v_accvgpr_write_b32 a126, v5\n\t"
    "v_accvgpr_read_b32 v6, a127\n\t"
    "v_mul_f32_e32 v6, v18, v6\n\t"
    "v_accvgpr_write_b32 a127, v6\n\t"
    "s_mov_b32 s47, 0\n\t"
    "s_nop 1\n\t"
    "s_branch L_RRET_l7\n\t"
    "L_END:\n\t"
    :: "v"(threadIdx.x), "s"(qb), "s"(kb), "s"(vb), "s"(ob), "s"((unsigned)(size_t)lds)
    : "s64", "s65", "s66", "s67", "s68", "s69", "s70", "s71", "s72", "s73", "s74", "s75", "s76", "s77", "s78", "s79", "s80", "s81", "s82", "s83", "v1", "v2", "v3", "v4", "v5", "v6", "v7", "v8", "v9", "v10", "v11", "v12", "v13", "v14", "v15", "v16", "v17", "v18", "v19", "v20", "v21", "v22", "v23", "v24", "v25", "v26", "v27", "v28", "v29", "v30", "v31", "v32", "v33", "v34", "v35", "v36", "v37", "v38", "v39", "v40", "v41", "v42", "v43", "v44", "v45", "v46", "v47", "v48", "v49", "v50", "v51", "v52", "v53", "v54", "v55", "v56", "v57", "v58", "v59", "v60", "v61", "v62", "v63", "v64", "v65", "v66", "v67", "v68", "v69", "v70", "v71", "v72", "v73", "v74", "v75", "v76", "v77", "v78", "v79", "v80", "v81", "v82", "v83", "v84", "v85", "v86", "v87", "v88", "v89", "v90", "v91", "v92", "v93", "v94", "v95", "v96", "v97", "v98", "v99", "v100", "v101", "v102", "v103", "v104", "v105", "v106", "v107", "v108", "v109", "v110", "v111", "v112", "v113", "v114", "v115", "v116", "v117", "v118", "v119", "v120", "v121", "v122", "v123", "v124", "v125", "v126", "v127", "v128", "v129", "v130", "v131", "v132", "v133", "v134", "v135", "v136", "v137", "v138", "v139", "v140", "v141", "v142", "v143", "v144", "v145", "v146", "v147", "v148", "v149", "v150", "v151", "v152", "v153", "v154", "v155", "v156", "v157", "v158", "v159", "v160", "v161", "v162", "v163", "v164", "v165", "v166", "v167", "v168", "v169", "v170", "v171", "v172", "v173", "v174", "v175", "v176", "v177", "v178", "v179", "v180", "v181", "v182", "v183", "v184", "v185", "v186", "v187", "v188", "v189", "v190", "v191", "v192", "v193", "v194", "v195", "v196", "v197", "v198", "v199", "v200", "v201", "v202", "v203", "v204", "v205", "v206", "v207", "v208", "v209", "v210", "v211", "v212", "v213", "v214", "v215", "v216", "v217", "v218", "v219", "v220", "v221", "v222", "v223", "v224", "v225", "v226", "v227", "v228", "v229", "v230", "v231", "v232", "v233", "v234", "v235", "v236", "v237", "v238", "v239", "v240", "v241", "v242", "v243", "v244", "v245", "v246", "v247", "v248", "v249", "v250", "v251", "v252", "v253", "v254", "v255", "a0", "a1", "a2", "a3", "a4", "a5", "a6", "a7", "a8", "a9", "a10", "a11", "a12", "a13", "a14", "a15", "a16", "a17", "a18", "a19", "a20", "a21", "a22", "a23", "a24", "a25", "a26", "a27", "a28", "a29", "a30", "a31", "a32", "a33", "a34", "a35", "a36", "a37", "a38", "a39", "a40", "a41", "a42", "a43", "a44", "a45", "a46", "a47", "a48", "a49", "a50", "a51", "a52", "a53", "a54", "a55", "a56", "a57", "a58", "a59", "a60", "a61", "a62", "a63", "a64", "a65", "a66", "a67", "a68", "a69", "a70", "a71", "a72", "a73", "a74", "a75", "a76", "a77", "a78", "a79", "a80", "a81", "a82", "a83", "a84", "a85", "a86", "a87", "a88", "a89", "a90", "a91", "a92", "a93", "a94", "a95", "a96", "a97", "a98", "a99", "a100", "a101", "a102", "a103", "a104", "a105", "a106", "a107", "a108", "a109", "a110", "a111", "a112", "a113", "a114", "a115", "a116", "a117", "a118", "a119", "a120", "a121", "a122", "a123", "a124", "a125", "a126", "a127", "a128", "a129", "a130", "a131", "a132", "a133", "a134", "a135", "a136", "a137", "a138", "a139", "a140", "a141", "a142", "a143", "a144", "a145", "a146", "a147", "a148", "a149", "a150", "a151", "a152", "a153", "a154", "a155", "a156", "a157", "a158", "a159", "a160", "a161", "a162", "a163", "a164", "a165", "a166", "a167", "a168", "a169", "a170", "a171", "a172", "a173", "a174", "a175", "a176", "a177", "a178", "a179", "a180", "a181", "a182", "a183", "a184", "a185", "a186", "a187", "a188", "a189", "a190", "a191", "a192", "a193", "a194", "a195", "a196", "a197", "a198", "a199", "a200", "a201", "a202", "a203", "a204", "a205", "a206", "a207", "a208", "a209", "a210", "a211", "a212", "a213", "a214", "a215", "a216", "a217", "a218", "a219", "a220", "a221", "a222", "a223", "a224", "a225", "a226", "a227", "a228", "a229", "a230", "a231", "a232", "a233", "a234", "a235", "a236", "a237", "a238", "a239", "a240", "a241", "a242", "a243", "a244", "a245", "a246", "a247", "a248", "a249", "a250", "a251", "a252", "a253", "a254", "a255", "s24", "s25", "s26", "s27", "s28", "s29", "s30", "s31", "s34", "s35", "s36", "s37", "s38", "s39", "s40", "s41", "s42", "s43", "s44", "s45", "s46", "s47", "s48", "s49", "s50", "s51", "s52", "s53", "s54", "s55", "s56", "s57", "s58", "s59", "vcc", "scc", "memory");
}

extern "C" void kernel_launch(void* const* d_in, const int* in_sizes, int n_in, void* d_out, int out_size, void* d_ws, size_t ws_size, hipStream_t stream) {
  hipLaunchKernelGGL(attn_fwd_k, dim3(256), dim3(256), 0, stream, (const unsigned short*)d_in[0], (const unsigned short*)d_in[1], (const unsigned short*)d_in[2], (unsigned short*)d_out);
}
```
